```python
import torch

def _softmax(x, axis=-1):
    return torch.softmax(x, dim=axis)

def _gelu(x):
    return torch.nn.functional.gelu(x, approximate='tanh')

def _silu(x):
    return torch.nn.functional.silu(x)

def _sigmoid(x):
    return torch.sigmoid(x)

def _layernorm(x, axis=-1, eps=1e-05):
    m = x.mean(dim=axis, keepdim=True)
    v = x.var(dim=axis, keepdim=True, unbiased=False)
    return (x - m) / torch.sqrt(v + eps)

def _scatter_add(tgt, idx, src, axis=0):
    return tgt.clone().index_add_(axis, idx, src)

def _segment_sum(src, seg, n):
    out = torch.zeros((n,) + src.shape[1:], dtype=src.dtype)
    return out.index_add_(0, seg, src)

def _pad(x, pw):
    flat = tuple((v for pair in reversed(pw) for v in pair))
    return torch.nn.functional.pad(x, flat)
kernel_name = 'adaLN_cond__gqa_block_cached__mixer_channel_mlp__prompt'

def setup_inputs():

    _w = lambda a: (lambda t: t.to(torch.bfloat16) if t.is_floating_point() else t)(torch.as_tensor(a))

    def _A_setup():
        B, Sq, D = (1, 2048, 448)
        Hq, Hkv, Sk, Dh = (16, 8, 2048, 256)
        assert Hq % Hkv == 0, (Hq, Hkv)
        return {'x': _w((lambda *s: torch.randn(*s, dtype=torch.bfloat16) * 0.1)(B, Sq, D)), 'k_cache': _w((lambda *s: torch.randn(*s, dtype=torch.bfloat16) * 0.8)(B, Hkv, Sk, Dh)), 'v_cache': _w((lambda *s: torch.randn(*s, dtype=torch.bfloat16) * 1.6)(B, Hkv, Sk, Dh)), 'wq': _w((lambda *s: torch.randn(*s, dtype=torch.bfloat16) * 0.8)(D, Hq * Dh) * D ** (-0.5)), 'wo': _w((lambda *s: torch.randn(*s, dtype=torch.bfloat16) * 1.6)(Hq * Dh, D) * (Hq * Dh) ** (-0.5))}
    _A_in = _A_setup()

    def _B_setup():
        B, S, D, F = (1, 2048, 448, 1792)
        return {'x': _w((lambda *s: torch.randn(*s, dtype=torch.bfloat16) * 0.1)(B, S, D)), 'w1': _w((lambda *s: torch.randn(*s, dtype=torch.bfloat16) * 3.2)(D, F) * D ** (-0.5)), 'b1': _w((lambda *s: torch.randn(*s, dtype=torch.bfloat16) * 3.2)(F)), 'w2': _w((lambda *s: torch.randn(*s, dtype=torch.bfloat16) * 3.2)(F, D) * F ** (-0.5)), 'b2': _w((lambda *s: torch.randn(*s, dtype=torch.bfloat16) * 3.2)(D))}
    _B_in = _B_setup()
    return {'A_x': _A_in['x'], 'A_k_cache': _A_in['k_cache'], 'A_v_cache': _A_in['v_cache'], 'A_wq': _A_in['wq'], 'A_wo': _A_in['wo'], 'B_x': _B_in['x'], 'B_w1': _B_in['w1'], 'B_b1': _B_in['b1'], 'B_w2': _B_in['w2'], 'B_b2': _B_in['b2'], 'c': _w((lambda *s: torch.randn(*s, dtype=torch.bfloat16) * 0.2)(1, 448) * 8.0), 'w_mod': _w((lambda *s: torch.randn(*s, dtype=torch.bfloat16) * 0.2)(448, 6 * 448) * 448 ** (-0.5) * 8.0)}

def reference(A_x, A_k_cache, A_v_cache, A_wq, A_wo, B_x, B_w1, B_b1, B_w2, B_b2, c, w_mod):

    def _A_ref(x, k_cache, v_cache, wq, wo):
        B, Sq, D = x.shape
        Hq, Dh = (16, 256)
        Hkv = k_cache.shape[1]
        rep = Hq // Hkv
        q = (lambda x, *d: x.permute(*d))((x @ wq).reshape(B, Sq, Hq, Dh), 0, 2, 1, 3)
        k = (lambda x, n, axis: torch.repeat_interleave(x, n, dim=axis))(k_cache, rep, axis=1)
        v = (lambda x, n, axis: torch.repeat_interleave(x, n, dim=axis))(v_cache, rep, axis=1)
        scores = torch.einsum('bhqd,bhkd->bhqk', q, k) * Dh ** (-0.5)
        p = _softmax(scores, axis=-1)
        o = torch.einsum('bhqk,bhkd->bhqd', p, v)
        return (lambda x, *d: x.permute(*d))(o, 0, 2, 1, 3).reshape(B, Sq, Hq * Dh) @ wo

    def _B_ref(x, w1, b1, w2, b2):
        y = _layernorm(x, axis=-1)
        h = _gelu(torch.einsum('bsd,df->bsf', y, w1) + b1)
        return torch.einsum('bsf,fd->bsd', h, w2) + b2
    x = A_x
    mod = (_silu(c) @ w_mod).reshape(c.shape[0], 6, 1, -1)
    sa, ca, ga, sb, cb, gb = (mod[:, 0].clone(), mod[:, 1].clone(), mod[:, 2].clone(), mod[:, 3].clone(), mod[:, 4].clone(), mod[:, 5].clone())
    h = _layernorm(x, axis=-1) * (1 + ca) + sa
    x = x + ga * _A_ref(h, A_k_cache, A_v_cache, A_wq, A_wo)
    h = _layernorm(x, axis=-1) * (1 + cb) + sb
    x = x + gb * _B_ref(h, B_w1, B_b1, B_w2, B_b2)
    return x
```

```cpp
#include <hip/hip_runtime.h>
#include <hip/hip_bf16.h>

typedef unsigned short u16;
typedef unsigned u32x2 __attribute__((ext_vector_type(2)));
typedef unsigned u32x4 __attribute__((ext_vector_type(4)));
typedef unsigned u32x8 __attribute__((ext_vector_type(8)));
typedef unsigned u32x16 __attribute__((ext_vector_type(16)));
typedef float f32x2 __attribute__((ext_vector_type(2)));
typedef float f32x4 __attribute__((ext_vector_type(4)));
typedef float f32x16 __attribute__((ext_vector_type(16)));
typedef __bf16 bf16x2 __attribute__((ext_vector_type(2)));
typedef __bf16 bf16x8 __attribute__((ext_vector_type(8)));
#define DI __device__ __forceinline__

constexpr int SQ = 2048, DM = 448, FF = 1792;
constexpr size_t FBYTES = (size_t)SQ * DM * 2;
constexpr size_t WS_MOD = 0;
constexpr size_t WS_MODP = 16384;
constexpr size_t WS_FLAG = 20480;
constexpr size_t WS_XN = 32768;
constexpr size_t WS_Y = WS_XN + FBYTES;
constexpr size_t WS_X1 = WS_Y + FBYTES;
constexpr size_t WS_PART = WS_X1 + FBYTES;
constexpr size_t WS_PART2 = WS_PART + 8 * FBYTES;
constexpr size_t WS_DBG = WS_PART2 + 8 * FBYTES;
constexpr float QSCALE = 0.09016844005556021f;

DI float bf2f(u16 v) { return __uint_as_float((unsigned)v << 16); }
DI unsigned cvtpk(float lo, float hi) { f32x2 v = {lo, hi}; bf16x2 b = __builtin_convertvector(v, bf16x2); return __builtin_bit_cast(unsigned, b); }
DI float rbf(float x) { return __uint_as_float(cvtpk(x, 0.f) << 16); }
DI unsigned enc2(float a, float b) {
  const unsigned lo = cvtpk(a, 0.f), ub = __float_as_uint(b);
  return (__builtin_elementwise_sub_sat((ub & 0x7fffffffu) + 0x8000u, lo) & 0x7fff0000u) | (ub & 0x80000000u) | lo;
}
DI float lo16(unsigned v) { return __uint_as_float(v << 16); }
DI float hi16(unsigned v) { return __uint_as_float(v & 0xffff0000u); }

template <int C> DI float dpp(float v) { return __int_as_float(__builtin_amdgcn_update_dpp(0, __float_as_int(v), C, 0xf, 0xf, true)); }
DI float sum16(float v) { v += dpp<0xb1>(v); v += dpp<0x4e>(v); v += dpp<0x141>(v); v += dpp<0x140>(v); return v; }
DI float xor16(float v) { const auto r = __builtin_amdgcn_permlane16_swap(__float_as_uint(v), __float_as_uint(v), false, false); return __uint_as_float(r[0]) + __uint_as_float(r[1]); }
DI float xor32(float v) { const auto r = __builtin_amdgcn_permlane32_swap(__float_as_uint(v), __float_as_uint(v), false, false); return __uint_as_float(r[0]) + __uint_as_float(r[1]); }
DI float sumq8(float v) { v += dpp<0x128>(v); return xor32(xor16(v)); }

DI __amdgpu_buffer_rsrc_t mkrsrc(const void* p, unsigned bytes) {
  const unsigned long long a = (unsigned long long)p;
  const unsigned lo = __builtin_amdgcn_readfirstlane((unsigned)a), hi = __builtin_amdgcn_readfirstlane((unsigned)(a >> 32));
  return __builtin_amdgcn_make_buffer_rsrc((void*)(((unsigned long long)hi << 32) | lo), (short)0, (int)bytes, 0x00020000);
}
constexpr int AUX0 = 0, AUXO = 0;
DI unsigned warm_line(const char* wq, const char* kc, const char* vc, int g, int li) {
  const char* p = li < 3584 ? wq + (size_t)(li >> 3) * 8192 + g * 1024 + (li & 7) * 128 : (li < 4352 ? kc + (li - 3584) * 128 : vc + (li - 4352) * 128) + (size_t)g * 1048576;
  unsigned d;
  asm volatile("global_load_dword %0, %1, off" : "=v"(d) : "v"(p));
  return d;
}
DI unsigned warm_code(unsigned off) {
  unsigned d; unsigned long long pc;
  asm volatile("s_getpc_b64 %1\nLC0_%=:\n\tv_min_u32 %0, KTAIL_END-LC0_%=-8, %2\n\tglobal_load_dword %0, %0, %1" : "=v"(d), "=&s"(pc) : "v"(off));
  return d;
}
DI void warm_done(unsigned d) { asm volatile("s_waitcnt vmcnt(0)" ::"v"(d)); }
__global__ __launch_bounds__(256) void k0_prep(const u16* __restrict__ x, const u16* __restrict__ c, const u16* __restrict__ wmod, const char* __restrict__ wq,
                                               const char* __restrict__ kc, const char* __restrict__ vc, char* __restrict__ ws) {
  __shared__ __attribute__((aligned(16))) char sm[512];
  const int tid = threadIdx.x, b = blockIdx.x;
  const unsigned cw = warm_code(b >= 212 ? 8192 + ((b - 212) >> 3) * 32768 + tid * 128 : tid < 64 ? tid * 128 : 0);
  if (b < 84) {
    const int ct = tid & 3, rg = tid >> 2;
    u32x4 w[7]; float sv[7];
#pragma unroll
    for (int i = 0; i < 7; ++i) w[i] = *(const u32x4*)(wmod + (size_t)(rg + 64 * i) * 2688 + b * 32 + ct * 8);
#pragma unroll
    for (int i = 0; i < 7; ++i) { const float cv = bf2f(c[rg + 64 * i]); sv[i] = rbf(cv * __builtin_amdgcn_rcpf(1.f + __expf(-cv))); }
    float acc[8] = {0, 0, 0, 0, 0, 0, 0, 0};
#pragma unroll
    for (int i = 0; i < 7; ++i)
#pragma unroll
      for (int e = 0; e < 4; ++e) { acc[2 * e] += sv[i] * lo16(w[i][e]); acc[2 * e + 1] += sv[i] * hi16(w[i][e]); }
#pragma unroll
    for (int e = 0; e < 8; ++e) {
      acc[e] += dpp<0x124>(acc[e]); acc[e] = sumq8(acc[e]);
    }
    float* red = (float*)sm;
    if ((tid & 63) < 4) {
#pragma unroll
      for (int e = 0; e < 8; ++e) red[(tid >> 6) * 32 + ct * 8 + e] = acc[e];
    }
    __syncthreads();
    if (tid < 32) {
      const float v = rbf(red[tid] + red[32 + tid] + red[64 + tid] + red[96 + tid]);
      const int col = b * 32 + tid;
      ((float*)(ws + WS_MOD))[col] = (col >= 4 * DM && col < 5 * DM) ? rbf(1.f + v) : v;
      if (col < 896) {
        const int isA = col >= 448, cc = isA ? col - 448 : col, s = cc >> 4, r = cc & 15, h = (r >> 2) & 1, j = ((r >> 3) << 2) | (r & 3);
        ((float*)(ws + WS_MODP))[((s * 2 + h) * 4 + (j >> 1)) * 4 + (isA ? 0 : 2) + (j & 1)] = (isA ? rbf(1.f + v) : v) * QSCALE;
      }
    }
  } else if (b < 212) {
    const int j = tid & 15, row = (b - 84) * 16 + (tid >> 4);
    const unsigned wv = warm_line(wq, kc, vc, b & 7, ((b - 84) >> 3) * 256 + tid);
    u32x2 v[7];
#pragma unroll
    for (int i = 0; i < 7; ++i) v[i] = *(const u32x2*)(x + (size_t)row * DM + (j + 16 * i) * 4);
    float f[28], s = 0;
#pragma unroll
    for (int i = 0; i < 7; ++i) { f[4 * i] = lo16(v[i][0]); f[4 * i + 1] = hi16(v[i][0]); f[4 * i + 2] = lo16(v[i][1]); f[4 * i + 3] = hi16(v[i][1]); }
    float q = 0;
#pragma unroll
    for (int e = 0; e < 28; ++e) { s += f[e]; q += f[e] * f[e]; }
    const float mean = sum16(s) * (1.f / DM), var = fmaxf(sum16(q) * (1.f / DM) - mean * mean, 0.f);
    const float rs = __builtin_amdgcn_rcpf(rbf(__builtin_amdgcn_sqrtf(rbf(rbf(var) + 1e-5f)))), mb = -rbf(mean) * rs;
    const __amdgpu_buffer_rsrc_t rn = mkrsrc(ws + WS_XN, (unsigned)FBYTES);
    const unsigned dst = (row >> 5) * 28672 + (j >> 2) * 1024 + ((row & 31) + 32 * (j & 1)) * 16 + ((j >> 1) & 1) * 8;
#pragma unroll
    for (int i = 0; i < 7; ++i)
      __builtin_amdgcn_raw_buffer_store_b64(u32x2{enc2(f[4 * i] * rs + mb, f[4 * i + 1] * rs + mb), enc2(f[4 * i + 2] * rs + mb, f[4 * i + 3] * rs + mb)}, rn, dst, i * 4096, AUX0);
    warm_done(wv);
  } else {
    if (b == 212 && tid < 192) *(u32x4*)(ws + WS_FLAG + (tid >> 1) * 128 + (tid & 1) * 16) = u32x4{0, 0, 0, 0};
    warm_done(warm_line(wq, kc, vc, b & 7, 4096 + ((b - 212) >> 3) * 256 + tid));
  }
  warm_done(cw);
}

typedef __attribute__((address_space(1))) unsigned gu32;
DI f32x2 up2(unsigned v) { return f32x2{lo16(v), hi16(v)}; }
DI u32x4 pack8(const f32x2* f) { return u32x4{cvtpk(f[0][0], f[0][1]), cvtpk(f[1][0], f[1][1]), cvtpk(f[2][0], f[2][1]), cvtpk(f[3][0], f[3][1])}; }
DI f32x2 ln_stats(const f32x2 (&f)[8], f32x2* red, unsigned l, unsigned w) {
  const f32x2 s2 = ((f[0] + f[1]) + (f[2] + f[3])) + ((f[4] + f[5]) + (f[6] + f[7]));
  const f32x2 q2 = (f[0] * f[0] + f[1] * f[1] + f[2] * f[2] + f[3] * f[3]) + (f[4] * f[4] + f[5] * f[5] + f[6] * f[6] + f[7] * f[7]);
  const float s = sumq8(s2[0] + s2[1]), q = sumq8(q2[0] + q2[1]);
  if (l < 8u) red[l * 4u + w] = f32x2{s, q};
  asm volatile("s_waitcnt lgkmcnt(0)\n\ts_barrier" ::: "memory");
  const f32x4 r0 = *(const f32x4*)(red + (l & 7u) * 4u), r1 = *(const f32x4*)(red + (l & 7u) * 4u + 2u);
  const f32x2 t = ((r0.xy + r0.zw) + (r1.xy + r1.zw)) * (1.f / DM);
  const float var = fmaxf(t[1] - t[0] * t[0], 0.f);
  const float rs = __builtin_amdgcn_rcpf(rbf(__builtin_amdgcn_sqrtf(rbf(rbf(var) + 1e-5f))));
  return f32x2{rs, -rbf(t[0]) * rs};
}
DI unsigned w1fill(unsigned j, unsigned l) {
  const unsigned p = 64u * j + l, r = p / 28u, c = (p - r * 28u) ^ (((r >> 2) & 1u) << 1);
  return r * 3584u + c * 16u - (j & 3u) * 1024u;
}
__global__ __launch_bounds__(256, 1) void k1_main(const char* __restrict__ kc, const char* __restrict__ vc, const char* __restrict__ wq,
                                                  const char* __restrict__ wo, const char* __restrict__ w1, const char* __restrict__ w2, const u16* __restrict__ x,
                                                  const char* __restrict__ b1, const u16* __restrict__ b2, char* __restrict__ ws, u16* __restrict__ out) {
  __shared__ __attribute__((aligned(16))) char lds[163840];
  const unsigned tid = threadIdx.x, l = tid & 63u, w = __builtin_amdgcn_readfirstlane(tid >> 6);
  const unsigned g = blockIdx.x & 7u, rb = blockIdx.x >> 3, r2 = w & 1u, rb32 = 2u * rb + r2, hs = w >> 1;
  const unsigned lb = (unsigned)(size_t)lds;
  const unsigned l31 = l & 31u, h = l >> 5, n = l & 15u, kq = l >> 4, loff = (kq >> 1) * 1024u + (kq & 1u) * 512u + n * 16u;
  const unsigned r7 = 4u * (kq & 1u) + (n >> 2), r8 = l >> 3;
  u32x4 wd;
#pragma unroll
  for (unsigned j = 0; j < 4; ++j) { const unsigned rr = 2u * j + h; wd[j] = rr * 8192u + ((l31 ^ ((rr & 1u) | ((rr & 6u) << 1))) << 4) - j * 1024u; }
  const __amdgpu_buffer_rsrc_t srdwq = mkrsrc(wq + g * 1024u, 448u * 8192u - g * 1024u), srdxn = mkrsrc(ws + WS_XN, (unsigned)FBYTES), srdmp = mkrsrc(ws + WS_MODP, 3584u);
  const unsigned xoff = (2u * rb + (w >> 1)) * 28672u + (w & 1u) * 1024u, wobase = (256u * hs + 64u * (w & 1u)) * 896u;
  asm volatile(
    "s_nop 4\n\t"
    "s_mov_b32 s60, %[wid]\n\t"
    "s_lshl_b32 s68, s60, 10\n\t"
    "s_add_u32 s75, s68, %[ldsb]\n\t"
    "s_add_u32 s75, s75, 0x20000\n\t"
    "s_mov_b32 m0, s75\n\t"
    "s_mov_b32 s73, %[xoff]\n\t"
    "s_lshl_b32 s69, s60, 12\n\t"
    "s_add_u32 s69, s69, %[ldsb]\n\t"
    "s_lshl_b32 s67, s60, 16\n\t"
    "buffer_load_dwordx4 v52, %[srdmp], s68 offen lds\n\t"
    "s_add_u32 m0, s75, 0x1000\n\t"
    "s_add_u32 s68, s73, 0x0\n\t"
    "buffer_load_dwordx4 v52, %[srdxn], s68 offen lds\n\t"
    "s_add_u32 m0, s75, 0x2000\n\t"
    "s_add_u32 s68, s73, 0x800\n\t"
    "buffer_load_dwordx4 v52, %[srdxn], s68 offen lds\n\t"
    "s_add_u32 m0, s69, 0x8000\n\t"
    "s_add_u32 s68, s67, 0x0\n\t"
    "buffer_load_dwordx4 v40, %[srdwq], s68 offen lds\n\t"
    "buffer_load_dwordx4 v41, %[srdwq], s68 offen offset:1024 lds\n\t"
    "buffer_load_dwordx4 v42, %[srdwq], s68 offen offset:2048 lds\n\t"
    "buffer_load_dwordx4 v43, %[srdwq], s68 offen offset:3072 lds\n\t"
    "s_add_u32 m0, s69, 0x18000\n\t"
    "s_add_u32 s68, s67, 0x200\n\t"
    "buffer_load_dwordx4 v40, %[srdwq], s68 offen lds\n\t"
    "buffer_load_dwordx4 v41, %[srdwq], s68 offen offset:1024 lds\n\t"
    "buffer_load_dwordx4 v42, %[srdwq], s68 offen offset:2048 lds\n\t"
    "buffer_load_dwordx4 v43, %[srdwq], s68 offen offset:3072 lds\n\t"
    "s_add_u32 m0, s75, 0x3000\n\t"
    "s_add_u32 s68, s73, 0x1000\n\t"
    "buffer_load_dwordx4 v52, %[srdxn], s68 offen lds\n\t"
    "s_add_u32 m0, s69, 0xc000\n\t"
    "s_add_u32 s68, s67, 0x40000\n\t"
    "buffer_load_dwordx4 v40, %[srdwq], s68 offen lds\n\t"
    "buffer_load_dwordx4 v41, %[srdwq], s68 offen offset:1024 lds\n\t"
    "buffer_load_dwordx4 v42, %[srdwq], s68 offen offset:2048 lds\n\t"
    "buffer_load_dwordx4 v43, %[srdwq], s68 offen offset:3072 lds\n\t"
    "s_add_u32 m0, s69, 0x1c000\n\t"
    "s_add_u32 s68, s67, 0x40200\n\t"
    "buffer_load_dwordx4 v40, %[srdwq], s68 offen lds\n\t"
    "buffer_load_dwordx4 v41, %[srdwq], s68 offen offset:1024 lds\n\t"
    "buffer_load_dwordx4 v42, %[srdwq], s68 offen offset:2048 lds\n\t"
    "buffer_load_dwordx4 v43, %[srdwq], s68 offen offset:3072 lds\n\t"

    :
    : [srdwq] "s"(srdwq), [srdxn] "s"(srdxn), [srdmp] "s"(srdmp), [wid] "s"(w), [ldsb] "s"(lb), [xoff] "s"(xoff), "{v[40:43]}"(wd), "{v52}"(l * 16u)
    : "s60", "s61", "s62", "s63", "s64", "s65", "s66", "s67", "s68", "s69", "s70", "s71", "s72", "s73", "s74", "s75", "s98", "s99", "vcc", "scc", "memory"
  );
  __builtin_amdgcn_sched_barrier(0);
  u32x16 aA, aB, aC;
  u32x8 aD;
#pragma unroll
  for (unsigned j = 0; j < 8; ++j) {
    aA[j] = h * 512u + ((l31 ^ ((2u * j + h) & 15u)) << 4) + (j >> 2) * 4096u;
    aA[8 + j] = lb + 65536u + (4u * kq + (n >> 2)) * 512u + (((2u * (j ^ r7)) + ((n >> 1) & 1u)) << 4) + (n & 1u) * 8u;
    aB[j] = lb + hs * 65536u + (16u * (kq >> 1) + r7) * 512u +
            (((4u * (j >> 1) + (j & 1u) + 2u * ((n >> 1) & 1u)) ^ ((r7 & 1u) | ((r7 & 6u) << 1))) << 4) + (n & 1u) * 8u;
  }
#pragma unroll
  for (unsigned j = 0; j < 4; ++j) {
    const unsigned rr = 2u * j + h;
    aB[8 + j] = lb + n * 512u + (((4u * j + kq) ^ n) << 4);
    aB[12 + j] = h * 512u + ((l31 ^ (rr << 1)) << 4);
    aC[j] = wd[j];
    aC[4 + j] = lb + ((j >> 1) ^ hs) * 16384u + (4u * kq + (n >> 2)) * 128u +
                (((4u * hs + 2u * ((n >> 1) & 1u) + (j & 1u)) ^ (((r7 >> 1) & 1u) + 4u * (r7 >> 2))) << 4) + (n & 1u) * 8u;
    aC[8 + j] = (8u * j + r8) * 896u + (((l & 7u) ^ (((r8 >> 1) & 1u) + 4u * (r8 >> 2))) << 4) - j * 1024u;
  }
  aC[12] = l * 16u;
  aC[13] = loff;
  aC[14] = lb + 131072u + kq * 64u;
  aC[15] = lb + 131072u + 4096u + r2 * 2048u + loff;
  aD[0] = lb + 65536u + hs * 65536u + r2 * 16384u + l * 16u;
  aD[1] = lb + 131072u - hs * 65536u + r2 * 16384u + l * 16u;
  const bool kv = w < 2u, is2 = w == 2u, od = rb & 1u;
  unsigned long long p0 = (size_t)kc, p1 = (size_t)vc, p2 = (size_t)wo, p3 = (size_t)w2, p4 = (size_t)w1;
  asm volatile("" : "+s"(p0), "+s"(p1), "+s"(p2), "+s"(p3), "+s"(p4));
  p0 = w == 0u ? p0 : p1; p3 = od ? p3 : p4; p2 = is2 ? p2 : p3; p0 = kv ? p0 : p2;
  unsigned gm = od ? 200704u : 448u, ro = od ? 12544u : 100352u, tstr = od ? 512u : 3584u, rs = rb >> 1;
  asm volatile("" : "+s"(gm), "+s"(ro), "+s"(tstr), "+s"(rs), "+s"(p0));
  gm = is2 ? 458752u : gm; ro = is2 ? 14336u : ro; tstr = is2 ? 512u : tstr; rs = w < 3u ? rb : rs;
  asm volatile("" : "+s"(gm), "+s"(ro), "+s"(tstr), "+s"(rs));
  gm = kv ? 1048576u : gm; ro = kv ? 1024u : ro; tstr = kv ? 32768u : tstr;
  asm volatile("" : "+s"(gm), "+s"(ro), "+s"(tstr));
  const char* tb = (const char*)(p0 + g * gm);
  const unsigned tsz = gm == 448u ? 448u * 3584u - g * 448u : gm, kvm = kv ? 32768u : 0u, to = rs * ro + (l & (kv ? 7u : 3u)) * 128u;
  aD[2] = 5u * kvm + to;
  aD[3] = (2u + (r8 & 3u)) * kvm + to;
  const __amdgpu_buffer_rsrc_t srdt = mkrsrc(tb, tsz);
  aD[4] = (rb & 3u) * 32768u + tid * 128u;
  aD[5] = w1fill(4, l); aD[6] = w1fill(5, l); aD[7] = w1fill(6, l);
  const u32x4 aE = {w1fill(0, l), w1fill(1, l), w1fill(2, l), w1fill(3, l)};
  const __amdgpu_buffer_rsrc_t srdw1 = mkrsrc(w1 + g * 448u, 448u * 3584u - g * 448u);
  const __amdgpu_buffer_rsrc_t srdk = mkrsrc(kc + (size_t)g * 1048576u, 1048576u), srdv = mkrsrc(vc + (size_t)g * 1048576u, 1048576u);
  const __amdgpu_buffer_rsrc_t srdwo = mkrsrc(wo + (size_t)g * 458752u, 458752u);
  const __amdgpu_buffer_rsrc_t srdpt = mkrsrc(ws + WS_PART + g * FBYTES + rb32 * 28672u + hs * 2048u, 28672u - hs * 2048u);
  asm volatile(
    "s_nop 4\n\t"
    "LS1_%=:\n\t"
    "s_getpc_b64 s[98:99]\n\t"
    "s_mov_b32 s60, %[wid]\n\t"
    "s_lshl_b32 s68, s60, 10\n\t"
    "s_add_u32 s75, s68, %[ldsb]\n\t"
    "s_add_u32 s75, s75, 0x20000\n\t"
    "s_mov_b32 m0, s75\n\t"
    "s_mov_b32 s73, %[xoff]\n\t"
    "s_lshl_b32 s69, s60, 12\n\t"
    "s_add_u32 s69, s69, %[ldsb]\n\t"
    "s_lshl_b32 s67, s60, 16\n\t"
    "v_min_u32 v253, KTAIL_END-LS1_%=-8, v60\n\t"
    "global_load_dword v7, v253, s[98:99]\n\t"
    "v_mov_b32 v6, v58\n\t"
    "s_lshl_b32 s61, s60, 13\n\t"
    "s_mov_b32 s62, s61\n\t"
    "s_add_u32 s63, s61, %[ldsb]\n\t"
    "s_mov_b32 s72, %[wobase]\n\t"
    "s_mov_b32 s66, 0x41000000\n\t"
    "s_mov_b32 s65, 0\n\t"
    "v_mov_b32 v208, 0x3f803f80\n\t"
    "v_mov_b32 v209, 0x3f803f80\n\t"
    "v_mov_b32 v210, 0x3f803f80\n\t"
    "v_mov_b32 v211, 0x3f803f80\n\t"
    "s_waitcnt vmcnt(19)\n\t"
    "s_barrier\n\t"
    "ds_read_b128 v[220:223], v55\n\t"
    "ds_read_b128 v[224:227], v55 offset:256\n\t"
    "ds_read_b128 v[236:239], v54\n\t"
    "ds_read_b128 v[240:243], v54 offset:16\n\t"
    "ds_read_b128 v[244:247], v54 offset:32\n\t"
    "ds_read_b128 v[248:251], v54 offset:48\n\t"
    "s_add_u32 m0, s75, 0x4000\n\t"
    "s_add_u32 s68, s73, 0x1800\n\t"
    "buffer_load_dwordx4 v52, %[srdxn], s68 offen lds\n\t"
    "s_add_u32 m0, s69, 0x0\n\t"
    "s_add_u32 s68, s67, 0x80000\n\t"
    "buffer_load_dwordx4 v40, %[srdwq], s68 offen lds\n\t"
    "buffer_load_dwordx4 v41, %[srdwq], s68 offen offset:1024 lds\n\t"
    "buffer_load_dwordx4 v42, %[srdwq], s68 offen offset:2048 lds\n\t"
    "s_waitcnt lgkmcnt(0)\n\t"
    "v_lshlrev_b32 v252, 16, v220\n\t"
    "v_fma_f32 v253, v220, v237, v239\n\t"
    "v_fma_f32 v252, v252, v236, v238\n\t"
    "v_cvt_pk_bf16_f32 v220, v252, v253\n\t"
    "v_lshlrev_b32 v254, 16, v224\n\t"
    "v_fma_f32 v255, v224, v237, v239\n\t"
    "v_fma_f32 v254, v254, v236, v238\n\t"
    "v_cvt_pk_bf16_f32 v224, v254, v255\n\t"
    "v_lshlrev_b32 v252, 16, v221\n\t"
    "v_fma_f32 v253, v221, v241, v243\n\t"
    "v_fma_f32 v252, v252, v240, v242\n\t"
    "v_cvt_pk_bf16_f32 v221, v252, v253\n\t"
    "v_lshlrev_b32 v254, 16, v225\n\t"
    "v_fma_f32 v255, v225, v241, v243\n\t"
    "v_fma_f32 v254, v254, v240, v242\n\t"
    "v_cvt_pk_bf16_f32 v225, v254, v255\n\t"
    "v_lshlrev_b32 v252, 16, v222\n\t"
    "v_fma_f32 v253, v222, v245, v247\n\t"
    "v_fma_f32 v252, v252, v244, v246\n\t"
    "v_cvt_pk_bf16_f32 v222, v252, v253\n\t"
    "v_lshlrev_b32 v254, 16, v226\n\t"
    "v_fma_f32 v255, v226, v245, v247\n\t"
    "v_fma_f32 v254, v254, v244, v246\n\t"
    "v_cvt_pk_bf16_f32 v226, v254, v255\n\t"
    "v_lshlrev_b32 v252, 16, v223\n\t"
    "v_fma_f32 v253, v223, v249, v251\n\t"
    "v_fma_f32 v252, v252, v248, v250\n\t"
    "v_cvt_pk_bf16_f32 v223, v252, v253\n\t"
    "v_lshlrev_b32 v254, 16, v227\n\t"
    "v_fma_f32 v255, v227, v249, v251\n\t"
    "v_fma_f32 v254, v254, v248, v250\n\t"
    "v_cvt_pk_bf16_f32 v227, v254, v255\n\t"
    "s_waitcnt vmcnt(14)\n\t"
    "s_barrier\n\t"
    "ds_read_b64_tr_b16 a[192:193], v24 offset:32768\n\t"
    "ds_read_b64_tr_b16 a[194:195], v24 offset:36864\n\t"
    "ds_read_b64_tr_b16 a[196:197], v25 offset:32768\n\t"
    "ds_read_b64_tr_b16 a[198:199], v25 offset:36864\n\t"
    "ds_read_b64_tr_b16 a[200:201], v26 offset:32768\n\t"
    "ds_read_b64_tr_b16 a[202:203], v26 offset:36864\n\t"
    "ds_read_b64_tr_b16 a[204:205], v27 offset:32768\n\t"
    "ds_read_b64_tr_b16 a[206:207], v27 offset:36864\n\t"
    "ds_read_b64_tr_b16 a[208:209], v28 offset:32768\n\t"
    "ds_read_b64_tr_b16 a[210:211], v28 offset:36864\n\t"
    "ds_read_b64_tr_b16 a[212:213], v29 offset:32768\n\t"
    "ds_read_b64_tr_b16 a[214:215], v29 offset:36864\n\t"
    "s_waitcnt lgkmcnt(8)\n\t"
    "v_mfma_f32_16x16x32_bf16 v[64:67], a[192:195], v[220:223], 0\n\t"
    "ds_read_b64_tr_b16 a[216:217], v30 offset:32768\n\t"
    "ds_read_b64_tr_b16 a[218:219], v30 offset:36864\n\t"
    "v_mfma_f32_16x16x32_bf16 v[68:71], a[192:195], v[224:227], 0\n\t"
    "ds_read_b128 v[228:231], v55 offset:4096\n\t"
    "ds_read_b128 v[232:235], v55 offset:4352\n\t"
    "v_mfma_f32_16x16x32_bf16 v[128:131], a[196:199], v[220:223], 0\n\t"
    "ds_read_b64_tr_b16 a[220:221], v31 offset:32768\n\t"
    "ds_read_b64_tr_b16 a[222:223], v31 offset:36864\n\t"
    "ds_read_b128 v[236:239], v54 offset:256\n\t"
    "ds_read_b128 v[240:243], v54 offset:272\n\t"
    "v_mfma_f32_16x16x32_bf16 v[132:135], a[196:199], v[224:227], 0\n\t"
    "buffer_load_dwordx4 v43, %[srdwq], s68 offen offset:3072 lds\n\t"
    "s_add_u32 m0, s69, 0x10000\n\t"
    "ds_read_b128 v[244:247], v54 offset:288\n\t"
    "ds_read_b128 v[248:251], v54 offset:304\n\t"
    "s_waitcnt lgkmcnt(14)\n\t"
    "v_mfma_f32_16x16x32_bf16 v[72:75], a[200:203], v[220:223], 0\n\t"
    "ds_read_b64_tr_b16 a[224:225], v24 offset:33024\n\t"
    "ds_read_b64_tr_b16 a[226:227], v24 offset:37120\n\t"
    "s_add_u32 s68, s67, 0x80200\n\t"
    "v_mfma_f32_16x16x32_bf16 v[76:79], a[200:203], v[224:227], 0\n\t"
    "buffer_load_dwordx4 v40, %[srdwq], s68 offen lds\n\t"
    "v_mfma_f32_16x16x32_bf16 v[136:139], a[204:207], v[220:223], 0\n\t"
    "ds_read_b64_tr_b16 a[228:229], v25 offset:33024\n\t"
    "ds_read_b64_tr_b16 a[230:231], v25 offset:37120\n\t"
    "v_mfma_f32_16x16x32_bf16 v[140:143], a[204:207], v[224:227], 0\n\t"
    "buffer_load_dwordx4 v41, %[srdwq], s68 offen offset:1024 lds\n\t"
    "s_waitcnt lgkmcnt(14)\n\t"
    "v_mfma_f32_16x16x32_bf16 v[80:83], a[208:211], v[220:223], 0\n\t"
    "ds_read_b64_tr_b16 a[232:233], v26 offset:33024\n\t"
    "ds_read_b64_tr_b16 a[234:235], v26 offset:37120\n\t"
    "v_mfma_f32_16x16x32_bf16 v[84:87], a[208:211], v[224:227], 0\n\t"
    "s_waitcnt lgkmcnt(6)\n\t"
    "v_lshlrev_b32 v252, 16, v228\n\t"
    "v_fma_f32 v253, v228, v237, v239\n\t"
    "v_mfma_f32_16x16x32_bf16 v[144:147], a[212:215], v[220:223], 0\n\t"
    "ds_read_b64_tr_b16 a[236:237], v27 offset:33024\n\t"
    "ds_read_b64_tr_b16 a[238:239], v27 offset:37120\n\t"
    "v_fma_f32 v252, v252, v236, v238\n\t"
    "v_mfma_f32_16x16x32_bf16 v[148:151], a[212:215], v[224:227], 0\n\t"
    "buffer_load_dwordx4 v42, %[srdwq], s68 offen offset:2048 lds\n\t"
    "v_cvt_pk_bf16_f32 v228, v252, v253\n\t"
    "v_lshlrev_b32 v254, 16, v232\n\t"
    "v_mfma_f32_16x16x32_bf16 v[88:91], a[216:219], v[220:223], 0\n\t"
    "ds_read_b64_tr_b16 a[240:241], v28 offset:33024\n\t"
    "ds_read_b64_tr_b16 a[242:243], v28 offset:37120\n\t"
    "v_fma_f32 v255, v232, v237, v239\n\t"
    "v_mfma_f32_16x16x32_bf16 v[92:95], a[216:219], v[224:227], 0\n\t"
    "buffer_load_dwordx4 v43, %[srdwq], s68 offen offset:3072 lds\n\t"
    "v_fma_f32 v254, v254, v236, v238\n\t"
    "v_cvt_pk_bf16_f32 v232, v254, v255\n\t"
    "v_mfma_f32_16x16x32_bf16 v[152:155], a[220:223], v[220:223], 0\n\t"
    "ds_read_b64_tr_b16 a[244:245], v29 offset:33024\n\t"
    "ds_read_b64_tr_b16 a[246:247], v29 offset:37120\n\t"
    "v_lshlrev_b32 v252, 16, v229\n\t"
    "v_mfma_f32_16x16x32_bf16 v[156:159], a[220:223], v[224:227], 0\n\t"
    "v_fma_f32 v253, v229, v241, v243\n\t"
    "v_fma_f32 v252, v252, v240, v242\n\t"
    "s_waitcnt lgkmcnt(8)\n\t"
    "v_mfma_f32_16x16x32_bf16 v[96:99], a[224:227], v[220:223], 0\n\t"
    "ds_read_b64_tr_b16 a[248:249], v30 offset:33024\n\t"
    "ds_read_b64_tr_b16 a[250:251], v30 offset:37120\n\t"
    "v_cvt_pk_bf16_f32 v229, v252, v253\n\t"
    "v_mfma_f32_16x16x32_bf16 v[100:103], a[224:227], v[224:227], 0\n\t"
    "s_waitcnt vmcnt(10)\n\t"
    "s_barrier\n\t"
    "v_lshlrev_b32 v254, 16, v233\n\t"
    "v_fma_f32 v255, v233, v241, v243\n\t"
    "v_mfma_f32_16x16x32_bf16 v[160:163], a[228:231], v[220:223], 0\n\t"
    "ds_read_b64_tr_b16 a[252:253], v31 offset:33024\n\t"
    "ds_read_b64_tr_b16 a[254:255], v31 offset:37120\n\t"
    "v_fma_f32 v254, v254, v240, v242\n\t"
    "v_mfma_f32_16x16x32_bf16 v[164:167], a[228:231], v[224:227], 0\n\t"
    "s_add_u32 m0, s75, 0x5000\n\t"
    "s_add_u32 s68, s73, 0x2000\n\t"
    "v_cvt_pk_bf16_f32 v233, v254, v255\n\t"
    "s_waitcnt lgkmcnt(8)\n\t"
    "v_mfma_f32_16x16x32_bf16 v[104:107], a[232:235], v[220:223], 0\n\t"
    "ds_read_b64_tr_b16 a[192:193], v24 offset:49152\n\t"
    "ds_read_b64_tr_b16 a[194:195], v24 offset:53248\n\t"
    "v_lshlrev_b32 v252, 16, v230\n\t"
    "v_fma_f32 v253, v230, v245, v247\n\t"
    "v_mfma_f32_16x16x32_bf16 v[108:111], a[232:235], v[224:227], 0\n\t"
    "buffer_load_dwordx4 v52, %[srdxn], s68 offen lds\n\t"
    "s_add_u32 m0, s69, 0x4000\n\t"
    "s_add_u32 s68, s67, 0xc0000\n\t"
    "v_fma_f32 v252, v252, v244, v246\n\t"
    "v_mfma_f32_16x16x32_bf16 v[168:171], a[236:239], v[220:223], 0\n\t"
    "ds_read_b64_tr_b16 a[196:197], v25 offset:49152\n\t"
    "ds_read_b64_tr_b16 a[198:199], v25 offset:53248\n\t"
    "v_cvt_pk_bf16_f32 v230, v252, v253\n\t"
    "v_lshlrev_b32 v254, 16, v234\n\t"
    "v_mfma_f32_16x16x32_bf16 v[172:175], a[236:239], v[224:227], 0\n\t"
    "buffer_load_dwordx4 v40, %[srdwq], s68 offen lds\n\t"
    "v_fma_f32 v255, v234, v245, v247\n\t"
    "s_waitcnt lgkmcnt(8)\n\t"
    "v_mfma_f32_16x16x32_bf16 v[112:115], a[240:243], v[220:223], 0\n\t"
    "ds_read_b64_tr_b16 a[200:201], v26 offset:49152\n\t"
    "ds_read_b64_tr_b16 a[202:203], v26 offset:53248\n\t"
    "v_fma_f32 v254, v254, v244, v246\n\t"
    "v_cvt_pk_bf16_f32 v234, v254, v255\n\t"
    "v_mfma_f32_16x16x32_bf16 v[116:119], a[240:243], v[224:227], 0\n\t"
    "v_lshlrev_b32 v252, 16, v231\n\t"
    "v_mfma_f32_16x16x32_bf16 v[176:179], a[244:247], v[220:223], 0\n\t"
    "ds_read_b64_tr_b16 a[204:205], v27 offset:49152\n\t"
    "ds_read_b64_tr_b16 a[206:207], v27 offset:53248\n\t"
    "v_fma_f32 v253, v231, v249, v251\n\t"
    "v_fma_f32 v252, v252, v248, v250\n\t"
    "v_mfma_f32_16x16x32_bf16 v[180:183], a[244:247], v[224:227], 0\n\t"
    "buffer_load_dwordx4 v41, %[srdwq], s68 offen offset:1024 lds\n\t"
    "v_cvt_pk_bf16_f32 v231, v252, v253\n\t"
    "s_waitcnt lgkmcnt(8)\n\t"
    "v_mfma_f32_16x16x32_bf16 v[120:123], a[248:251], v[220:223], 0\n\t"
    "ds_read_b64_tr_b16 a[208:209], v28 offset:49152\n\t"
    "ds_read_b64_tr_b16 a[210:211], v28 offset:53248\n\t"
    "v_lshlrev_b32 v254, 16, v235\n\t"
    "v_fma_f32 v255, v235, v249, v251\n\t"
    "v_mfma_f32_16x16x32_bf16 v[124:127], a[248:251], v[224:227], 0\n\t"
    "buffer_load_dwordx4 v42, %[srdwq], s68 offen offset:2048 lds\n\t"
    "v_fma_f32 v254, v254, v248, v250\n\t"
    "v_mfma_f32_16x16x32_bf16 v[184:187], a[252:255], v[220:223], 0\n\t"
    "ds_read_b64_tr_b16 a[212:213], v29 offset:49152\n\t"
    "ds_read_b64_tr_b16 a[214:215], v29 offset:53248\n\t"
    "v_cvt_pk_bf16_f32 v235, v254, v255\n\t"
    "v_mfma_f32_16x16x32_bf16 v[188:191], a[252:255], v[224:227], 0\n\t"
    "s_waitcnt lgkmcnt(8)\n\t"
    "v_mfma_f32_16x16x32_bf16 v[64:67], a[192:195], v[228:231], v[64:67]\n\t"
    "ds_read_b64_tr_b16 a[216:217], v30 offset:49152\n\t"
    "ds_read_b64_tr_b16 a[218:219], v30 offset:53248\n\t"
    "v_mfma_f32_16x16x32_bf16 v[68:71], a[192:195], v[232:235], v[68:71]\n\t"
    "ds_read_b128 v[220:223], v55 offset:8192\n\t"
    "ds_read_b128 v[224:227], v55 offset:8448\n\t"
    "v_mfma_f32_16x16x32_bf16 v[128:131], a[196:199], v[228:231], v[128:131]\n\t"
    "ds_read_b64_tr_b16 a[220:221], v31 offset:49152\n\t"
    "ds_read_b64_tr_b16 a[222:223], v31 offset:53248\n\t"
    "ds_read_b128 v[236:239], v54 offset:512\n\t"
    "ds_read_b128 v[240:243], v54 offset:528\n\t"
    "v_mfma_f32_16x16x32_bf16 v[132:135], a[196:199], v[232:235], v[132:135]\n\t"
    "buffer_load_dwordx4 v43, %[srdwq], s68 offen offset:3072 lds\n\t"
    "s_add_u32 m0, s69, 0x14000\n\t"
    "ds_read_b128 v[244:247], v54 offset:544\n\t"
    "ds_read_b128 v[248:251], v54 offset:560\n\t"
    "s_waitcnt lgkmcnt(14)\n\t"
    "v_mfma_f32_16x16x32_bf16 v[72:75], a[200:203], v[228:231], v[72:75]\n\t"
    "ds_read_b64_tr_b16 a[224:225], v24 offset:49408\n\t"
    "ds_read_b64_tr_b16 a[226:227], v24 offset:53504\n\t"
    "s_add_u32 s68, s67, 0xc0200\n\t"
    "v_mfma_f32_16x16x32_bf16 v[76:79], a[200:203], v[232:235], v[76:79]\n\t"
    "buffer_load_dwordx4 v40, %[srdwq], s68 offen lds\n\t"
    "v_mfma_f32_16x16x32_bf16 v[136:139], a[204:207], v[228:231], v[136:139]\n\t"
    "ds_read_b64_tr_b16 a[228:229], v25 offset:49408\n\t"
    "ds_read_b64_tr_b16 a[230:231], v25 offset:53504\n\t"
    "v_mfma_f32_16x16x32_bf16 v[140:143], a[204:207], v[232:235], v[140:143]\n\t"
    "buffer_load_dwordx4 v41, %[srdwq], s68 offen offset:1024 lds\n\t"
    "s_waitcnt lgkmcnt(14)\n\t"
    "v_mfma_f32_16x16x32_bf16 v[80:83], a[208:211], v[228:231], v[80:83]\n\t"
    "ds_read_b64_tr_b16 a[232:233], v26 offset:49408\n\t"
    "ds_read_b64_tr_b16 a[234:235], v26 offset:53504\n\t"
    "v_mfma_f32_16x16x32_bf16 v[84:87], a[208:211], v[232:235], v[84:87]\n\t"
    "s_waitcnt lgkmcnt(6)\n\t"
    "v_lshlrev_b32 v252, 16, v220\n\t"
    "v_fma_f32 v253, v220, v237, v239\n\t"
    "v_mfma_f32_16x16x32_bf16 v[144:147], a[212:215], v[228:231], v[144:147]\n\t"
    "ds_read_b64_tr_b16 a[236:237], v27 offset:49408\n\t"
    "ds_read_b64_tr_b16 a[238:239], v27 offset:53504\n\t"
    "v_fma_f32 v252, v252, v236, v238\n\t"
    "v_mfma_f32_16x16x32_bf16 v[148:151], a[212:215], v[232:235], v[148:151]\n\t"
    "buffer_load_dwordx4 v42, %[srdwq], s68 offen offset:2048 lds\n\t"
    "v_cvt_pk_bf16_f32 v220, v252, v253\n\t"
    "v_lshlrev_b32 v254, 16, v224\n\t"
    "v_mfma_f32_16x16x32_bf16 v[88:91], a[216:219], v[228:231], v[88:91]\n\t"
    "ds_read_b64_tr_b16 a[240:241], v28 offset:49408\n\t"
    "ds_read_b64_tr_b16 a[242:243], v28 offset:53504\n\t"
    "v_fma_f32 v255, v224, v237, v239\n\t"
    "v_mfma_f32_16x16x32_bf16 v[92:95], a[216:219], v[232:235], v[92:95]\n\t"
    "buffer_load_dwordx4 v43, %[srdwq], s68 offen offset:3072 lds\n\t"
    "v_fma_f32 v254, v254, v236, v238\n\t"
    "v_cvt_pk_bf16_f32 v224, v254, v255\n\t"
    "v_mfma_f32_16x16x32_bf16 v[152:155], a[220:223], v[228:231], v[152:155]\n\t"
    "ds_read_b64_tr_b16 a[244:245], v29 offset:49408\n\t"
    "ds_read_b64_tr_b16 a[246:247], v29 offset:53504\n\t"
    "v_lshlrev_b32 v252, 16, v221\n\t"
    "v_mfma_f32_16x16x32_bf16 v[156:159], a[220:223], v[232:235], v[156:159]\n\t"
    "v_fma_f32 v253, v221, v241, v243\n\t"
    "v_fma_f32 v252, v252, v240, v242\n\t"
    "s_waitcnt lgkmcnt(8)\n\t"
    "v_mfma_f32_16x16x32_bf16 v[96:99], a[224:227], v[228:231], v[96:99]\n\t"
    "ds_read_b64_tr_b16 a[248:249], v30 offset:49408\n\t"
    "ds_read_b64_tr_b16 a[250:251], v30 offset:53504\n\t"
    "v_cvt_pk_bf16_f32 v221, v252, v253\n\t"
    "v_mfma_f32_16x16x32_bf16 v[100:103], a[224:227], v[232:235], v[100:103]\n\t"
    "s_waitcnt vmcnt(9)\n\t"
    "s_barrier\n\t"
    "v_lshlrev_b32 v254, 16, v225\n\t"
    "v_fma_f32 v255, v225, v241, v243\n\t"
    "v_mfma_f32_16x16x32_bf16 v[160:163], a[228:231], v[228:231], v[160:163]\n\t"
    "ds_read_b64_tr_b16 a[252:253], v31 offset:49408\n\t"
    "ds_read_b64_tr_b16 a[254:255], v31 offset:53504\n\t"
    "v_fma_f32 v254, v254, v240, v242\n\t"
    "v_mfma_f32_16x16x32_bf16 v[164:167], a[228:231], v[232:235], v[164:167]\n\t"
    "s_add_u32 m0, s75, 0x6000\n\t"
    "s_add_u32 s68, s73, 0x2800\n\t"
    "v_cvt_pk_bf16_f32 v225, v254, v255\n\t"
    "s_waitcnt lgkmcnt(8)\n\t"
    "v_mfma_f32_16x16x32_bf16 v[104:107], a[232:235], v[228:231], v[104:107]\n\t"
    "ds_read_b64_tr_b16 a[192:193], v24\n\t"
    "ds_read_b64_tr_b16 a[194:195], v24 offset:4096\n\t"
    "v_lshlrev_b32 v252, 16, v222\n\t"
    "v_fma_f32 v253, v222, v245, v247\n\t"
    "v_mfma_f32_16x16x32_bf16 v[108:111], a[232:235], v[232:235], v[108:111]\n\t"
    "buffer_load_dwordx4 v52, %[srdxn], s68 offen lds\n\t"
    "s_add_u32 m0, s69, 0x8000\n\t"
    "s_add_u32 s68, s67, 0x100000\n\t"
    "v_fma_f32 v252, v252, v244, v246\n\t"
    "v_mfma_f32_16x16x32_bf16 v[168:171], a[236:239], v[228:231], v[168:171]\n\t"
    "ds_read_b64_tr_b16 a[196:197], v25\n\t"
    "ds_read_b64_tr_b16 a[198:199], v25 offset:4096\n\t"
    "v_cvt_pk_bf16_f32 v222, v252, v253\n\t"
    "v_lshlrev_b32 v254, 16, v226\n\t"
    "v_mfma_f32_16x16x32_bf16 v[172:175], a[236:239], v[232:235], v[172:175]\n\t"
    "buffer_load_dwordx4 v40, %[srdwq], s68 offen lds\n\t"
    "v_fma_f32 v255, v226, v245, v247\n\t"
    "s_waitcnt lgkmcnt(8)\n\t"
    "v_mfma_f32_16x16x32_bf16 v[112:115], a[240:243], v[228:231], v[112:115]\n\t"
    "ds_read_b64_tr_b16 a[200:201], v26\n\t"
    "ds_read_b64_tr_b16 a[202:203], v26 offset:4096\n\t"
    "v_fma_f32 v254, v254, v244, v246\n\t"
    "v_cvt_pk_bf16_f32 v226, v254, v255\n\t"
    "v_mfma_f32_16x16x32_bf16 v[116:119], a[240:243], v[232:235], v[116:119]\n\t"
    "v_lshlrev_b32 v252, 16, v223\n\t"
    "v_mfma_f32_16x16x32_bf16 v[176:179], a[244:247], v[228:231], v[176:179]\n\t"
    "ds_read_b64_tr_b16 a[204:205], v27\n\t"
    "ds_read_b64_tr_b16 a[206:207], v27 offset:4096\n\t"
    "v_fma_f32 v253, v223, v249, v251\n\t"
    "v_fma_f32 v252, v252, v248, v250\n\t"
    "v_mfma_f32_16x16x32_bf16 v[180:183], a[244:247], v[232:235], v[180:183]\n\t"
    "buffer_load_dwordx4 v41, %[srdwq], s68 offen offset:1024 lds\n\t"
    "v_cvt_pk_bf16_f32 v223, v252, v253\n\t"
    "s_waitcnt lgkmcnt(8)\n\t"
    "v_mfma_f32_16x16x32_bf16 v[120:123], a[248:251], v[228:231], v[120:123]\n\t"
    "ds_read_b64_tr_b16 a[208:209], v28\n\t"
    "ds_read_b64_tr_b16 a[210:211], v28 offset:4096\n\t"
    "v_lshlrev_b32 v254, 16, v227\n\t"
    "v_fma_f32 v255, v227, v249, v251\n\t"
    "v_mfma_f32_16x16x32_bf16 v[124:127], a[248:251], v[232:235], v[124:127]\n\t"
    "buffer_load_dwordx4 v42, %[srdwq], s68 offen offset:2048 lds\n\t"
    "v_fma_f32 v254, v254, v248, v250\n\t"
    "v_mfma_f32_16x16x32_bf16 v[184:187], a[252:255], v[228:231], v[184:187]\n\t"
    "ds_read_b64_tr_b16 a[212:213], v29\n\t"
    "ds_read_b64_tr_b16 a[214:215], v29 offset:4096\n\t"
    "v_cvt_pk_bf16_f32 v227, v254, v255\n\t"
    "v_mfma_f32_16x16x32_bf16 v[188:191], a[252:255], v[232:235], v[188:191]\n\t"
    "s_waitcnt lgkmcnt(8)\n\t"
    "v_mfma_f32_16x16x32_bf16 v[64:67], a[192:195], v[220:223], v[64:67]\n\t"
    "ds_read_b64_tr_b16 a[216:217], v30\n\t"
    "ds_read_b64_tr_b16 a[218:219], v30 offset:4096\n\t"
    "v_mfma_f32_16x16x32_bf16 v[68:71], a[192:195], v[224:227], v[68:71]\n\t"
    "ds_read_b128 v[228:231], v55 offset:12288\n\t"
    "ds_read_b128 v[232:235], v55 offset:12544\n\t"
    "v_mfma_f32_16x16x32_bf16 v[128:131], a[196:199], v[220:223], v[128:131]\n\t"
    "ds_read_b64_tr_b16 a[220:221], v31\n\t"
    "ds_read_b64_tr_b16 a[222:223], v31 offset:4096\n\t"
    "ds_read_b128 v[236:239], v54 offset:768\n\t"
    "ds_read_b128 v[240:243], v54 offset:784\n\t"
    "v_mfma_f32_16x16x32_bf16 v[132:135], a[196:199], v[224:227], v[132:135]\n\t"
    "buffer_load_dwordx4 v43, %[srdwq], s68 offen offset:3072 lds\n\t"
    "s_add_u32 m0, s69, 0x18000\n\t"
    "ds_read_b128 v[244:247], v54 offset:800\n\t"
    "ds_read_b128 v[248:251], v54 offset:816\n\t"
    "s_waitcnt lgkmcnt(14)\n\t"
    "v_mfma_f32_16x16x32_bf16 v[72:75], a[200:203], v[220:223], v[72:75]\n\t"
    "ds_read_b64_tr_b16 a[224:225], v24 offset:256\n\t"
    "ds_read_b64_tr_b16 a[226:227], v24 offset:4352\n\t"
    "s_add_u32 s68, s67, 0x100200\n\t"
    "v_mfma_f32_16x16x32_bf16 v[76:79], a[200:203], v[224:227], v[76:79]\n\t"
    "buffer_load_dwordx4 v40, %[srdwq], s68 offen lds\n\t"
    "v_mfma_f32_16x16x32_bf16 v[136:139], a[204:207], v[220:223], v[136:139]\n\t"
    "ds_read_b64_tr_b16 a[228:229], v25 offset:256\n\t"
    "ds_read_b64_tr_b16 a[230:231], v25 offset:4352\n\t"
    "v_mfma_f32_16x16x32_bf16 v[140:143], a[204:207], v[224:227], v[140:143]\n\t"
    "buffer_load_dwordx4 v41, %[srdwq], s68 offen offset:1024 lds\n\t"
    "s_waitcnt lgkmcnt(14)\n\t"
    "v_mfma_f32_16x16x32_bf16 v[80:83], a[208:211], v[220:223], v[80:83]\n\t"
    "ds_read_b64_tr_b16 a[232:233], v26 offset:256\n\t"
    "ds_read_b64_tr_b16 a[234:235], v26 offset:4352\n\t"
    "v_mfma_f32_16x16x32_bf16 v[84:87], a[208:211], v[224:227], v[84:87]\n\t"
    "s_waitcnt lgkmcnt(6)\n\t"
    "v_lshlrev_b32 v252, 16, v228\n\t"
    "v_fma_f32 v253, v228, v237, v239\n\t"
    "v_mfma_f32_16x16x32_bf16 v[144:147], a[212:215], v[220:223], v[144:147]\n\t"
    "ds_read_b64_tr_b16 a[236:237], v27 offset:256\n\t"
    "ds_read_b64_tr_b16 a[238:239], v27 offset:4352\n\t"
    "v_fma_f32 v252, v252, v236, v238\n\t"
    "v_mfma_f32_16x16x32_bf16 v[148:151], a[212:215], v[224:227], v[148:151]\n\t"
    "buffer_load_dwordx4 v42, %[srdwq], s68 offen offset:2048 lds\n\t"
    "v_cvt_pk_bf16_f32 v228, v252, v253\n\t"
    "v_lshlrev_b32 v254, 16, v232\n\t"
    "v_mfma_f32_16x16x32_bf16 v[88:91], a[216:219], v[220:223], v[88:91]\n\t"
    "ds_read_b64_tr_b16 a[240:241], v28 offset:256\n\t"
    "ds_read_b64_tr_b16 a[242:243], v28 offset:4352\n\t"
    "v_fma_f32 v255, v232, v237, v239\n\t"
    "v_mfma_f32_16x16x32_bf16 v[92:95], a[216:219], v[224:227], v[92:95]\n\t"
    "buffer_load_dwordx4 v43, %[srdwq], s68 offen offset:3072 lds\n\t"
    "v_fma_f32 v254, v254, v236, v238\n\t"
    "v_cvt_pk_bf16_f32 v232, v254, v255\n\t"
    "v_mfma_f32_16x16x32_bf16 v[152:155], a[220:223], v[220:223], v[152:155]\n\t"
    "ds_read_b64_tr_b16 a[244:245], v29 offset:256\n\t"
    "ds_read_b64_tr_b16 a[246:247], v29 offset:4352\n\t"
    "v_lshlrev_b32 v252, 16, v229\n\t"
    "v_mfma_f32_16x16x32_bf16 v[156:159], a[220:223], v[224:227], v[156:159]\n\t"
    "v_fma_f32 v253, v229, v241, v243\n\t"
    "v_fma_f32 v252, v252, v240, v242\n\t"
    "s_waitcnt lgkmcnt(8)\n\t"
    "v_mfma_f32_16x16x32_bf16 v[96:99], a[224:227], v[220:223], v[96:99]\n\t"
    "ds_read_b64_tr_b16 a[248:249], v30 offset:256\n\t"
    "ds_read_b64_tr_b16 a[250:251], v30 offset:4352\n\t"
    "v_cvt_pk_bf16_f32 v229, v252, v253\n\t"
    "v_mfma_f32_16x16x32_bf16 v[100:103], a[224:227], v[224:227], v[100:103]\n\t"
    "s_waitcnt vmcnt(9)\n\t"
    "s_barrier\n\t"
    "v_lshlrev_b32 v254, 16, v233\n\t"
    "v_fma_f32 v255, v233, v241, v243\n\t"
    "v_mfma_f32_16x16x32_bf16 v[160:163], a[228:231], v[220:223], v[160:163]\n\t"
    "ds_read_b64_tr_b16 a[252:253], v31 offset:256\n\t"
    "ds_read_b64_tr_b16 a[254:255], v31 offset:4352\n\t"
    "v_fma_f32 v254, v254, v240, v242\n\t"
    "v_mfma_f32_16x16x32_bf16 v[164:167], a[228:231], v[224:227], v[164:167]\n\t"
    "s_add_u32 m0, s75, 0x7000\n\t"
    "s_add_u32 s68, s73, 0x3000\n\t"
    "v_cvt_pk_bf16_f32 v233, v254, v255\n\t"
    "s_waitcnt lgkmcnt(8)\n\t"
    "v_mfma_f32_16x16x32_bf16 v[104:107], a[232:235], v[220:223], v[104:107]\n\t"
    "ds_read_b64_tr_b16 a[192:193], v24 offset:16384\n\t"
    "ds_read_b64_tr_b16 a[194:195], v24 offset:20480\n\t"
    "v_lshlrev_b32 v252, 16, v230\n\t"
    "v_fma_f32 v253, v230, v245, v247\n\t"
    "v_mfma_f32_16x16x32_bf16 v[108:111], a[232:235], v[224:227], v[108:111]\n\t"
    "buffer_load_dwordx4 v52, %[srdxn], s68 offen lds\n\t"
    "s_add_u32 m0, s69, 0xc000\n\t"
    "s_add_u32 s68, s67, 0x140000\n\t"
    "v_fma_f32 v252, v252, v244, v246\n\t"
    "v_mfma_f32_16x16x32_bf16 v[168:171], a[236:239], v[220:223], v[168:171]\n\t"
    "ds_read_b64_tr_b16 a[196:197], v25 offset:16384\n\t"
    "ds_read_b64_tr_b16 a[198:199], v25 offset:20480\n\t"
    "v_cvt_pk_bf16_f32 v230, v252, v253\n\t"
    "v_lshlrev_b32 v254, 16, v234\n\t"
    "v_mfma_f32_16x16x32_bf16 v[172:175], a[236:239], v[224:227], v[172:175]\n\t"
    "buffer_load_dwordx4 v40, %[srdwq], s68 offen lds\n\t"
    "v_fma_f32 v255, v234, v245, v247\n\t"
    "s_waitcnt lgkmcnt(8)\n\t"
    "v_mfma_f32_16x16x32_bf16 v[112:115], a[240:243], v[220:223], v[112:115]\n\t"
    "ds_read_b64_tr_b16 a[200:201], v26 offset:16384\n\t"
    "ds_read_b64_tr_b16 a[202:203], v26 offset:20480\n\t"
    "v_fma_f32 v254, v254, v244, v246\n\t"
    "v_cvt_pk_bf16_f32 v234, v254, v255\n\t"
    "v_mfma_f32_16x16x32_bf16 v[116:119], a[240:243], v[224:227], v[116:119]\n\t"
    "v_lshlrev_b32 v252, 16, v231\n\t"
    "v_mfma_f32_16x16x32_bf16 v[176:179], a[244:247], v[220:223], v[176:179]\n\t"
    "ds_read_b64_tr_b16 a[204:205], v27 offset:16384\n\t"
    "ds_read_b64_tr_b16 a[206:207], v27 offset:20480\n\t"
    "v_fma_f32 v253, v231, v249, v251\n\t"
    "v_fma_f32 v252, v252, v248, v250\n\t"
    "v_mfma_f32_16x16x32_bf16 v[180:183], a[244:247], v[224:227], v[180:183]\n\t"
    "buffer_load_dwordx4 v41, %[srdwq], s68 offen offset:1024 lds\n\t"
    "v_cvt_pk_bf16_f32 v231, v252, v253\n\t"
    "s_waitcnt lgkmcnt(8)\n\t"
    "v_mfma_f32_16x16x32_bf16 v[120:123], a[248:251], v[220:223], v[120:123]\n\t"
    "ds_read_b64_tr_b16 a[208:209], v28 offset:16384\n\t"
    "ds_read_b64_tr_b16 a[210:211], v28 offset:20480\n\t"
    "v_lshlrev_b32 v254, 16, v235\n\t"
    "v_fma_f32 v255, v235, v249, v251\n\t"
    "v_mfma_f32_16x16x32_bf16 v[124:127], a[248:251], v[224:227], v[124:127]\n\t"
    "buffer_load_dwordx4 v42, %[srdwq], s68 offen offset:2048 lds\n\t"
    "v_fma_f32 v254, v254, v248, v250\n\t"
    "v_mfma_f32_16x16x32_bf16 v[184:187], a[252:255], v[220:223], v[184:187]\n\t"
    "ds_read_b64_tr_b16 a[212:213], v29 offset:16384\n\t"
    "ds_read_b64_tr_b16 a[214:215], v29 offset:20480\n\t"
    "v_cvt_pk_bf16_f32 v235, v254, v255\n\t"
    "v_mfma_f32_16x16x32_bf16 v[188:191], a[252:255], v[224:227], v[188:191]\n\t"
    "s_waitcnt lgkmcnt(8)\n\t"
    "v_mfma_f32_16x16x32_bf16 v[64:67], a[192:195], v[228:231], v[64:67]\n\t"
    "ds_read_b64_tr_b16 a[216:217], v30 offset:16384\n\t"
    "ds_read_b64_tr_b16 a[218:219], v30 offset:20480\n\t"
    "v_mfma_f32_16x16x32_bf16 v[68:71], a[192:195], v[232:235], v[68:71]\n\t"
    "ds_read_b128 v[220:223], v55 offset:16384\n\t"
    "ds_read_b128 v[224:227], v55 offset:16640\n\t"
    "v_mfma_f32_16x16x32_bf16 v[128:131], a[196:199], v[228:231], v[128:131]\n\t"
    "ds_read_b64_tr_b16 a[220:221], v31 offset:16384\n\t"
    "ds_read_b64_tr_b16 a[222:223], v31 offset:20480\n\t"
    "ds_read_b128 v[236:239], v54 offset:1024\n\t"
    "ds_read_b128 v[240:243], v54 offset:1040\n\t"
    "v_mfma_f32_16x16x32_bf16 v[132:135], a[196:199], v[232:235], v[132:135]\n\t"
    "buffer_load_dwordx4 v43, %[srdwq], s68 offen offset:3072 lds\n\t"
    "s_add_u32 m0, s69, 0x1c000\n\t"
    "ds_read_b128 v[244:247], v54 offset:1056\n\t"
    "ds_read_b128 v[248:251], v54 offset:1072\n\t"
    "s_waitcnt lgkmcnt(14)\n\t"
    "v_mfma_f32_16x16x32_bf16 v[72:75], a[200:203], v[228:231], v[72:75]\n\t"
    "ds_read_b64_tr_b16 a[224:225], v24 offset:16640\n\t"
    "ds_read_b64_tr_b16 a[226:227], v24 offset:20736\n\t"
    "s_add_u32 s68, s67, 0x140200\n\t"
    "v_mfma_f32_16x16x32_bf16 v[76:79], a[200:203], v[232:235], v[76:79]\n\t"
    "buffer_load_dwordx4 v40, %[srdwq], s68 offen lds\n\t"
    "v_mfma_f32_16x16x32_bf16 v[136:139], a[204:207], v[228:231], v[136:139]\n\t"
    "ds_read_b64_tr_b16 a[228:229], v25 offset:16640\n\t"
    "ds_read_b64_tr_b16 a[230:231], v25 offset:20736\n\t"
    "v_mfma_f32_16x16x32_bf16 v[140:143], a[204:207], v[232:235], v[140:143]\n\t"
    "buffer_load_dwordx4 v41, %[srdwq], s68 offen offset:1024 lds\n\t"
    "s_waitcnt lgkmcnt(14)\n\t"
    "v_mfma_f32_16x16x32_bf16 v[80:83], a[208:211], v[228:231], v[80:83]\n\t"
    "ds_read_b64_tr_b16 a[232:233], v26 offset:16640\n\t"
    "ds_read_b64_tr_b16 a[234:235], v26 offset:20736\n\t"
    "v_mfma_f32_16x16x32_bf16 v[84:87], a[208:211], v[232:235], v[84:87]\n\t"
    "s_waitcnt lgkmcnt(6)\n\t"
    "v_lshlrev_b32 v252, 16, v220\n\t"
    "v_fma_f32 v253, v220, v237, v239\n\t"
    "v_mfma_f32_16x16x32_bf16 v[144:147], a[212:215], v[228:231], v[144:147]\n\t"
    "ds_read_b64_tr_b16 a[236:237], v27 offset:16640\n\t"
    "ds_read_b64_tr_b16 a[238:239], v27 offset:20736\n\t"
    "v_fma_f32 v252, v252, v236, v238\n\t"
    "v_mfma_f32_16x16x32_bf16 v[148:151], a[212:215], v[232:235], v[148:151]\n\t"
    "buffer_load_dwordx4 v42, %[srdwq], s68 offen offset:2048 lds\n\t"
    "v_cvt_pk_bf16_f32 v220, v252, v253\n\t"
    "v_lshlrev_b32 v254, 16, v224\n\t"
    "v_mfma_f32_16x16x32_bf16 v[88:91], a[216:219], v[228:231], v[88:91]\n\t"
    "ds_read_b64_tr_b16 a[240:241], v28 offset:16640\n\t"
    "ds_read_b64_tr_b16 a[242:243], v28 offset:20736\n\t"
    "v_fma_f32 v255, v224, v237, v239\n\t"
    "v_mfma_f32_16x16x32_bf16 v[92:95], a[216:219], v[232:235], v[92:95]\n\t"
    "buffer_load_dwordx4 v43, %[srdwq], s68 offen offset:3072 lds\n\t"
    "v_fma_f32 v254, v254, v236, v238\n\t"
    "v_cvt_pk_bf16_f32 v224, v254, v255\n\t"
    "v_mfma_f32_16x16x32_bf16 v[152:155], a[220:223], v[228:231], v[152:155]\n\t"
    "ds_read_b64_tr_b16 a[244:245], v29 offset:16640\n\t"
    "ds_read_b64_tr_b16 a[246:247], v29 offset:20736\n\t"
    "v_lshlrev_b32 v252, 16, v221\n\t"
    "v_mfma_f32_16x16x32_bf16 v[156:159], a[220:223], v[232:235], v[156:159]\n\t"
    "v_fma_f32 v253, v221, v241, v243\n\t"
    "v_fma_f32 v252, v252, v240, v242\n\t"
    "s_waitcnt lgkmcnt(8)\n\t"
    "v_mfma_f32_16x16x32_bf16 v[96:99], a[224:227], v[228:231], v[96:99]\n\t"
    "ds_read_b64_tr_b16 a[248:249], v30 offset:16640\n\t"
    "ds_read_b64_tr_b16 a[250:251], v30 offset:20736\n\t"
    "v_cvt_pk_bf16_f32 v221, v252, v253\n\t"
    "v_mfma_f32_16x16x32_bf16 v[100:103], a[224:227], v[232:235], v[100:103]\n\t"
    "s_waitcnt vmcnt(9)\n\t"
    "s_barrier\n\t"
    "v_lshlrev_b32 v254, 16, v225\n\t"
    "v_fma_f32 v255, v225, v241, v243\n\t"
    "v_mfma_f32_16x16x32_bf16 v[160:163], a[228:231], v[228:231], v[160:163]\n\t"
    "ds_read_b64_tr_b16 a[252:253], v31 offset:16640\n\t"
    "ds_read_b64_tr_b16 a[254:255], v31 offset:20736\n\t"
    "v_fma_f32 v254, v254, v240, v242\n\t"
    "v_mfma_f32_16x16x32_bf16 v[164:167], a[228:231], v[232:235], v[164:167]\n\t"
    "s_add_u32 m0, s75, 0x1000\n\t"
    "s_add_u32 s68, s73, 0x3800\n\t"
    "v_cvt_pk_bf16_f32 v225, v254, v255\n\t"
    "s_waitcnt lgkmcnt(8)\n\t"
    "v_mfma_f32_16x16x32_bf16 v[104:107], a[232:235], v[228:231], v[104:107]\n\t"
    "ds_read_b64_tr_b16 a[192:193], v24 offset:32768\n\t"
    "ds_read_b64_tr_b16 a[194:195], v24 offset:36864\n\t"
    "v_lshlrev_b32 v252, 16, v222\n\t"
    "v_fma_f32 v253, v222, v245, v247\n\t"
    "v_mfma_f32_16x16x32_bf16 v[108:111], a[232:235], v[232:235], v[108:111]\n\t"
    "buffer_load_dwordx4 v52, %[srdxn], s68 offen lds\n\t"
    "s_add_u32 m0, s69, 0x0\n\t"
    "s_add_u32 s68, s67, 0x180000\n\t"
    "v_fma_f32 v252, v252, v244, v246\n\t"
    "v_mfma_f32_16x16x32_bf16 v[168:171], a[236:239], v[228:231], v[168:171]\n\t"
    "ds_read_b64_tr_b16 a[196:197], v25 offset:32768\n\t"
    "ds_read_b64_tr_b16 a[198:199], v25 offset:36864\n\t"
    "v_cvt_pk_bf16_f32 v222, v252, v253\n\t"
    "v_lshlrev_b32 v254, 16, v226\n\t"
    "v_mfma_f32_16x16x32_bf16 v[172:175], a[236:239], v[232:235], v[172:175]\n\t"
    "buffer_load_dwordx4 v40, %[srdwq], s68 offen lds\n\t"
    "v_fma_f32 v255, v226, v245, v247\n\t"
    "s_waitcnt lgkmcnt(8)\n\t"
    "v_mfma_f32_16x16x32_bf16 v[112:115], a[240:243], v[228:231], v[112:115]\n\t"
    "ds_read_b64_tr_b16 a[200:201], v26 offset:32768\n\t"
    "ds_read_b64_tr_b16 a[202:203], v26 offset:36864\n\t"
    "v_fma_f32 v254, v254, v244, v246\n\t"
    "v_cvt_pk_bf16_f32 v226, v254, v255\n\t"
    "v_mfma_f32_16x16x32_bf16 v[116:119], a[240:243], v[232:235], v[116:119]\n\t"
    "v_lshlrev_b32 v252, 16, v223\n\t"
    "v_mfma_f32_16x16x32_bf16 v[176:179], a[244:247], v[228:231], v[176:179]\n\t"
    "ds_read_b64_tr_b16 a[204:205], v27 offset:32768\n\t"
    "ds_read_b64_tr_b16 a[206:207], v27 offset:36864\n\t"
    "v_fma_f32 v253, v223, v249, v251\n\t"
    "v_fma_f32 v252, v252, v248, v250\n\t"
    "v_mfma_f32_16x16x32_bf16 v[180:183], a[244:247], v[232:235], v[180:183]\n\t"
    "buffer_load_dwordx4 v41, %[srdwq], s68 offen offset:1024 lds\n\t"
    "v_cvt_pk_bf16_f32 v223, v252, v253\n\t"
    "s_waitcnt lgkmcnt(8)\n\t"
    "v_mfma_f32_16x16x32_bf16 v[120:123], a[248:251], v[228:231], v[120:123]\n\t"
    "ds_read_b64_tr_b16 a[208:209], v28 offset:32768\n\t"
    "ds_read_b64_tr_b16 a[210:211], v28 offset:36864\n\t"
    "v_lshlrev_b32 v254, 16, v227\n\t"
    "v_fma_f32 v255, v227, v249, v251\n\t"
    "v_mfma_f32_16x16x32_bf16 v[124:127], a[248:251], v[232:235], v[124:127]\n\t"
    "buffer_load_dwordx4 v42, %[srdwq], s68 offen offset:2048 lds\n\t"
    "v_fma_f32 v254, v254, v248, v250\n\t"
    "v_mfma_f32_16x16x32_bf16 v[184:187], a[252:255], v[228:231], v[184:187]\n\t"
    "ds_read_b64_tr_b16 a[212:213], v29 offset:32768\n\t"
    "ds_read_b64_tr_b16 a[214:215], v29 offset:36864\n\t"
    "v_cvt_pk_bf16_f32 v227, v254, v255\n\t"
    "v_mfma_f32_16x16x32_bf16 v[188:191], a[252:255], v[232:235], v[188:191]\n\t"
    "s_waitcnt lgkmcnt(8)\n\t"
    "v_mfma_f32_16x16x32_bf16 v[64:67], a[192:195], v[220:223], v[64:67]\n\t"
    "ds_read_b64_tr_b16 a[216:217], v30 offset:32768\n\t"
    "ds_read_b64_tr_b16 a[218:219], v30 offset:36864\n\t"
    "v_mfma_f32_16x16x32_bf16 v[68:71], a[192:195], v[224:227], v[68:71]\n\t"
    "ds_read_b128 v[228:231], v55 offset:20480\n\t"
    "ds_read_b128 v[232:235], v55 offset:20736\n\t"
    "v_mfma_f32_16x16x32_bf16 v[128:131], a[196:199], v[220:223], v[128:131]\n\t"
    "ds_read_b64_tr_b16 a[220:221], v31 offset:32768\n\t"
    "ds_read_b64_tr_b16 a[222:223], v31 offset:36864\n\t"
    "ds_read_b128 v[236:239], v54 offset:1280\n\t"
    "ds_read_b128 v[240:243], v54 offset:1296\n\t"
    "v_mfma_f32_16x16x32_bf16 v[132:135], a[196:199], v[224:227], v[132:135]\n\t"
    "buffer_load_dwordx4 v43, %[srdwq], s68 offen offset:3072 lds\n\t"
    "s_add_u32 m0, s69, 0x10000\n\t"
    "ds_read_b128 v[244:247], v54 offset:1312\n\t"
    "ds_read_b128 v[248:251], v54 offset:1328\n\t"
    "s_waitcnt lgkmcnt(14)\n\t"
    "v_mfma_f32_16x16x32_bf16 v[72:75], a[200:203], v[220:223], v[72:75]\n\t"
    "ds_read_b64_tr_b16 a[224:225], v24 offset:33024\n\t"
    "ds_read_b64_tr_b16 a[226:227], v24 offset:37120\n\t"
    "s_add_u32 s68, s67, 0x180200\n\t"
    "v_mfma_f32_16x16x32_bf16 v[76:79], a[200:203], v[224:227], v[76:79]\n\t"
    "buffer_load_dwordx4 v40, %[srdwq], s68 offen lds\n\t"
    "v_mfma_f32_16x16x32_bf16 v[136:139], a[204:207], v[220:223], v[136:139]\n\t"
    "ds_read_b64_tr_b16 a[228:229], v25 offset:33024\n\t"
    "ds_read_b64_tr_b16 a[230:231], v25 offset:37120\n\t"
    "v_mfma_f32_16x16x32_bf16 v[140:143], a[204:207], v[224:227], v[140:143]\n\t"
    "buffer_load_dwordx4 v41, %[srdwq], s68 offen offset:1024 lds\n\t"
    "s_waitcnt lgkmcnt(14)\n\t"
    "v_mfma_f32_16x16x32_bf16 v[80:83], a[208:211], v[220:223], v[80:83]\n\t"
    "ds_read_b64_tr_b16 a[232:233], v26 offset:33024\n\t"
    "ds_read_b64_tr_b16 a[234:235], v26 offset:37120\n\t"
    "v_mfma_f32_16x16x32_bf16 v[84:87], a[208:211], v[224:227], v[84:87]\n\t"
    "s_waitcnt lgkmcnt(6)\n\t"
    "v_lshlrev_b32 v252, 16, v228\n\t"
    "v_fma_f32 v253, v228, v237, v239\n\t"
    "v_mfma_f32_16x16x32_bf16 v[144:147], a[212:215], v[220:223], v[144:147]\n\t"
    "ds_read_b64_tr_b16 a[236:237], v27 offset:33024\n\t"
    "ds_read_b64_tr_b16 a[238:239], v27 offset:37120\n\t"
    "v_fma_f32 v252, v252, v236, v238\n\t"
    "v_mfma_f32_16x16x32_bf16 v[148:151], a[212:215], v[224:227], v[148:151]\n\t"
    "buffer_load_dwordx4 v42, %[srdwq], s68 offen offset:2048 lds\n\t"
    "v_cvt_pk_bf16_f32 v228, v252, v253\n\t"
    "v_lshlrev_b32 v254, 16, v232\n\t"
    "v_mfma_f32_16x16x32_bf16 v[88:91], a[216:219], v[220:223], v[88:91]\n\t"
    "ds_read_b64_tr_b16 a[240:241], v28 offset:33024\n\t"
    "ds_read_b64_tr_b16 a[242:243], v28 offset:37120\n\t"
    "v_fma_f32 v255, v232, v237, v239\n\t"
    "v_mfma_f32_16x16x32_bf16 v[92:95], a[216:219], v[224:227], v[92:95]\n\t"
    "buffer_load_dwordx4 v43, %[srdwq], s68 offen offset:3072 lds\n\t"
    "v_fma_f32 v254, v254, v236, v238\n\t"
    "v_cvt_pk_bf16_f32 v232, v254, v255\n\t"
    "v_mfma_f32_16x16x32_bf16 v[152:155], a[220:223], v[220:223], v[152:155]\n\t"
    "ds_read_b64_tr_b16 a[244:245], v29 offset:33024\n\t"
    "ds_read_b64_tr_b16 a[246:247], v29 offset:37120\n\t"
    "v_lshlrev_b32 v252, 16, v229\n\t"
    "v_mfma_f32_16x16x32_bf16 v[156:159], a[220:223], v[224:227], v[156:159]\n\t"
    "v_fma_f32 v253, v229, v241, v243\n\t"
    "v_fma_f32 v252, v252, v240, v242\n\t"
    "s_waitcnt lgkmcnt(8)\n\t"
    "v_mfma_f32_16x16x32_bf16 v[96:99], a[224:227], v[220:223], v[96:99]\n\t"
    "ds_read_b64_tr_b16 a[248:249], v30 offset:33024\n\t"
    "ds_read_b64_tr_b16 a[250:251], v30 offset:37120\n\t"
    "v_cvt_pk_bf16_f32 v229, v252, v253\n\t"
    "v_mfma_f32_16x16x32_bf16 v[100:103], a[224:227], v[224:227], v[100:103]\n\t"
    "s_waitcnt vmcnt(9)\n\t"
    "s_barrier\n\t"
    "v_lshlrev_b32 v254, 16, v233\n\t"
    "v_fma_f32 v255, v233, v241, v243\n\t"
    "v_mfma_f32_16x16x32_bf16 v[160:163], a[228:231], v[220:223], v[160:163]\n\t"
    "ds_read_b64_tr_b16 a[252:253], v31 offset:33024\n\t"
    "ds_read_b64_tr_b16 a[254:255], v31 offset:37120\n\t"
    "v_fma_f32 v254, v254, v240, v242\n\t"
    "v_mfma_f32_16x16x32_bf16 v[164:167], a[228:231], v[224:227], v[164:167]\n\t"
    "s_add_u32 m0, s75, 0x2000\n\t"
    "s_add_u32 s68, s73, 0x4000\n\t"
    "v_cvt_pk_bf16_f32 v233, v254, v255\n\t"
    "s_waitcnt lgkmcnt(8)\n\t"
    "v_mfma_f32_16x16x32_bf16 v[104:107], a[232:235], v[220:223], v[104:107]\n\t"
    "ds_read_b64_tr_b16 a[192:193], v24 offset:49152\n\t"
    "ds_read_b64_tr_b16 a[194:195], v24 offset:53248\n\t"
    "v_lshlrev_b32 v252, 16, v230\n\t"
    "v_fma_f32 v253, v230, v245, v247\n\t"
    "v_mfma_f32_16x16x32_bf16 v[108:111], a[232:235], v[224:227], v[108:111]\n\t"
    "buffer_load_dwordx4 v52, %[srdxn], s68 offen lds\n\t"
    "s_add_u32 m0, s69, 0x4000\n\t"
    "s_add_u32 s68, s67, 0x1c0000\n\t"
    "v_fma_f32 v252, v252, v244, v246\n\t"
    "v_mfma_f32_16x16x32_bf16 v[168:171], a[236:239], v[220:223], v[168:171]\n\t"
    "ds_read_b64_tr_b16 a[196:197], v25 offset:49152\n\t"
    "ds_read_b64_tr_b16 a[198:199], v25 offset:53248\n\t"
    "v_cvt_pk_bf16_f32 v230, v252, v253\n\t"
    "v_lshlrev_b32 v254, 16, v234\n\t"
    "v_mfma_f32_16x16x32_bf16 v[172:175], a[236:239], v[224:227], v[172:175]\n\t"
    "buffer_load_dwordx4 v40, %[srdwq], s68 offen lds\n\t"
    "v_fma_f32 v255, v234, v245, v247\n\t"
    "s_waitcnt lgkmcnt(8)\n\t"
    "v_mfma_f32_16x16x32_bf16 v[112:115], a[240:243], v[220:223], v[112:115]\n\t"
    "ds_read_b64_tr_b16 a[200:201], v26 offset:49152\n\t"
    "ds_read_b64_tr_b16 a[202:203], v26 offset:53248\n\t"
    "v_fma_f32 v254, v254, v244, v246\n\t"
    "v_cvt_pk_bf16_f32 v234, v254, v255\n\t"
    "v_mfma_f32_16x16x32_bf16 v[116:119], a[240:243], v[224:227], v[116:119]\n\t"
    "v_lshlrev_b32 v252, 16, v231\n\t"
    "v_mfma_f32_16x16x32_bf16 v[176:179], a[244:247], v[220:223], v[176:179]\n\t"
    "ds_read_b64_tr_b16 a[204:205], v27 offset:49152\n\t"
    "ds_read_b64_tr_b16 a[206:207], v27 offset:53248\n\t"
    "v_fma_f32 v253, v231, v249, v251\n\t"
    "v_fma_f32 v252, v252, v248, v250\n\t"
    "v_mfma_f32_16x16x32_bf16 v[180:183], a[244:247], v[224:227], v[180:183]\n\t"
    "buffer_load_dwordx4 v41, %[srdwq], s68 offen offset:1024 lds\n\t"
    "v_cvt_pk_bf16_f32 v231, v252, v253\n\t"
    "s_waitcnt lgkmcnt(8)\n\t"
    "v_mfma_f32_16x16x32_bf16 v[120:123], a[248:251], v[220:223], v[120:123]\n\t"
    "ds_read_b64_tr_b16 a[208:209], v28 offset:49152\n\t"
    "ds_read_b64_tr_b16 a[210:211], v28 offset:53248\n\t"
    "v_lshlrev_b32 v254, 16, v235\n\t"
    "v_fma_f32 v255, v235, v249, v251\n\t"
    "v_mfma_f32_16x16x32_bf16 v[124:127], a[248:251], v[224:227], v[124:127]\n\t"
    "buffer_load_dwordx4 v42, %[srdwq], s68 offen offset:2048 lds\n\t"
    "v_fma_f32 v254, v254, v248, v250\n\t"
    "v_mfma_f32_16x16x32_bf16 v[184:187], a[252:255], v[220:223], v[184:187]\n\t"
    "ds_read_b64_tr_b16 a[212:213], v29 offset:49152\n\t"
    "ds_read_b64_tr_b16 a[214:215], v29 offset:53248\n\t"
    "v_cvt_pk_bf16_f32 v235, v254, v255\n\t"
    "v_mfma_f32_16x16x32_bf16 v[188:191], a[252:255], v[224:227], v[188:191]\n\t"
    "s_waitcnt lgkmcnt(8)\n\t"
    "v_mfma_f32_16x16x32_bf16 v[64:67], a[192:195], v[228:231], v[64:67]\n\t"
    "ds_read_b64_tr_b16 a[216:217], v30 offset:49152\n\t"
    "ds_read_b64_tr_b16 a[218:219], v30 offset:53248\n\t"
    "v_mfma_f32_16x16x32_bf16 v[68:71], a[192:195], v[232:235], v[68:71]\n\t"
    "ds_read_b128 v[220:223], v55 offset:24576\n\t"
    "ds_read_b128 v[224:227], v55 offset:24832\n\t"
    "v_mfma_f32_16x16x32_bf16 v[128:131], a[196:199], v[228:231], v[128:131]\n\t"
    "ds_read_b64_tr_b16 a[220:221], v31 offset:49152\n\t"
    "ds_read_b64_tr_b16 a[222:223], v31 offset:53248\n\t"
    "ds_read_b128 v[236:239], v54 offset:1536\n\t"
    "ds_read_b128 v[240:243], v54 offset:1552\n\t"
    "v_mfma_f32_16x16x32_bf16 v[132:135], a[196:199], v[232:235], v[132:135]\n\t"
    "buffer_load_dwordx4 v43, %[srdwq], s68 offen offset:3072 lds\n\t"
    "s_add_u32 m0, s69, 0x14000\n\t"
    "ds_read_b128 v[244:247], v54 offset:1568\n\t"
    "ds_read_b128 v[248:251], v54 offset:1584\n\t"
    "s_waitcnt lgkmcnt(14)\n\t"
    "v_mfma_f32_16x16x32_bf16 v[72:75], a[200:203], v[228:231], v[72:75]\n\t"
    "ds_read_b64_tr_b16 a[224:225], v24 offset:49408\n\t"
    "ds_read_b64_tr_b16 a[226:227], v24 offset:53504\n\t"
    "s_add_u32 s68, s67, 0x1c0200\n\t"
    "v_mfma_f32_16x16x32_bf16 v[76:79], a[200:203], v[232:235], v[76:79]\n\t"
    "buffer_load_dwordx4 v40, %[srdwq], s68 offen lds\n\t"
    "v_mfma_f32_16x16x32_bf16 v[136:139], a[204:207], v[228:231], v[136:139]\n\t"
    "ds_read_b64_tr_b16 a[228:229], v25 offset:49408\n\t"
    "ds_read_b64_tr_b16 a[230:231], v25 offset:53504\n\t"
    "v_mfma_f32_16x16x32_bf16 v[140:143], a[204:207], v[232:235], v[140:143]\n\t"
    "buffer_load_dwordx4 v41, %[srdwq], s68 offen offset:1024 lds\n\t"
    "s_waitcnt lgkmcnt(14)\n\t"
    "v_mfma_f32_16x16x32_bf16 v[80:83], a[208:211], v[228:231], v[80:83]\n\t"
    "ds_read_b64_tr_b16 a[232:233], v26 offset:49408\n\t"
    "ds_read_b64_tr_b16 a[234:235], v26 offset:53504\n\t"
    "v_mfma_f32_16x16x32_bf16 v[84:87], a[208:211], v[232:235], v[84:87]\n\t"
    "s_waitcnt lgkmcnt(6)\n\t"
    "v_lshlrev_b32 v252, 16, v220\n\t"
    "v_fma_f32 v253, v220, v237, v239\n\t"
    "v_mfma_f32_16x16x32_bf16 v[144:147], a[212:215], v[228:231], v[144:147]\n\t"
    "ds_read_b64_tr_b16 a[236:237], v27 offset:49408\n\t"
    "ds_read_b64_tr_b16 a[238:239], v27 offset:53504\n\t"
    "v_fma_f32 v252, v252, v236, v238\n\t"
    "v_mfma_f32_16x16x32_bf16 v[148:151], a[212:215], v[232:235], v[148:151]\n\t"
    "buffer_load_dwordx4 v42, %[srdwq], s68 offen offset:2048 lds\n\t"
    "v_cvt_pk_bf16_f32 v220, v252, v253\n\t"
    "v_lshlrev_b32 v254, 16, v224\n\t"
    "v_mfma_f32_16x16x32_bf16 v[88:91], a[216:219], v[228:231], v[88:91]\n\t"
    "ds_read_b64_tr_b16 a[240:241], v28 offset:49408\n\t"
    "ds_read_b64_tr_b16 a[242:243], v28 offset:53504\n\t"
    "v_fma_f32 v255, v224, v237, v239\n\t"
    "v_mfma_f32_16x16x32_bf16 v[92:95], a[216:219], v[232:235], v[92:95]\n\t"
    "buffer_load_dwordx4 v43, %[srdwq], s68 offen offset:3072 lds\n\t"
    "v_fma_f32 v254, v254, v236, v238\n\t"
    "v_cvt_pk_bf16_f32 v224, v254, v255\n\t"
    "v_mfma_f32_16x16x32_bf16 v[152:155], a[220:223], v[228:231], v[152:155]\n\t"
    "ds_read_b64_tr_b16 a[244:245], v29 offset:49408\n\t"
    "ds_read_b64_tr_b16 a[246:247], v29 offset:53504\n\t"
    "v_lshlrev_b32 v252, 16, v221\n\t"
    "v_mfma_f32_16x16x32_bf16 v[156:159], a[220:223], v[232:235], v[156:159]\n\t"
    "v_fma_f32 v253, v221, v241, v243\n\t"
    "v_fma_f32 v252, v252, v240, v242\n\t"
    "s_waitcnt lgkmcnt(8)\n\t"
    "v_mfma_f32_16x16x32_bf16 v[96:99], a[224:227], v[228:231], v[96:99]\n\t"
    "ds_read_b64_tr_b16 a[248:249], v30 offset:49408\n\t"
    "ds_read_b64_tr_b16 a[250:251], v30 offset:53504\n\t"
    "v_cvt_pk_bf16_f32 v221, v252, v253\n\t"
    "v_mfma_f32_16x16x32_bf16 v[100:103], a[224:227], v[232:235], v[100:103]\n\t"
    "s_waitcnt vmcnt(9)\n\t"
    "s_barrier\n\t"
    "v_lshlrev_b32 v254, 16, v225\n\t"
    "v_fma_f32 v255, v225, v241, v243\n\t"
    "v_mfma_f32_16x16x32_bf16 v[160:163], a[228:231], v[228:231], v[160:163]\n\t"
    "ds_read_b64_tr_b16 a[252:253], v31 offset:49408\n\t"
    "ds_read_b64_tr_b16 a[254:255], v31 offset:53504\n\t"
    "v_fma_f32 v254, v254, v240, v242\n\t"
    "v_mfma_f32_16x16x32_bf16 v[164:167], a[228:231], v[232:235], v[164:167]\n\t"
    "s_add_u32 m0, s75, 0x3000\n\t"
    "s_add_u32 s68, s73, 0x4800\n\t"
    "v_cvt_pk_bf16_f32 v225, v254, v255\n\t"
    "s_waitcnt lgkmcnt(8)\n\t"
    "v_mfma_f32_16x16x32_bf16 v[104:107], a[232:235], v[228:231], v[104:107]\n\t"
    "ds_read_b64_tr_b16 a[192:193], v24\n\t"
    "ds_read_b64_tr_b16 a[194:195], v24 offset:4096\n\t"
    "v_lshlrev_b32 v252, 16, v222\n\t"
    "v_fma_f32 v253, v222, v245, v247\n\t"
    "v_mfma_f32_16x16x32_bf16 v[108:111], a[232:235], v[232:235], v[108:111]\n\t"
    "buffer_load_dwordx4 v52, %[srdxn], s68 offen lds\n\t"
    "s_add_u32 m0, s69, 0x8000\n\t"
    "s_add_u32 s68, s67, 0x200000\n\t"
    "v_fma_f32 v252, v252, v244, v246\n\t"
    "v_mfma_f32_16x16x32_bf16 v[168:171], a[236:239], v[228:231], v[168:171]\n\t"
    "ds_read_b64_tr_b16 a[196:197], v25\n\t"
    "ds_read_b64_tr_b16 a[198:199], v25 offset:4096\n\t"
    "v_cvt_pk_bf16_f32 v222, v252, v253\n\t"
    "v_lshlrev_b32 v254, 16, v226\n\t"
    "v_mfma_f32_16x16x32_bf16 v[172:175], a[236:239], v[232:235], v[172:175]\n\t"
    "buffer_load_dwordx4 v40, %[srdwq], s68 offen lds\n\t"
    "v_fma_f32 v255, v226, v245, v247\n\t"
    "s_waitcnt lgkmcnt(8)\n\t"
    "v_mfma_f32_16x16x32_bf16 v[112:115], a[240:243], v[228:231], v[112:115]\n\t"
    "ds_read_b64_tr_b16 a[200:201], v26\n\t"
    "ds_read_b64_tr_b16 a[202:203], v26 offset:4096\n\t"
    "v_fma_f32 v254, v254, v244, v246\n\t"
    "v_cvt_pk_bf16_f32 v226, v254, v255\n\t"
    "v_mfma_f32_16x16x32_bf16 v[116:119], a[240:243], v[232:235], v[116:119]\n\t"
    "v_lshlrev_b32 v252, 16, v223\n\t"
    "v_mfma_f32_16x16x32_bf16 v[176:179], a[244:247], v[228:231], v[176:179]\n\t"
    "ds_read_b64_tr_b16 a[204:205], v27\n\t"
    "ds_read_b64_tr_b16 a[206:207], v27 offset:4096\n\t"
    "v_fma_f32 v253, v223, v249, v251\n\t"
    "v_fma_f32 v252, v252, v248, v250\n\t"
    "v_mfma_f32_16x16x32_bf16 v[180:183], a[244:247], v[232:235], v[180:183]\n\t"
    "buffer_load_dwordx4 v41, %[srdwq], s68 offen offset:1024 lds\n\t"
    "v_cvt_pk_bf16_f32 v223, v252, v253\n\t"
    "s_waitcnt lgkmcnt(8)\n\t"
    "v_mfma_f32_16x16x32_bf16 v[120:123], a[248:251], v[228:231], v[120:123]\n\t"
    "ds_read_b64_tr_b16 a[208:209], v28\n\t"
    "ds_read_b64_tr_b16 a[210:211], v28 offset:4096\n\t"
    "v_lshlrev_b32 v254, 16, v227\n\t"
    "v_fma_f32 v255, v227, v249, v251\n\t"
    "v_mfma_f32_16x16x32_bf16 v[124:127], a[248:251], v[232:235], v[124:127]\n\t"
    "buffer_load_dwordx4 v42, %[srdwq], s68 offen offset:2048 lds\n\t"
    "v_fma_f32 v254, v254, v248, v250\n\t"
    "v_mfma_f32_16x16x32_bf16 v[184:187], a[252:255], v[228:231], v[184:187]\n\t"
    "ds_read_b64_tr_b16 a[212:213], v29\n\t"
    "ds_read_b64_tr_b16 a[214:215], v29 offset:4096\n\t"
    "v_cvt_pk_bf16_f32 v227, v254, v255\n\t"
    "v_mfma_f32_16x16x32_bf16 v[188:191], a[252:255], v[232:235], v[188:191]\n\t"
    "s_waitcnt lgkmcnt(8)\n\t"
    "v_mfma_f32_16x16x32_bf16 v[64:67], a[192:195], v[220:223], v[64:67]\n\t"
    "ds_read_b64_tr_b16 a[216:217], v30\n\t"
    "ds_read_b64_tr_b16 a[218:219], v30 offset:4096\n\t"
    "v_mfma_f32_16x16x32_bf16 v[68:71], a[192:195], v[224:227], v[68:71]\n\t"
    "ds_read_b128 v[228:231], v55\n\t"
    "ds_read_b128 v[232:235], v55 offset:256\n\t"
    "v_mfma_f32_16x16x32_bf16 v[128:131], a[196:199], v[220:223], v[128:131]\n\t"
    "ds_read_b64_tr_b16 a[220:221], v31\n\t"
    "ds_read_b64_tr_b16 a[222:223], v31 offset:4096\n\t"
    "ds_read_b128 v[236:239], v54 offset:1792\n\t"
    "ds_read_b128 v[240:243], v54 offset:1808\n\t"
    "v_mfma_f32_16x16x32_bf16 v[132:135], a[196:199], v[224:227], v[132:135]\n\t"
    "buffer_load_dwordx4 v43, %[srdwq], s68 offen offset:3072 lds\n\t"
    "s_add_u32 m0, s69, 0x18000\n\t"
    "ds_read_b128 v[244:247], v54 offset:1824\n\t"
    "ds_read_b128 v[248:251], v54 offset:1840\n\t"
    "s_waitcnt lgkmcnt(14)\n\t"
    "v_mfma_f32_16x16x32_bf16 v[72:75], a[200:203], v[220:223], v[72:75]\n\t"
    "ds_read_b64_tr_b16 a[224:225], v24 offset:256\n\t"
    "ds_read_b64_tr_b16 a[226:227], v24 offset:4352\n\t"
    "s_add_u32 s68, s67, 0x200200\n\t"
    "v_mfma_f32_16x16x32_bf16 v[76:79], a[200:203], v[224:227], v[76:79]\n\t"
    "buffer_load_dwordx4 v40, %[srdwq], s68 offen lds\n\t"
    "v_mfma_f32_16x16x32_bf16 v[136:139], a[204:207], v[220:223], v[136:139]\n\t"
    "ds_read_b64_tr_b16 a[228:229], v25 offset:256\n\t"
    "ds_read_b64_tr_b16 a[230:231], v25 offset:4352\n\t"
    "v_mfma_f32_16x16x32_bf16 v[140:143], a[204:207], v[224:227], v[140:143]\n\t"
    "buffer_load_dwordx4 v41, %[srdwq], s68 offen offset:1024 lds\n\t"
    "s_waitcnt lgkmcnt(14)\n\t"
    "v_mfma_f32_16x16x32_bf16 v[80:83], a[208:211], v[220:223], v[80:83]\n\t"
    "ds_read_b64_tr_b16 a[232:233], v26 offset:256\n\t"
    "ds_read_b64_tr_b16 a[234:235], v26 offset:4352\n\t"
    "v_mfma_f32_16x16x32_bf16 v[84:87], a[208:211], v[224:227], v[84:87]\n\t"
    "s_waitcnt lgkmcnt(6)\n\t"
    "v_lshlrev_b32 v252, 16, v228\n\t"
    "v_fma_f32 v253, v228, v237, v239\n\t"
    "v_mfma_f32_16x16x32_bf16 v[144:147], a[212:215], v[220:223], v[144:147]\n\t"
    "ds_read_b64_tr_b16 a[236:237], v27 offset:256\n\t"
    "ds_read_b64_tr_b16 a[238:239], v27 offset:4352\n\t"
    "v_fma_f32 v252, v252, v236, v238\n\t"
    "v_mfma_f32_16x16x32_bf16 v[148:151], a[212:215], v[224:227], v[148:151]\n\t"
    "buffer_load_dwordx4 v42, %[srdwq], s68 offen offset:2048 lds\n\t"
    "v_cvt_pk_bf16_f32 v228, v252, v253\n\t"
    "v_lshlrev_b32 v254, 16, v232\n\t"
    "v_mfma_f32_16x16x32_bf16 v[88:91], a[216:219], v[220:223], v[88:91]\n\t"
    "ds_read_b64_tr_b16 a[240:241], v28 offset:256\n\t"
    "ds_read_b64_tr_b16 a[242:243], v28 offset:4352\n\t"
    "v_fma_f32 v255, v232, v237, v239\n\t"
    "v_mfma_f32_16x16x32_bf16 v[92:95], a[216:219], v[224:227], v[92:95]\n\t"
    "buffer_load_dwordx4 v43, %[srdwq], s68 offen offset:3072 lds\n\t"
    "v_fma_f32 v254, v254, v236, v238\n\t"
    "v_cvt_pk_bf16_f32 v232, v254, v255\n\t"
    "v_mfma_f32_16x16x32_bf16 v[152:155], a[220:223], v[220:223], v[152:155]\n\t"
    "ds_read_b64_tr_b16 a[244:245], v29 offset:256\n\t"
    "ds_read_b64_tr_b16 a[246:247], v29 offset:4352\n\t"
    "v_lshlrev_b32 v252, 16, v229\n\t"
    "v_mfma_f32_16x16x32_bf16 v[156:159], a[220:223], v[224:227], v[156:159]\n\t"
    "v_fma_f32 v253, v229, v241, v243\n\t"
    "v_fma_f32 v252, v252, v240, v242\n\t"
    "s_waitcnt lgkmcnt(8)\n\t"
    "v_mfma_f32_16x16x32_bf16 v[96:99], a[224:227], v[220:223], v[96:99]\n\t"
    "ds_read_b64_tr_b16 a[248:249], v30 offset:256\n\t"
    "ds_read_b64_tr_b16 a[250:251], v30 offset:4352\n\t"
    "v_cvt_pk_bf16_f32 v229, v252, v253\n\t"
    "v_mfma_f32_16x16x32_bf16 v[100:103], a[224:227], v[224:227], v[100:103]\n\t"
    "s_waitcnt vmcnt(9)\n\t"
    "s_barrier\n\t"
    "v_lshlrev_b32 v254, 16, v233\n\t"
    "v_fma_f32 v255, v233, v241, v243\n\t"
    "v_mfma_f32_16x16x32_bf16 v[160:163], a[228:231], v[220:223], v[160:163]\n\t"
    "ds_read_b64_tr_b16 a[252:253], v31 offset:256\n\t"
    "ds_read_b64_tr_b16 a[254:255], v31 offset:4352\n\t"
    "v_fma_f32 v254, v254, v240, v242\n\t"
    "v_mfma_f32_16x16x32_bf16 v[164:167], a[228:231], v[224:227], v[164:167]\n\t"
    "s_add_u32 m0, s75, 0x4000\n\t"
    "s_add_u32 s68, s73, 0x5000\n\t"
    "v_cvt_pk_bf16_f32 v233, v254, v255\n\t"
    "s_waitcnt lgkmcnt(8)\n\t"
    "v_mfma_f32_16x16x32_bf16 v[104:107], a[232:235], v[220:223], v[104:107]\n\t"
    "ds_read_b64_tr_b16 a[192:193], v24 offset:16384\n\t"
    "ds_read_b64_tr_b16 a[194:195], v24 offset:20480\n\t"
    "v_lshlrev_b32 v252, 16, v230\n\t"
    "v_fma_f32 v253, v230, v245, v247\n\t"
    "v_mfma_f32_16x16x32_bf16 v[108:111], a[232:235], v[224:227], v[108:111]\n\t"
    "buffer_load_dwordx4 v52, %[srdxn], s68 offen lds\n\t"
    "s_add_u32 m0, s69, 0xc000\n\t"
    "s_add_u32 s68, s67, 0x240000\n\t"
    "v_fma_f32 v252, v252, v244, v246\n\t"
    "v_mfma_f32_16x16x32_bf16 v[168:171], a[236:239], v[220:223], v[168:171]\n\t"
    "ds_read_b64_tr_b16 a[196:197], v25 offset:16384\n\t"
    "ds_read_b64_tr_b16 a[198:199], v25 offset:20480\n\t"
    "v_cvt_pk_bf16_f32 v230, v252, v253\n\t"
    "v_lshlrev_b32 v254, 16, v234\n\t"
    "v_mfma_f32_16x16x32_bf16 v[172:175], a[236:239], v[224:227], v[172:175]\n\t"
    "buffer_load_dwordx4 v40, %[srdwq], s68 offen lds\n\t"
    "v_fma_f32 v255, v234, v245, v247\n\t"
    "s_waitcnt lgkmcnt(8)\n\t"
    "v_mfma_f32_16x16x32_bf16 v[112:115], a[240:243], v[220:223], v[112:115]\n\t"
    "ds_read_b64_tr_b16 a[200:201], v26 offset:16384\n\t"
    "ds_read_b64_tr_b16 a[202:203], v26 offset:20480\n\t"
    "v_fma_f32 v254, v254, v244, v246\n\t"
    "v_cvt_pk_bf16_f32 v234, v254, v255\n\t"
    "v_mfma_f32_16x16x32_bf16 v[116:119], a[240:243], v[224:227], v[116:119]\n\t"
    "v_lshlrev_b32 v252, 16, v231\n\t"
    "v_mfma_f32_16x16x32_bf16 v[176:179], a[244:247], v[220:223], v[176:179]\n\t"
    "ds_read_b64_tr_b16 a[204:205], v27 offset:16384\n\t"
    "ds_read_b64_tr_b16 a[206:207], v27 offset:20480\n\t"
    "v_fma_f32 v253, v231, v249, v251\n\t"
    "v_fma_f32 v252, v252, v248, v250\n\t"
    "v_mfma_f32_16x16x32_bf16 v[180:183], a[244:247], v[224:227], v[180:183]\n\t"
    "buffer_load_dwordx4 v41, %[srdwq], s68 offen offset:1024 lds\n\t"
    "v_cvt_pk_bf16_f32 v231, v252, v253\n\t"
    "s_waitcnt lgkmcnt(8)\n\t"
    "v_mfma_f32_16x16x32_bf16 v[120:123], a[248:251], v[220:223], v[120:123]\n\t"
    "ds_read_b64_tr_b16 a[208:209], v28 offset:16384\n\t"
    "ds_read_b64_tr_b16 a[210:211], v28 offset:20480\n\t"
    "v_lshlrev_b32 v254, 16, v235\n\t"
    "v_fma_f32 v255, v235, v249, v251\n\t"
    "v_mfma_f32_16x16x32_bf16 v[124:127], a[248:251], v[224:227], v[124:127]\n\t"
    "buffer_load_dwordx4 v42, %[srdwq], s68 offen offset:2048 lds\n\t"
    "v_fma_f32 v254, v254, v248, v250\n\t"
    "v_mfma_f32_16x16x32_bf16 v[184:187], a[252:255], v[220:223], v[184:187]\n\t"
    "ds_read_b64_tr_b16 a[212:213], v29 offset:16384\n\t"
    "ds_read_b64_tr_b16 a[214:215], v29 offset:20480\n\t"
    "v_cvt_pk_bf16_f32 v235, v254, v255\n\t"
    "v_mfma_f32_16x16x32_bf16 v[188:191], a[252:255], v[224:227], v[188:191]\n\t"
    "s_waitcnt lgkmcnt(8)\n\t"
    "v_mfma_f32_16x16x32_bf16 v[64:67], a[192:195], v[228:231], v[64:67]\n\t"
    "ds_read_b64_tr_b16 a[216:217], v30 offset:16384\n\t"
    "ds_read_b64_tr_b16 a[218:219], v30 offset:20480\n\t"
    "v_mfma_f32_16x16x32_bf16 v[68:71], a[192:195], v[232:235], v[68:71]\n\t"
    "ds_read_b128 v[220:223], v55 offset:4096\n\t"
    "ds_read_b128 v[224:227], v55 offset:4352\n\t"
    "v_mfma_f32_16x16x32_bf16 v[128:131], a[196:199], v[228:231], v[128:131]\n\t"
    "ds_read_b64_tr_b16 a[220:221], v31 offset:16384\n\t"
    "ds_read_b64_tr_b16 a[222:223], v31 offset:20480\n\t"
    "ds_read_b128 v[236:239], v54 offset:2048\n\t"
    "ds_read_b128 v[240:243], v54 offset:2064\n\t"
    "v_mfma_f32_16x16x32_bf16 v[132:135], a[196:199], v[232:235], v[132:135]\n\t"
    "buffer_load_dwordx4 v43, %[srdwq], s68 offen offset:3072 lds\n\t"
    "s_add_u32 m0, s69, 0x1c000\n\t"
    "ds_read_b128 v[244:247], v54 offset:2080\n\t"
    "ds_read_b128 v[248:251], v54 offset:2096\n\t"
    "s_waitcnt lgkmcnt(14)\n\t"
    "v_mfma_f32_16x16x32_bf16 v[72:75], a[200:203], v[228:231], v[72:75]\n\t"
    "ds_read_b64_tr_b16 a[224:225], v24 offset:16640\n\t"
    "ds_read_b64_tr_b16 a[226:227], v24 offset:20736\n\t"
    "s_add_u32 s68, s67, 0x240200\n\t"
    "v_mfma_f32_16x16x32_bf16 v[76:79], a[200:203], v[232:235], v[76:79]\n\t"
    "buffer_load_dwordx4 v40, %[srdwq], s68 offen lds\n\t"
    "v_mfma_f32_16x16x32_bf16 v[136:139], a[204:207], v[228:231], v[136:139]\n\t"
    "ds_read_b64_tr_b16 a[228:229], v25 offset:16640\n\t"
    "ds_read_b64_tr_b16 a[230:231], v25 offset:20736\n\t"
    "v_mfma_f32_16x16x32_bf16 v[140:143], a[204:207], v[232:235], v[140:143]\n\t"
    "buffer_load_dwordx4 v41, %[srdwq], s68 offen offset:1024 lds\n\t"
    "s_waitcnt lgkmcnt(14)\n\t"
    "v_mfma_f32_16x16x32_bf16 v[80:83], a[208:211], v[228:231], v[80:83]\n\t"
    "ds_read_b64_tr_b16 a[232:233], v26 offset:16640\n\t"
    "ds_read_b64_tr_b16 a[234:235], v26 offset:20736\n\t"
    "v_mfma_f32_16x16x32_bf16 v[84:87], a[208:211], v[232:235], v[84:87]\n\t"
    "s_waitcnt lgkmcnt(6)\n\t"
    "v_lshlrev_b32 v252, 16, v220\n\t"
    "v_fma_f32 v253, v220, v237, v239\n\t"
    "v_mfma_f32_16x16x32_bf16 v[144:147], a[212:215], v[228:231], v[144:147]\n\t"
    "ds_read_b64_tr_b16 a[236:237], v27 offset:16640\n\t"
    "ds_read_b64_tr_b16 a[238:239], v27 offset:20736\n\t"
    "v_fma_f32 v252, v252, v236, v238\n\t"
    "v_mfma_f32_16x16x32_bf16 v[148:151], a[212:215], v[232:235], v[148:151]\n\t"
    "buffer_load_dwordx4 v42, %[srdwq], s68 offen offset:2048 lds\n\t"
    "v_cvt_pk_bf16_f32 v220, v252, v253\n\t"
    "v_lshlrev_b32 v254, 16, v224\n\t"
    "v_mfma_f32_16x16x32_bf16 v[88:91], a[216:219], v[228:231], v[88:91]\n\t"
    "ds_read_b64_tr_b16 a[240:241], v28 offset:16640\n\t"
    "ds_read_b64_tr_b16 a[242:243], v28 offset:20736\n\t"
    "v_fma_f32 v255, v224, v237, v239\n\t"
    "v_mfma_f32_16x16x32_bf16 v[92:95], a[216:219], v[232:235], v[92:95]\n\t"
    "buffer_load_dwordx4 v43, %[srdwq], s68 offen offset:3072 lds\n\t"
    "v_fma_f32 v254, v254, v236, v238\n\t"
    "v_cvt_pk_bf16_f32 v224, v254, v255\n\t"
    "v_mfma_f32_16x16x32_bf16 v[152:155], a[220:223], v[228:231], v[152:155]\n\t"
    "ds_read_b64_tr_b16 a[244:245], v29 offset:16640\n\t"
    "ds_read_b64_tr_b16 a[246:247], v29 offset:20736\n\t"
    "v_lshlrev_b32 v252, 16, v221\n\t"
    "v_mfma_f32_16x16x32_bf16 v[156:159], a[220:223], v[232:235], v[156:159]\n\t"
    "v_fma_f32 v253, v221, v241, v243\n\t"
    "v_fma_f32 v252, v252, v240, v242\n\t"
    "s_waitcnt lgkmcnt(8)\n\t"
    "v_mfma_f32_16x16x32_bf16 v[96:99], a[224:227], v[228:231], v[96:99]\n\t"
    "ds_read_b64_tr_b16 a[248:249], v30 offset:16640\n\t"
    "ds_read_b64_tr_b16 a[250:251], v30 offset:20736\n\t"
    "v_cvt_pk_bf16_f32 v221, v252, v253\n\t"
    "v_mfma_f32_16x16x32_bf16 v[100:103], a[224:227], v[232:235], v[100:103]\n\t"
    "s_waitcnt vmcnt(9)\n\t"
    "s_barrier\n\t"
    "v_lshlrev_b32 v254, 16, v225\n\t"
    "v_fma_f32 v255, v225, v241, v243\n\t"
    "v_mfma_f32_16x16x32_bf16 v[160:163], a[228:231], v[228:231], v[160:163]\n\t"
    "ds_read_b64_tr_b16 a[252:253], v31 offset:16640\n\t"
    "ds_read_b64_tr_b16 a[254:255], v31 offset:20736\n\t"
    "v_fma_f32 v254, v254, v240, v242\n\t"
    "v_mfma_f32_16x16x32_bf16 v[164:167], a[228:231], v[232:235], v[164:167]\n\t"
    "s_add_u32 m0, s75, 0x5000\n\t"
    "s_add_u32 s68, s73, 0x5800\n\t"
    "v_cvt_pk_bf16_f32 v225, v254, v255\n\t"
    "s_waitcnt lgkmcnt(8)\n\t"
    "v_mfma_f32_16x16x32_bf16 v[104:107], a[232:235], v[228:231], v[104:107]\n\t"
    "ds_read_b64_tr_b16 a[192:193], v24 offset:32768\n\t"
    "ds_read_b64_tr_b16 a[194:195], v24 offset:36864\n\t"
    "v_lshlrev_b32 v252, 16, v222\n\t"
    "v_fma_f32 v253, v222, v245, v247\n\t"
    "v_mfma_f32_16x16x32_bf16 v[108:111], a[232:235], v[232:235], v[108:111]\n\t"
    "buffer_load_dwordx4 v52, %[srdxn], s68 offen lds\n\t"
    "s_add_u32 m0, s69, 0x0\n\t"
    "s_add_u32 s68, s67, 0x280000\n\t"
    "v_fma_f32 v252, v252, v244, v246\n\t"
    "v_mfma_f32_16x16x32_bf16 v[168:171], a[236:239], v[228:231], v[168:171]\n\t"
    "ds_read_b64_tr_b16 a[196:197], v25 offset:32768\n\t"
    "ds_read_b64_tr_b16 a[198:199], v25 offset:36864\n\t"
    "v_cvt_pk_bf16_f32 v222, v252, v253\n\t"
    "v_lshlrev_b32 v254, 16, v226\n\t"
    "v_mfma_f32_16x16x32_bf16 v[172:175], a[236:239], v[232:235], v[172:175]\n\t"
    "buffer_load_dwordx4 v40, %[srdwq], s68 offen lds\n\t"
    "v_fma_f32 v255, v226, v245, v247\n\t"
    "s_waitcnt lgkmcnt(8)\n\t"
    "v_mfma_f32_16x16x32_bf16 v[112:115], a[240:243], v[228:231], v[112:115]\n\t"
    "ds_read_b64_tr_b16 a[200:201], v26 offset:32768\n\t"
    "ds_read_b64_tr_b16 a[202:203], v26 offset:36864\n\t"
    "v_fma_f32 v254, v254, v244, v246\n\t"
    "v_cvt_pk_bf16_f32 v226, v254, v255\n\t"
    "v_mfma_f32_16x16x32_bf16 v[116:119], a[240:243], v[232:235], v[116:119]\n\t"
    "v_lshlrev_b32 v252, 16, v223\n\t"
    "v_mfma_f32_16x16x32_bf16 v[176:179], a[244:247], v[228:231], v[176:179]\n\t"
    "ds_read_b64_tr_b16 a[204:205], v27 offset:32768\n\t"
    "ds_read_b64_tr_b16 a[206:207], v27 offset:36864\n\t"
    "v_fma_f32 v253, v223, v249, v251\n\t"
    "v_fma_f32 v252, v252, v248, v250\n\t"
    "v_mfma_f32_16x16x32_bf16 v[180:183], a[244:247], v[232:235], v[180:183]\n\t"
    "buffer_load_dwordx4 v41, %[srdwq], s68 offen offset:1024 lds\n\t"
    "v_cvt_pk_bf16_f32 v223, v252, v253\n\t"
    "s_waitcnt lgkmcnt(8)\n\t"
    "v_mfma_f32_16x16x32_bf16 v[120:123], a[248:251], v[228:231], v[120:123]\n\t"
    "ds_read_b64_tr_b16 a[208:209], v28 offset:32768\n\t"
    "ds_read_b64_tr_b16 a[210:211], v28 offset:36864\n\t"
    "v_lshlrev_b32 v254, 16, v227\n\t"
    "v_fma_f32 v255, v227, v249, v251\n\t"
    "v_mfma_f32_16x16x32_bf16 v[124:127], a[248:251], v[232:235], v[124:127]\n\t"
    "buffer_load_dwordx4 v42, %[srdwq], s68 offen offset:2048 lds\n\t"
    "v_fma_f32 v254, v254, v248, v250\n\t"
    "v_mfma_f32_16x16x32_bf16 v[184:187], a[252:255], v[228:231], v[184:187]\n\t"
    "ds_read_b64_tr_b16 a[212:213], v29 offset:32768\n\t"
    "ds_read_b64_tr_b16 a[214:215], v29 offset:36864\n\t"
    "v_cvt_pk_bf16_f32 v227, v254, v255\n\t"
    "v_mfma_f32_16x16x32_bf16 v[188:191], a[252:255], v[232:235], v[188:191]\n\t"
    "s_waitcnt lgkmcnt(8)\n\t"
    "v_mfma_f32_16x16x32_bf16 v[64:67], a[192:195], v[220:223], v[64:67]\n\t"
    "ds_read_b64_tr_b16 a[216:217], v30 offset:32768\n\t"
    "ds_read_b64_tr_b16 a[218:219], v30 offset:36864\n\t"
    "v_mfma_f32_16x16x32_bf16 v[68:71], a[192:195], v[224:227], v[68:71]\n\t"
    "ds_read_b128 v[228:231], v55 offset:8192\n\t"
    "ds_read_b128 v[232:235], v55 offset:8448\n\t"
    "v_mfma_f32_16x16x32_bf16 v[128:131], a[196:199], v[220:223], v[128:131]\n\t"
    "ds_read_b64_tr_b16 a[220:221], v31 offset:32768\n\t"
    "ds_read_b64_tr_b16 a[222:223], v31 offset:36864\n\t"
    "ds_read_b128 v[236:239], v54 offset:2304\n\t"
    "ds_read_b128 v[240:243], v54 offset:2320\n\t"
    "v_mfma_f32_16x16x32_bf16 v[132:135], a[196:199], v[224:227], v[132:135]\n\t"
    "buffer_load_dwordx4 v43, %[srdwq], s68 offen offset:3072 lds\n\t"
    "s_add_u32 m0, s69, 0x10000\n\t"
    "ds_read_b128 v[244:247], v54 offset:2336\n\t"
    "ds_read_b128 v[248:251], v54 offset:2352\n\t"
    "s_waitcnt lgkmcnt(14)\n\t"
    "v_mfma_f32_16x16x32_bf16 v[72:75], a[200:203], v[220:223], v[72:75]\n\t"
    "ds_read_b64_tr_b16 a[224:225], v24 offset:33024\n\t"
    "ds_read_b64_tr_b16 a[226:227], v24 offset:37120\n\t"
    "s_add_u32 s68, s67, 0x280200\n\t"
    "v_mfma_f32_16x16x32_bf16 v[76:79], a[200:203], v[224:227], v[76:79]\n\t"
    "buffer_load_dwordx4 v40, %[srdwq], s68 offen lds\n\t"
    "v_mfma_f32_16x16x32_bf16 v[136:139], a[204:207], v[220:223], v[136:139]\n\t"
    "ds_read_b64_tr_b16 a[228:229], v25 offset:33024\n\t"
    "ds_read_b64_tr_b16 a[230:231], v25 offset:37120\n\t"
    "v_mfma_f32_16x16x32_bf16 v[140:143], a[204:207], v[224:227], v[140:143]\n\t"
    "buffer_load_dwordx4 v41, %[srdwq], s68 offen offset:1024 lds\n\t"
    "s_waitcnt lgkmcnt(14)\n\t"
    "v_mfma_f32_16x16x32_bf16 v[80:83], a[208:211], v[220:223], v[80:83]\n\t"
    "ds_read_b64_tr_b16 a[232:233], v26 offset:33024\n\t"
    "ds_read_b64_tr_b16 a[234:235], v26 offset:37120\n\t"
    "v_mfma_f32_16x16x32_bf16 v[84:87], a[208:211], v[224:227], v[84:87]\n\t"
    "s_waitcnt lgkmcnt(6)\n\t"
    "v_lshlrev_b32 v252, 16, v228\n\t"
    "v_fma_f32 v253, v228, v237, v239\n\t"
    "v_mfma_f32_16x16x32_bf16 v[144:147], a[212:215], v[220:223], v[144:147]\n\t"
    "ds_read_b64_tr_b16 a[236:237], v27 offset:33024\n\t"
    "ds_read_b64_tr_b16 a[238:239], v27 offset:37120\n\t"
    "v_fma_f32 v252, v252, v236, v238\n\t"
    "v_mfma_f32_16x16x32_bf16 v[148:151], a[212:215], v[224:227], v[148:151]\n\t"
    "buffer_load_dwordx4 v42, %[srdwq], s68 offen offset:2048 lds\n\t"
    "v_cvt_pk_bf16_f32 v228, v252, v253\n\t"
    "v_lshlrev_b32 v254, 16, v232\n\t"
    "v_mfma_f32_16x16x32_bf16 v[88:91], a[216:219], v[220:223], v[88:91]\n\t"
    "ds_read_b64_tr_b16 a[240:241], v28 offset:33024\n\t"
    "ds_read_b64_tr_b16 a[242:243], v28 offset:37120\n\t"
    "v_fma_f32 v255, v232, v237, v239\n\t"
    "v_mfma_f32_16x16x32_bf16 v[92:95], a[216:219], v[224:227], v[92:95]\n\t"
    "buffer_load_dwordx4 v43, %[srdwq], s68 offen offset:3072 lds\n\t"
    "v_fma_f32 v254, v254, v236, v238\n\t"
    "v_cvt_pk_bf16_f32 v232, v254, v255\n\t"
    "v_mfma_f32_16x16x32_bf16 v[152:155], a[220:223], v[220:223], v[152:155]\n\t"
    "ds_read_b64_tr_b16 a[244:245], v29 offset:33024\n\t"
    "ds_read_b64_tr_b16 a[246:247], v29 offset:37120\n\t"
    "v_lshlrev_b32 v252, 16, v229\n\t"
    "v_mfma_f32_16x16x32_bf16 v[156:159], a[220:223], v[224:227], v[156:159]\n\t"
    "v_fma_f32 v253, v229, v241, v243\n\t"
    "v_fma_f32 v252, v252, v240, v242\n\t"
    "s_waitcnt lgkmcnt(8)\n\t"
    "v_mfma_f32_16x16x32_bf16 v[96:99], a[224:227], v[220:223], v[96:99]\n\t"
    "ds_read_b64_tr_b16 a[248:249], v30 offset:33024\n\t"
    "ds_read_b64_tr_b16 a[250:251], v30 offset:37120\n\t"
    "v_cvt_pk_bf16_f32 v229, v252, v253\n\t"
    "v_mfma_f32_16x16x32_bf16 v[100:103], a[224:227], v[224:227], v[100:103]\n\t"
    "s_waitcnt vmcnt(9)\n\t"
    "s_barrier\n\t"
    "v_lshlrev_b32 v254, 16, v233\n\t"
    "v_fma_f32 v255, v233, v241, v243\n\t"
    "v_mfma_f32_16x16x32_bf16 v[160:163], a[228:231], v[220:223], v[160:163]\n\t"
    "ds_read_b64_tr_b16 a[252:253], v31 offset:33024\n\t"
    "ds_read_b64_tr_b16 a[254:255], v31 offset:37120\n\t"
    "v_fma_f32 v254, v254, v240, v242\n\t"
    "v_mfma_f32_16x16x32_bf16 v[164:167], a[228:231], v[224:227], v[164:167]\n\t"
    "s_add_u32 m0, s75, 0x6000\n\t"
    "s_add_u32 s68, s73, 0x6000\n\t"
    "v_cvt_pk_bf16_f32 v233, v254, v255\n\t"
    "s_waitcnt lgkmcnt(8)\n\t"
    "v_mfma_f32_16x16x32_bf16 v[104:107], a[232:235], v[220:223], v[104:107]\n\t"
    "ds_read_b64_tr_b16 a[192:193], v24 offset:49152\n\t"
    "ds_read_b64_tr_b16 a[194:195], v24 offset:53248\n\t"
    "v_lshlrev_b32 v252, 16, v230\n\t"
    "v_fma_f32 v253, v230, v245, v247\n\t"
    "v_mfma_f32_16x16x32_bf16 v[108:111], a[232:235], v[224:227], v[108:111]\n\t"
    "buffer_load_dwordx4 v52, %[srdxn], s68 offen lds\n\t"
    "s_add_u32 m0, s69, 0x4000\n\t"
    "s_add_u32 s68, s67, 0x2c0000\n\t"
    "v_fma_f32 v252, v252, v244, v246\n\t"
    "v_mfma_f32_16x16x32_bf16 v[168:171], a[236:239], v[220:223], v[168:171]\n\t"
    "ds_read_b64_tr_b16 a[196:197], v25 offset:49152\n\t"
    "ds_read_b64_tr_b16 a[198:199], v25 offset:53248\n\t"
    "v_cvt_pk_bf16_f32 v230, v252, v253\n\t"
    "v_lshlrev_b32 v254, 16, v234\n\t"
    "v_mfma_f32_16x16x32_bf16 v[172:175], a[236:239], v[224:227], v[172:175]\n\t"
    "buffer_load_dwordx4 v40, %[srdwq], s68 offen lds\n\t"
    "v_fma_f32 v255, v234, v245, v247\n\t"
    "s_waitcnt lgkmcnt(8)\n\t"
    "v_mfma_f32_16x16x32_bf16 v[112:115], a[240:243], v[220:223], v[112:115]\n\t"
    "ds_read_b64_tr_b16 a[200:201], v26 offset:49152\n\t"
    "ds_read_b64_tr_b16 a[202:203], v26 offset:53248\n\t"
    "v_fma_f32 v254, v254, v244, v246\n\t"
    "v_cvt_pk_bf16_f32 v234, v254, v255\n\t"
    "v_mfma_f32_16x16x32_bf16 v[116:119], a[240:243], v[224:227], v[116:119]\n\t"
    "v_lshlrev_b32 v252, 16, v231\n\t"
    "v_mfma_f32_16x16x32_bf16 v[176:179], a[244:247], v[220:223], v[176:179]\n\t"
    "ds_read_b64_tr_b16 a[204:205], v27 offset:49152\n\t"
    "ds_read_b64_tr_b16 a[206:207], v27 offset:53248\n\t"
    "v_fma_f32 v253, v231, v249, v251\n\t"
    "v_fma_f32 v252, v252, v248, v250\n\t"
    "v_mfma_f32_16x16x32_bf16 v[180:183], a[244:247], v[224:227], v[180:183]\n\t"
    "buffer_load_dwordx4 v41, %[srdwq], s68 offen offset:1024 lds\n\t"
    "v_cvt_pk_bf16_f32 v231, v252, v253\n\t"
    "s_waitcnt lgkmcnt(8)\n\t"
    "v_mfma_f32_16x16x32_bf16 v[120:123], a[248:251], v[220:223], v[120:123]\n\t"
    "ds_read_b64_tr_b16 a[208:209], v28 offset:49152\n\t"
    "ds_read_b64_tr_b16 a[210:211], v28 offset:53248\n\t"
    "v_lshlrev_b32 v254, 16, v235\n\t"
    "v_fma_f32 v255, v235, v249, v251\n\t"
    "v_mfma_f32_16x16x32_bf16 v[124:127], a[248:251], v[224:227], v[124:127]\n\t"
    "buffer_load_dwordx4 v42, %[srdwq], s68 offen offset:2048 lds\n\t"
    "v_fma_f32 v254, v254, v248, v250\n\t"
    "v_mfma_f32_16x16x32_bf16 v[184:187], a[252:255], v[220:223], v[184:187]\n\t"
    "ds_read_b64_tr_b16 a[212:213], v29 offset:49152\n\t"
    "ds_read_b64_tr_b16 a[214:215], v29 offset:53248\n\t"
    "v_cvt_pk_bf16_f32 v235, v254, v255\n\t"
    "v_mfma_f32_16x16x32_bf16 v[188:191], a[252:255], v[224:227], v[188:191]\n\t"
    "s_waitcnt lgkmcnt(8)\n\t"
    "v_mfma_f32_16x16x32_bf16 v[64:67], a[192:195], v[228:231], v[64:67]\n\t"
    "ds_read_b64_tr_b16 a[216:217], v30 offset:49152\n\t"
    "ds_read_b64_tr_b16 a[218:219], v30 offset:53248\n\t"
    "v_mfma_f32_16x16x32_bf16 v[68:71], a[192:195], v[232:235], v[68:71]\n\t"
    "ds_read_b128 v[220:223], v55 offset:12288\n\t"
    "ds_read_b128 v[224:227], v55 offset:12544\n\t"
    "v_mfma_f32_16x16x32_bf16 v[128:131], a[196:199], v[228:231], v[128:131]\n\t"
    "ds_read_b64_tr_b16 a[220:221], v31 offset:49152\n\t"
    "ds_read_b64_tr_b16 a[222:223], v31 offset:53248\n\t"
    "ds_read_b128 v[236:239], v54 offset:2560\n\t"
    "ds_read_b128 v[240:243], v54 offset:2576\n\t"
    "v_mfma_f32_16x16x32_bf16 v[132:135], a[196:199], v[232:235], v[132:135]\n\t"
    "buffer_load_dwordx4 v43, %[srdwq], s68 offen offset:3072 lds\n\t"
    "s_add_u32 m0, s69, 0x14000\n\t"
    "ds_read_b128 v[244:247], v54 offset:2592\n\t"
    "ds_read_b128 v[248:251], v54 offset:2608\n\t"
    "s_waitcnt lgkmcnt(14)\n\t"
    "v_mfma_f32_16x16x32_bf16 v[72:75], a[200:203], v[228:231], v[72:75]\n\t"
    "ds_read_b64_tr_b16 a[224:225], v24 offset:49408\n\t"
    "ds_read_b64_tr_b16 a[226:227], v24 offset:53504\n\t"
    "s_add_u32 s68, s67, 0x2c0200\n\t"
    "v_mfma_f32_16x16x32_bf16 v[76:79], a[200:203], v[232:235], v[76:79]\n\t"
    "buffer_load_dwordx4 v40, %[srdwq], s68 offen lds\n\t"
    "v_mfma_f32_16x16x32_bf16 v[136:139], a[204:207], v[228:231], v[136:139]\n\t"
    "ds_read_b64_tr_b16 a[228:229], v25 offset:49408\n\t"
    "ds_read_b64_tr_b16 a[230:231], v25 offset:53504\n\t"
    "v_mfma_f32_16x16x32_bf16 v[140:143], a[204:207], v[232:235], v[140:143]\n\t"
    "buffer_load_dwordx4 v41, %[srdwq], s68 offen offset:1024 lds\n\t"
    "s_waitcnt lgkmcnt(14)\n\t"
    "v_mfma_f32_16x16x32_bf16 v[80:83], a[208:211], v[228:231], v[80:83]\n\t"
    "ds_read_b64_tr_b16 a[232:233], v26 offset:49408\n\t"
    "ds_read_b64_tr_b16 a[234:235], v26 offset:53504\n\t"
    "v_mfma_f32_16x16x32_bf16 v[84:87], a[208:211], v[232:235], v[84:87]\n\t"
    "s_waitcnt lgkmcnt(6)\n\t"
    "v_lshlrev_b32 v252, 16, v220\n\t"
    "v_fma_f32 v253, v220, v237, v239\n\t"
    "v_mfma_f32_16x16x32_bf16 v[144:147], a[212:215], v[228:231], v[144:147]\n\t"
    "ds_read_b64_tr_b16 a[236:237], v27 offset:49408\n\t"
    "ds_read_b64_tr_b16 a[238:239], v27 offset:53504\n\t"
    "v_fma_f32 v252, v252, v236, v238\n\t"
    "v_mfma_f32_16x16x32_bf16 v[148:151], a[212:215], v[232:235], v[148:151]\n\t"
    "buffer_load_dwordx4 v42, %[srdwq], s68 offen offset:2048 lds\n\t"
    "v_cvt_pk_bf16_f32 v220, v252, v253\n\t"
    "v_lshlrev_b32 v254, 16, v224\n\t"
    "v_mfma_f32_16x16x32_bf16 v[88:91], a[216:219], v[228:231], v[88:91]\n\t"
    "ds_read_b64_tr_b16 a[240:241], v28 offset:49408\n\t"
    "ds_read_b64_tr_b16 a[242:243], v28 offset:53504\n\t"
    "v_fma_f32 v255, v224, v237, v239\n\t"
    "v_mfma_f32_16x16x32_bf16 v[92:95], a[216:219], v[232:235], v[92:95]\n\t"
    "buffer_load_dwordx4 v43, %[srdwq], s68 offen offset:3072 lds\n\t"
    "v_fma_f32 v254, v254, v236, v238\n\t"
    "v_cvt_pk_bf16_f32 v224, v254, v255\n\t"
    "v_mfma_f32_16x16x32_bf16 v[152:155], a[220:223], v[228:231], v[152:155]\n\t"
    "ds_read_b64_tr_b16 a[244:245], v29 offset:49408\n\t"
    "ds_read_b64_tr_b16 a[246:247], v29 offset:53504\n\t"
    "v_lshlrev_b32 v252, 16, v221\n\t"
    "v_mfma_f32_16x16x32_bf16 v[156:159], a[220:223], v[232:235], v[156:159]\n\t"
    "v_fma_f32 v253, v221, v241, v243\n\t"
    "v_fma_f32 v252, v252, v240, v242\n\t"
    "s_waitcnt lgkmcnt(8)\n\t"
    "v_mfma_f32_16x16x32_bf16 v[96:99], a[224:227], v[228:231], v[96:99]\n\t"
    "ds_read_b64_tr_b16 a[248:249], v30 offset:49408\n\t"
    "ds_read_b64_tr_b16 a[250:251], v30 offset:53504\n\t"
    "v_cvt_pk_bf16_f32 v221, v252, v253\n\t"
    "v_mfma_f32_16x16x32_bf16 v[100:103], a[224:227], v[232:235], v[100:103]\n\t"
    "s_waitcnt vmcnt(9)\n\t"
    "s_barrier\n\t"
    "v_lshlrev_b32 v254, 16, v225\n\t"
    "v_fma_f32 v255, v225, v241, v243\n\t"
    "v_mfma_f32_16x16x32_bf16 v[160:163], a[228:231], v[228:231], v[160:163]\n\t"
    "ds_read_b64_tr_b16 a[252:253], v31 offset:49408\n\t"
    "ds_read_b64_tr_b16 a[254:255], v31 offset:53504\n\t"
    "v_fma_f32 v254, v254, v240, v242\n\t"
    "v_mfma_f32_16x16x32_bf16 v[164:167], a[228:231], v[232:235], v[164:167]\n\t"
    "s_add_u32 m0, s75, 0x7000\n\t"
    "s_add_u32 s68, s73, 0x6800\n\t"
    "v_cvt_pk_bf16_f32 v225, v254, v255\n\t"
    "s_waitcnt lgkmcnt(8)\n\t"
    "v_mfma_f32_16x16x32_bf16 v[104:107], a[232:235], v[228:231], v[104:107]\n\t"
    "ds_read_b64_tr_b16 a[192:193], v24\n\t"
    "ds_read_b64_tr_b16 a[194:195], v24 offset:4096\n\t"
    "v_lshlrev_b32 v252, 16, v222\n\t"
    "v_fma_f32 v253, v222, v245, v247\n\t"
    "v_mfma_f32_16x16x32_bf16 v[108:111], a[232:235], v[232:235], v[108:111]\n\t"
    "buffer_load_dwordx4 v52, %[srdxn], s68 offen lds\n\t"
    "s_add_u32 m0, s69, 0x8000\n\t"
    "s_add_u32 s68, s67, 0x300000\n\t"
    "v_fma_f32 v252, v252, v244, v246\n\t"
    "v_mfma_f32_16x16x32_bf16 v[168:171], a[236:239], v[228:231], v[168:171]\n\t"
    "ds_read_b64_tr_b16 a[196:197], v25\n\t"
    "ds_read_b64_tr_b16 a[198:199], v25 offset:4096\n\t"
    "v_cvt_pk_bf16_f32 v222, v252, v253\n\t"
    "v_lshlrev_b32 v254, 16, v226\n\t"
    "v_mfma_f32_16x16x32_bf16 v[172:175], a[236:239], v[232:235], v[172:175]\n\t"
    "buffer_load_dwordx4 v40, %[srdwq], s68 offen lds\n\t"
    "v_fma_f32 v255, v226, v245, v247\n\t"
    "s_waitcnt lgkmcnt(8)\n\t"
    "v_mfma_f32_16x16x32_bf16 v[112:115], a[240:243], v[228:231], v[112:115]\n\t"
    "ds_read_b64_tr_b16 a[200:201], v26\n\t"
    "ds_read_b64_tr_b16 a[202:203], v26 offset:4096\n\t"
    "v_fma_f32 v254, v254, v244, v246\n\t"
    "v_cvt_pk_bf16_f32 v226, v254, v255\n\t"
    "v_mfma_f32_16x16x32_bf16 v[116:119], a[240:243], v[232:235], v[116:119]\n\t"
    "v_lshlrev_b32 v252, 16, v223\n\t"
    "v_mfma_f32_16x16x32_bf16 v[176:179], a[244:247], v[228:231], v[176:179]\n\t"
    "ds_read_b64_tr_b16 a[204:205], v27\n\t"
    "ds_read_b64_tr_b16 a[206:207], v27 offset:4096\n\t"
    "v_fma_f32 v253, v223, v249, v251\n\t"
    "v_fma_f32 v252, v252, v248, v250\n\t"
    "v_mfma_f32_16x16x32_bf16 v[180:183], a[244:247], v[232:235], v[180:183]\n\t"
    "buffer_load_dwordx4 v41, %[srdwq], s68 offen offset:1024 lds\n\t"
    "v_cvt_pk_bf16_f32 v223, v252, v253\n\t"
    "s_waitcnt lgkmcnt(8)\n\t"
    "v_mfma_f32_16x16x32_bf16 v[120:123], a[248:251], v[228:231], v[120:123]\n\t"
    "ds_read_b64_tr_b16 a[208:209], v28\n\t"
    "ds_read_b64_tr_b16 a[210:211], v28 offset:4096\n\t"
    "v_lshlrev_b32 v254, 16, v227\n\t"
    "v_fma_f32 v255, v227, v249, v251\n\t"
    "v_mfma_f32_16x16x32_bf16 v[124:127], a[248:251], v[232:235], v[124:127]\n\t"
    "buffer_load_dwordx4 v42, %[srdwq], s68 offen offset:2048 lds\n\t"
    "v_fma_f32 v254, v254, v248, v250\n\t"
    "v_mfma_f32_16x16x32_bf16 v[184:187], a[252:255], v[228:231], v[184:187]\n\t"
    "ds_read_b64_tr_b16 a[212:213], v29\n\t"
    "ds_read_b64_tr_b16 a[214:215], v29 offset:4096\n\t"
    "v_cvt_pk_bf16_f32 v227, v254, v255\n\t"
    "v_mfma_f32_16x16x32_bf16 v[188:191], a[252:255], v[232:235], v[188:191]\n\t"
    "s_waitcnt lgkmcnt(8)\n\t"
    "v_mfma_f32_16x16x32_bf16 v[64:67], a[192:195], v[220:223], v[64:67]\n\t"
    "ds_read_b64_tr_b16 a[216:217], v30\n\t"
    "ds_read_b64_tr_b16 a[218:219], v30 offset:4096\n\t"
    "v_mfma_f32_16x16x32_bf16 v[68:71], a[192:195], v[224:227], v[68:71]\n\t"
    "ds_read_b128 v[228:231], v55 offset:16384\n\t"
    "ds_read_b128 v[232:235], v55 offset:16640\n\t"
    "v_mfma_f32_16x16x32_bf16 v[128:131], a[196:199], v[220:223], v[128:131]\n\t"
    "ds_read_b64_tr_b16 a[220:221], v31\n\t"
    "ds_read_b64_tr_b16 a[222:223], v31 offset:4096\n\t"
    "ds_read_b128 v[236:239], v54 offset:2816\n\t"
    "ds_read_b128 v[240:243], v54 offset:2832\n\t"
    "v_mfma_f32_16x16x32_bf16 v[132:135], a[196:199], v[224:227], v[132:135]\n\t"
    "buffer_load_dwordx4 v43, %[srdwq], s68 offen offset:3072 lds\n\t"
    "s_add_u32 m0, s69, 0x18000\n\t"
    "ds_read_b128 v[244:247], v54 offset:2848\n\t"
    "ds_read_b128 v[248:251], v54 offset:2864\n\t"
    "s_waitcnt lgkmcnt(14)\n\t"
    "v_mfma_f32_16x16x32_bf16 v[72:75], a[200:203], v[220:223], v[72:75]\n\t"
    "ds_read_b64_tr_b16 a[224:225], v24 offset:256\n\t"
    "ds_read_b64_tr_b16 a[226:227], v24 offset:4352\n\t"
    "s_add_u32 s68, s67, 0x300200\n\t"
    "v_mfma_f32_16x16x32_bf16 v[76:79], a[200:203], v[224:227], v[76:79]\n\t"
    "buffer_load_dwordx4 v40, %[srdwq], s68 offen lds\n\t"
    "v_mfma_f32_16x16x32_bf16 v[136:139], a[204:207], v[220:223], v[136:139]\n\t"
    "ds_read_b64_tr_b16 a[228:229], v25 offset:256\n\t"
    "ds_read_b64_tr_b16 a[230:231], v25 offset:4352\n\t"
    "v_mfma_f32_16x16x32_bf16 v[140:143], a[204:207], v[224:227], v[140:143]\n\t"
    "buffer_load_dwordx4 v41, %[srdwq], s68 offen offset:1024 lds\n\t"
    "s_waitcnt lgkmcnt(14)\n\t"
    "v_mfma_f32_16x16x32_bf16 v[80:83], a[208:211], v[220:223], v[80:83]\n\t"
    "ds_read_b64_tr_b16 a[232:233], v26 offset:256\n\t"
    "ds_read_b64_tr_b16 a[234:235], v26 offset:4352\n\t"
    "v_mfma_f32_16x16x32_bf16 v[84:87], a[208:211], v[224:227], v[84:87]\n\t"
    "s_waitcnt lgkmcnt(6)\n\t"
    "v_lshlrev_b32 v252, 16, v228\n\t"
    "v_fma_f32 v253, v228, v237, v239\n\t"
    "v_mfma_f32_16x16x32_bf16 v[144:147], a[212:215], v[220:223], v[144:147]\n\t"
    "ds_read_b64_tr_b16 a[236:237], v27 offset:256\n\t"
    "ds_read_b64_tr_b16 a[238:239], v27 offset:4352\n\t"
    "v_fma_f32 v252, v252, v236, v238\n\t"
    "v_mfma_f32_16x16x32_bf16 v[148:151], a[212:215], v[224:227], v[148:151]\n\t"
    "buffer_load_dwordx4 v42, %[srdwq], s68 offen offset:2048 lds\n\t"
    "v_cvt_pk_bf16_f32 v228, v252, v253\n\t"
    "v_lshlrev_b32 v254, 16, v232\n\t"
    "v_mfma_f32_16x16x32_bf16 v[88:91], a[216:219], v[220:223], v[88:91]\n\t"
    "ds_read_b64_tr_b16 a[240:241], v28 offset:256\n\t"
    "ds_read_b64_tr_b16 a[242:243], v28 offset:4352\n\t"
    "v_fma_f32 v255, v232, v237, v239\n\t"
    "v_mfma_f32_16x16x32_bf16 v[92:95], a[216:219], v[224:227], v[92:95]\n\t"
    "buffer_load_dwordx4 v43, %[srdwq], s68 offen offset:3072 lds\n\t"
    "v_fma_f32 v254, v254, v236, v238\n\t"
    "v_cvt_pk_bf16_f32 v232, v254, v255\n\t"
    "v_mfma_f32_16x16x32_bf16 v[152:155], a[220:223], v[220:223], v[152:155]\n\t"
    "ds_read_b64_tr_b16 a[244:245], v29 offset:256\n\t"
    "ds_read_b64_tr_b16 a[246:247], v29 offset:4352\n\t"
    "v_lshlrev_b32 v252, 16, v229\n\t"
    "v_mfma_f32_16x16x32_bf16 v[156:159], a[220:223], v[224:227], v[156:159]\n\t"
    "v_fma_f32 v253, v229, v241, v243\n\t"
    "v_fma_f32 v252, v252, v240, v242\n\t"
    "s_waitcnt lgkmcnt(8)\n\t"
    "v_mfma_f32_16x16x32_bf16 v[96:99], a[224:227], v[220:223], v[96:99]\n\t"
    "ds_read_b64_tr_b16 a[248:249], v30 offset:256\n\t"
    "ds_read_b64_tr_b16 a[250:251], v30 offset:4352\n\t"
    "v_cvt_pk_bf16_f32 v229, v252, v253\n\t"
    "v_mfma_f32_16x16x32_bf16 v[100:103], a[224:227], v[224:227], v[100:103]\n\t"
    "s_waitcnt vmcnt(9)\n\t"
    "s_barrier\n\t"
    "v_lshlrev_b32 v254, 16, v233\n\t"
    "v_fma_f32 v255, v233, v241, v243\n\t"
    "v_mfma_f32_16x16x32_bf16 v[160:163], a[228:231], v[220:223], v[160:163]\n\t"
    "ds_read_b64_tr_b16 a[252:253], v31 offset:256\n\t"
    "ds_read_b64_tr_b16 a[254:255], v31 offset:4352\n\t"
    "v_fma_f32 v254, v254, v240, v242\n\t"
    "v_mfma_f32_16x16x32_bf16 v[164:167], a[228:231], v[224:227], v[164:167]\n\t"
    "s_add_u32 m0, s69, 0xc000\n\t"
    "s_add_u32 s68, s67, 0x340000\n\t"
    "v_cvt_pk_bf16_f32 v233, v254, v255\n\t"
    "s_waitcnt lgkmcnt(8)\n\t"
    "v_mfma_f32_16x16x32_bf16 v[104:107], a[232:235], v[220:223], v[104:107]\n\t"
    "ds_read_b64_tr_b16 a[192:193], v24 offset:16384\n\t"
    "ds_read_b64_tr_b16 a[194:195], v24 offset:20480\n\t"
    "v_lshlrev_b32 v252, 16, v230\n\t"
    "v_fma_f32 v253, v230, v245, v247\n\t"
    "v_mfma_f32_16x16x32_bf16 v[108:111], a[232:235], v[224:227], v[108:111]\n\t"
    "buffer_load_dwordx4 v40, %[srdwq], s68 offen lds\n\t"
    "v_fma_f32 v252, v252, v244, v246\n\t"
    "v_mfma_f32_16x16x32_bf16 v[168:171], a[236:239], v[220:223], v[168:171]\n\t"
    "ds_read_b64_tr_b16 a[196:197], v25 offset:16384\n\t"
    "ds_read_b64_tr_b16 a[198:199], v25 offset:20480\n\t"
    "v_cvt_pk_bf16_f32 v230, v252, v253\n\t"
    "v_lshlrev_b32 v254, 16, v234\n\t"
    "v_mfma_f32_16x16x32_bf16 v[172:175], a[236:239], v[224:227], v[172:175]\n\t"
    "buffer_load_dwordx4 v41, %[srdwq], s68 offen offset:1024 lds\n\t"
    "v_fma_f32 v255, v234, v245, v247\n\t"
    "s_waitcnt lgkmcnt(8)\n\t"
    "v_mfma_f32_16x16x32_bf16 v[112:115], a[240:243], v[220:223], v[112:115]\n\t"
    "ds_read_b64_tr_b16 a[200:201], v26 offset:16384\n\t"
    "ds_read_b64_tr_b16 a[202:203], v26 offset:20480\n\t"
    "v_fma_f32 v254, v254, v244, v246\n\t"
    "v_cvt_pk_bf16_f32 v234, v254, v255\n\t"
    "v_mfma_f32_16x16x32_bf16 v[116:119], a[240:243], v[224:227], v[116:119]\n\t"
    "v_lshlrev_b32 v252, 16, v231\n\t"
    "v_mfma_f32_16x16x32_bf16 v[176:179], a[244:247], v[220:223], v[176:179]\n\t"
    "ds_read_b64_tr_b16 a[204:205], v27 offset:16384\n\t"
    "ds_read_b64_tr_b16 a[206:207], v27 offset:20480\n\t"
    "v_fma_f32 v253, v231, v249, v251\n\t"
    "v_fma_f32 v252, v252, v248, v250\n\t"
    "v_mfma_f32_16x16x32_bf16 v[180:183], a[244:247], v[224:227], v[180:183]\n\t"
    "buffer_load_dwordx4 v42, %[srdwq], s68 offen offset:2048 lds\n\t"
    "v_cvt_pk_bf16_f32 v231, v252, v253\n\t"
    "s_waitcnt lgkmcnt(8)\n\t"
    "v_mfma_f32_16x16x32_bf16 v[120:123], a[248:251], v[220:223], v[120:123]\n\t"
    "ds_read_b64_tr_b16 a[208:209], v28 offset:16384\n\t"
    "ds_read_b64_tr_b16 a[210:211], v28 offset:20480\n\t"
    "v_lshlrev_b32 v254, 16, v235\n\t"
    "v_fma_f32 v255, v235, v249, v251\n\t"
    "v_mfma_f32_16x16x32_bf16 v[124:127], a[248:251], v[224:227], v[124:127]\n\t"
    "buffer_load_dwordx4 v43, %[srdwq], s68 offen offset:3072 lds\n\t"
    "v_fma_f32 v254, v254, v248, v250\n\t"
    "v_mfma_f32_16x16x32_bf16 v[184:187], a[252:255], v[220:223], v[184:187]\n\t"
    "ds_read_b64_tr_b16 a[212:213], v29 offset:16384\n\t"
    "ds_read_b64_tr_b16 a[214:215], v29 offset:20480\n\t"
    "v_cvt_pk_bf16_f32 v235, v254, v255\n\t"
    "v_mfma_f32_16x16x32_bf16 v[188:191], a[252:255], v[224:227], v[188:191]\n\t"
    "s_waitcnt lgkmcnt(8)\n\t"
    "v_mfma_f32_16x16x32_bf16 v[64:67], a[192:195], v[228:231], v[64:67]\n\t"
    "ds_read_b64_tr_b16 a[216:217], v30 offset:16384\n\t"
    "ds_read_b64_tr_b16 a[218:219], v30 offset:20480\n\t"
    "v_mfma_f32_16x16x32_bf16 v[68:71], a[192:195], v[232:235], v[68:71]\n\t"
    "s_add_u32 m0, s69, 0x1c000\n\t"
    "s_add_u32 s68, s67, 0x340200\n\t"
    "ds_read_b128 v[220:223], v55 offset:20480\n\t"
    "ds_read_b128 v[224:227], v55 offset:20736\n\t"
    "v_mfma_f32_16x16x32_bf16 v[128:131], a[196:199], v[228:231], v[128:131]\n\t"
    "ds_read_b64_tr_b16 a[220:221], v31 offset:16384\n\t"
    "ds_read_b64_tr_b16 a[222:223], v31 offset:20480\n\t"
    "ds_read_b128 v[236:239], v54 offset:3072\n\t"
    "ds_read_b128 v[240:243], v54 offset:3088\n\t"
    "v_mfma_f32_16x16x32_bf16 v[132:135], a[196:199], v[232:235], v[132:135]\n\t"
    "buffer_load_dwordx4 v40, %[srdwq], s68 offen lds\n\t"
    "ds_read_b128 v[244:247], v54 offset:3104\n\t"
    "ds_read_b128 v[248:251], v54 offset:3120\n\t"
    "s_waitcnt lgkmcnt(14)\n\t"
    "v_mfma_f32_16x16x32_bf16 v[72:75], a[200:203], v[228:231], v[72:75]\n\t"
    "ds_read_b64_tr_b16 a[224:225], v24 offset:16640\n\t"
    "ds_read_b64_tr_b16 a[226:227], v24 offset:20736\n\t"
    "v_mfma_f32_16x16x32_bf16 v[76:79], a[200:203], v[232:235], v[76:79]\n\t"
    "buffer_load_dwordx4 v41, %[srdwq], s68 offen offset:1024 lds\n\t"
    "v_mfma_f32_16x16x32_bf16 v[136:139], a[204:207], v[228:231], v[136:139]\n\t"
    "ds_read_b64_tr_b16 a[228:229], v25 offset:16640\n\t"
    "ds_read_b64_tr_b16 a[230:231], v25 offset:20736\n\t"
    "v_mfma_f32_16x16x32_bf16 v[140:143], a[204:207], v[232:235], v[140:143]\n\t"
    "s_waitcnt lgkmcnt(14)\n\t"
    "v_mfma_f32_16x16x32_bf16 v[80:83], a[208:211], v[228:231], v[80:83]\n\t"
    "ds_read_b64_tr_b16 a[232:233], v26 offset:16640\n\t"
    "ds_read_b64_tr_b16 a[234:235], v26 offset:20736\n\t"
    "v_mfma_f32_16x16x32_bf16 v[84:87], a[208:211], v[232:235], v[84:87]\n\t"
    "buffer_load_dwordx4 v42, %[srdwq], s68 offen offset:2048 lds\n\t"
    "s_waitcnt lgkmcnt(6)\n\t"
    "v_lshlrev_b32 v252, 16, v220\n\t"
    "v_fma_f32 v253, v220, v237, v239\n\t"
    "v_mfma_f32_16x16x32_bf16 v[144:147], a[212:215], v[228:231], v[144:147]\n\t"
    "ds_read_b64_tr_b16 a[236:237], v27 offset:16640\n\t"
    "ds_read_b64_tr_b16 a[238:239], v27 offset:20736\n\t"
    "v_fma_f32 v252, v252, v236, v238\n\t"
    "v_mfma_f32_16x16x32_bf16 v[148:151], a[212:215], v[232:235], v[148:151]\n\t"
    "v_cvt_pk_bf16_f32 v220, v252, v253\n\t"
    "v_lshlrev_b32 v254, 16, v224\n\t"
    "v_mfma_f32_16x16x32_bf16 v[88:91], a[216:219], v[228:231], v[88:91]\n\t"
    "ds_read_b64_tr_b16 a[240:241], v28 offset:16640\n\t"
    "ds_read_b64_tr_b16 a[242:243], v28 offset:20736\n\t"
    "v_fma_f32 v255, v224, v237, v239\n\t"
    "v_mfma_f32_16x16x32_bf16 v[92:95], a[216:219], v[232:235], v[92:95]\n\t"
    "buffer_load_dwordx4 v43, %[srdwq], s68 offen offset:3072 lds\n\t"
    "v_fma_f32 v254, v254, v236, v238\n\t"
    "v_cvt_pk_bf16_f32 v224, v254, v255\n\t"
    "v_mfma_f32_16x16x32_bf16 v[152:155], a[220:223], v[228:231], v[152:155]\n\t"
    "ds_read_b64_tr_b16 a[244:245], v29 offset:16640\n\t"
    "ds_read_b64_tr_b16 a[246:247], v29 offset:20736\n\t"
    "v_lshlrev_b32 v252, 16, v221\n\t"
    "v_mfma_f32_16x16x32_bf16 v[156:159], a[220:223], v[232:235], v[156:159]\n\t"
    "v_fma_f32 v253, v221, v241, v243\n\t"
    "v_fma_f32 v252, v252, v240, v242\n\t"
    "s_waitcnt lgkmcnt(8)\n\t"
    "v_mfma_f32_16x16x32_bf16 v[96:99], a[224:227], v[228:231], v[96:99]\n\t"
    "ds_read_b64_tr_b16 a[248:249], v30 offset:16640\n\t"
    "ds_read_b64_tr_b16 a[250:251], v30 offset:20736\n\t"
    "v_cvt_pk_bf16_f32 v221, v252, v253\n\t"
    "v_mfma_f32_16x16x32_bf16 v[100:103], a[224:227], v[232:235], v[100:103]\n\t"
    "s_waitcnt vmcnt(8)\n\t"
    "s_barrier\n\t"
    "v_lshlrev_b32 v254, 16, v225\n\t"
    "v_fma_f32 v255, v225, v241, v243\n\t"
    "v_mfma_f32_16x16x32_bf16 v[160:163], a[228:231], v[228:231], v[160:163]\n\t"
    "ds_read_b64_tr_b16 a[252:253], v31 offset:16640\n\t"
    "ds_read_b64_tr_b16 a[254:255], v31 offset:20736\n\t"
    "v_fma_f32 v254, v254, v240, v242\n\t"
    "v_mfma_f32_16x16x32_bf16 v[164:167], a[228:231], v[232:235], v[164:167]\n\t"
    "v_cvt_pk_bf16_f32 v225, v254, v255\n\t"
    "s_waitcnt lgkmcnt(8)\n\t"
    "v_mfma_f32_16x16x32_bf16 v[104:107], a[232:235], v[228:231], v[104:107]\n\t"
    "ds_read_b64_tr_b16 a[192:193], v24 offset:32768\n\t"
    "ds_read_b64_tr_b16 a[194:195], v24 offset:36864\n\t"
    "v_lshlrev_b32 v252, 16, v222\n\t"
    "v_fma_f32 v253, v222, v245, v247\n\t"
    "v_mfma_f32_16x16x32_bf16 v[108:111], a[232:235], v[232:235], v[108:111]\n\t"
    "v_fma_f32 v252, v252, v244, v246\n\t"
    "v_mfma_f32_16x16x32_bf16 v[168:171], a[236:239], v[228:231], v[168:171]\n\t"
    "ds_read_b64_tr_b16 a[196:197], v25 offset:32768\n\t"
    "ds_read_b64_tr_b16 a[198:199], v25 offset:36864\n\t"
    "v_cvt_pk_bf16_f32 v222, v252, v253\n\t"
    "v_lshlrev_b32 v254, 16, v226\n\t"
    "v_mfma_f32_16x16x32_bf16 v[172:175], a[236:239], v[232:235], v[172:175]\n\t"
    "v_fma_f32 v255, v226, v245, v247\n\t"
    "s_waitcnt lgkmcnt(8)\n\t"
    "v_mfma_f32_16x16x32_bf16 v[112:115], a[240:243], v[228:231], v[112:115]\n\t"
    "ds_read_b64_tr_b16 a[200:201], v26 offset:32768\n\t"
    "ds_read_b64_tr_b16 a[202:203], v26 offset:36864\n\t"
    "v_fma_f32 v254, v254, v244, v246\n\t"
    "v_cvt_pk_bf16_f32 v226, v254, v255\n\t"
    "v_mfma_f32_16x16x32_bf16 v[116:119], a[240:243], v[232:235], v[116:119]\n\t"
    "v_lshlrev_b32 v252, 16, v223\n\t"
    "v_mfma_f32_16x16x32_bf16 v[176:179], a[244:247], v[228:231], v[176:179]\n\t"
    "ds_read_b64_tr_b16 a[204:205], v27 offset:32768\n\t"
    "ds_read_b64_tr_b16 a[206:207], v27 offset:36864\n\t"
    "v_fma_f32 v253, v223, v249, v251\n\t"
    "v_fma_f32 v252, v252, v248, v250\n\t"
    "v_mfma_f32_16x16x32_bf16 v[180:183], a[244:247], v[232:235], v[180:183]\n\t"
    "v_cvt_pk_bf16_f32 v223, v252, v253\n\t"
    "s_waitcnt lgkmcnt(8)\n\t"
    "v_mfma_f32_16x16x32_bf16 v[120:123], a[248:251], v[228:231], v[120:123]\n\t"
    "ds_read_b64_tr_b16 a[208:209], v28 offset:32768\n\t"
    "ds_read_b64_tr_b16 a[210:211], v28 offset:36864\n\t"
    "v_lshlrev_b32 v254, 16, v227\n\t"
    "v_fma_f32 v255, v227, v249, v251\n\t"
    "v_mfma_f32_16x16x32_bf16 v[124:127], a[248:251], v[232:235], v[124:127]\n\t"
    "v_fma_f32 v254, v254, v248, v250\n\t"
    "v_mfma_f32_16x16x32_bf16 v[184:187], a[252:255], v[228:231], v[184:187]\n\t"
    "ds_read_b64_tr_b16 a[212:213], v29 offset:32768\n\t"
    "ds_read_b64_tr_b16 a[214:215], v29 offset:36864\n\t"
    "v_cvt_pk_bf16_f32 v227, v254, v255\n\t"
    "v_mfma_f32_16x16x32_bf16 v[188:191], a[252:255], v[232:235], v[188:191]\n\t"
    "s_waitcnt lgkmcnt(8)\n\t"
    "v_mfma_f32_16x16x32_bf16 v[64:67], a[192:195], v[220:223], v[64:67]\n\t"
    "ds_read_b64_tr_b16 a[216:217], v30 offset:32768\n\t"
    "ds_read_b64_tr_b16 a[218:219], v30 offset:36864\n\t"
    "v_mfma_f32_16x16x32_bf16 v[68:71], a[192:195], v[224:227], v[68:71]\n\t"
    "ds_read_b128 v[228:231], v55 offset:24576\n\t"
    "ds_read_b128 v[232:235], v55 offset:24832\n\t"
    "v_mfma_f32_16x16x32_bf16 v[128:131], a[196:199], v[220:223], v[128:131]\n\t"
    "ds_read_b64_tr_b16 a[220:221], v31 offset:32768\n\t"
    "ds_read_b64_tr_b16 a[222:223], v31 offset:36864\n\t"
    "ds_read_b128 v[236:239], v54 offset:3328\n\t"
    "ds_read_b128 v[240:243], v54 offset:3344\n\t"
    "v_mfma_f32_16x16x32_bf16 v[132:135], a[196:199], v[224:227], v[132:135]\n\t"
    "ds_read_b128 v[244:247], v54 offset:3360\n\t"
    "ds_read_b128 v[248:251], v54 offset:3376\n\t"
    "s_waitcnt lgkmcnt(14)\n\t"
    "v_mfma_f32_16x16x32_bf16 v[72:75], a[200:203], v[220:223], v[72:75]\n\t"
    "ds_read_b64_tr_b16 a[224:225], v24 offset:33024\n\t"
    "ds_read_b64_tr_b16 a[226:227], v24 offset:37120\n\t"
    "v_mfma_f32_16x16x32_bf16 v[76:79], a[200:203], v[224:227], v[76:79]\n\t"
    "v_mfma_f32_16x16x32_bf16 v[136:139], a[204:207], v[220:223], v[136:139]\n\t"
    "ds_read_b64_tr_b16 a[228:229], v25 offset:33024\n\t"
    "ds_read_b64_tr_b16 a[230:231], v25 offset:37120\n\t"
    "v_mfma_f32_16x16x32_bf16 v[140:143], a[204:207], v[224:227], v[140:143]\n\t"
    "s_waitcnt lgkmcnt(14)\n\t"
    "v_mfma_f32_16x16x32_bf16 v[80:83], a[208:211], v[220:223], v[80:83]\n\t"
    "ds_read_b64_tr_b16 a[232:233], v26 offset:33024\n\t"
    "ds_read_b64_tr_b16 a[234:235], v26 offset:37120\n\t"
    "v_mfma_f32_16x16x32_bf16 v[84:87], a[208:211], v[224:227], v[84:87]\n\t"
    "s_waitcnt lgkmcnt(6)\n\t"
    "v_lshlrev_b32 v252, 16, v228\n\t"
    "v_fma_f32 v253, v228, v237, v239\n\t"
    "v_mfma_f32_16x16x32_bf16 v[144:147], a[212:215], v[220:223], v[144:147]\n\t"
    "ds_read_b64_tr_b16 a[236:237], v27 offset:33024\n\t"
    "ds_read_b64_tr_b16 a[238:239], v27 offset:37120\n\t"
    "v_fma_f32 v252, v252, v236, v238\n\t"
    "v_mfma_f32_16x16x32_bf16 v[148:151], a[212:215], v[224:227], v[148:151]\n\t"
    "v_cvt_pk_bf16_f32 v228, v252, v253\n\t"
    "v_lshlrev_b32 v254, 16, v232\n\t"
    "v_mfma_f32_16x16x32_bf16 v[88:91], a[216:219], v[220:223], v[88:91]\n\t"
    "ds_read_b64_tr_b16 a[240:241], v28 offset:33024\n\t"
    "ds_read_b64_tr_b16 a[242:243], v28 offset:37120\n\t"
    "v_fma_f32 v255, v232, v237, v239\n\t"
    "v_mfma_f32_16x16x32_bf16 v[92:95], a[216:219], v[224:227], v[92:95]\n\t"
    "v_fma_f32 v254, v254, v236, v238\n\t"
    "v_cvt_pk_bf16_f32 v232, v254, v255\n\t"
    "v_mfma_f32_16x16x32_bf16 v[152:155], a[220:223], v[220:223], v[152:155]\n\t"
    "ds_read_b64_tr_b16 a[244:245], v29 offset:33024\n\t"
    "ds_read_b64_tr_b16 a[246:247], v29 offset:37120\n\t"
    "v_lshlrev_b32 v252, 16, v229\n\t"
    "v_mfma_f32_16x16x32_bf16 v[156:159], a[220:223], v[224:227], v[156:159]\n\t"
    "v_fma_f32 v253, v229, v241, v243\n\t"
    "v_fma_f32 v252, v252, v240, v242\n\t"
    "s_waitcnt lgkmcnt(8)\n\t"
    "v_mfma_f32_16x16x32_bf16 v[96:99], a[224:227], v[220:223], v[96:99]\n\t"
    "ds_read_b64_tr_b16 a[248:249], v30 offset:33024\n\t"
    "ds_read_b64_tr_b16 a[250:251], v30 offset:37120\n\t"
    "v_cvt_pk_bf16_f32 v229, v252, v253\n\t"
    "v_mfma_f32_16x16x32_bf16 v[100:103], a[224:227], v[224:227], v[100:103]\n\t"
    "s_waitcnt vmcnt(0)\n\t"
    "s_barrier\n\t"
    "v_lshlrev_b32 v254, 16, v233\n\t"
    "v_fma_f32 v255, v233, v241, v243\n\t"
    "v_mfma_f32_16x16x32_bf16 v[160:163], a[228:231], v[220:223], v[160:163]\n\t"
    "ds_read_b64_tr_b16 a[252:253], v31 offset:33024\n\t"
    "ds_read_b64_tr_b16 a[254:255], v31 offset:37120\n\t"
    "v_fma_f32 v254, v254, v240, v242\n\t"
    "v_mfma_f32_16x16x32_bf16 v[164:167], a[228:231], v[224:227], v[164:167]\n\t"
    "s_add_u32 m0, s63, 0x0\n\t"
    "v_cvt_pk_bf16_f32 v233, v254, v255\n\t"
    "s_waitcnt lgkmcnt(8)\n\t"
    "v_mfma_f32_16x16x32_bf16 v[104:107], a[232:235], v[220:223], v[104:107]\n\t"
    "ds_read_b64_tr_b16 a[192:193], v24 offset:49152\n\t"
    "ds_read_b64_tr_b16 a[194:195], v24 offset:53248\n\t"
    "buffer_load_dwordx4 v8, %[srdk], s61 offen lds\n\t"
    "v_lshlrev_b32 v252, 16, v230\n\t"
    "v_fma_f32 v253, v230, v245, v247\n\t"
    "v_mfma_f32_16x16x32_bf16 v[108:111], a[232:235], v[224:227], v[108:111]\n\t"
    "buffer_load_dwordx4 v9, %[srdk], s61 offen offset:1024 lds\n\t"
    "v_fma_f32 v252, v252, v244, v246\n\t"
    "v_mfma_f32_16x16x32_bf16 v[168:171], a[236:239], v[220:223], v[168:171]\n\t"
    "ds_read_b64_tr_b16 a[196:197], v25 offset:49152\n\t"
    "ds_read_b64_tr_b16 a[198:199], v25 offset:53248\n\t"
    "buffer_load_dwordx4 v10, %[srdk], s61 offen offset:2048 lds\n\t"
    "v_cvt_pk_bf16_f32 v230, v252, v253\n\t"
    "v_lshlrev_b32 v254, 16, v234\n\t"
    "v_mfma_f32_16x16x32_bf16 v[172:175], a[236:239], v[224:227], v[172:175]\n\t"
    "v_fma_f32 v255, v234, v245, v247\n\t"
    "s_waitcnt lgkmcnt(8)\n\t"
    "v_mfma_f32_16x16x32_bf16 v[112:115], a[240:243], v[220:223], v[112:115]\n\t"
    "ds_read_b64_tr_b16 a[200:201], v26 offset:49152\n\t"
    "ds_read_b64_tr_b16 a[202:203], v26 offset:53248\n\t"
    "buffer_load_dwordx4 v11, %[srdk], s61 offen offset:3072 lds\n\t"
    "s_add_u32 m0, s63, 0x1000\n\t"
    "v_fma_f32 v254, v254, v244, v246\n\t"
    "v_cvt_pk_bf16_f32 v234, v254, v255\n\t"
    "v_mfma_f32_16x16x32_bf16 v[116:119], a[240:243], v[224:227], v[116:119]\n\t"
    "buffer_load_dwordx4 v12, %[srdk], s61 offen lds\n\t"
    "v_lshlrev_b32 v252, 16, v231\n\t"
    "v_mfma_f32_16x16x32_bf16 v[176:179], a[244:247], v[220:223], v[176:179]\n\t"
    "ds_read_b64_tr_b16 a[204:205], v27 offset:49152\n\t"
    "ds_read_b64_tr_b16 a[206:207], v27 offset:53248\n\t"
    "buffer_load_dwordx4 v13, %[srdk], s61 offen offset:1024 lds\n\t"
    "v_fma_f32 v253, v231, v249, v251\n\t"
    "v_fma_f32 v252, v252, v248, v250\n\t"
    "v_mfma_f32_16x16x32_bf16 v[180:183], a[244:247], v[224:227], v[180:183]\n\t"
    "v_cvt_pk_bf16_f32 v231, v252, v253\n\t"
    "s_waitcnt lgkmcnt(8)\n\t"
    "v_mfma_f32_16x16x32_bf16 v[120:123], a[248:251], v[220:223], v[120:123]\n\t"
    "ds_read_b64_tr_b16 a[208:209], v28 offset:49152\n\t"
    "ds_read_b64_tr_b16 a[210:211], v28 offset:53248\n\t"
    "buffer_load_dwordx4 v14, %[srdk], s61 offen offset:2048 lds\n\t"
    "v_lshlrev_b32 v254, 16, v235\n\t"
    "v_fma_f32 v255, v235, v249, v251\n\t"
    "v_mfma_f32_16x16x32_bf16 v[124:127], a[248:251], v[224:227], v[124:127]\n\t"
    "buffer_load_dwordx4 v15, %[srdk], s61 offen offset:3072 lds\n\t"
    "v_fma_f32 v254, v254, v248, v250\n\t"
    "v_mfma_f32_16x16x32_bf16 v[184:187], a[252:255], v[220:223], v[184:187]\n\t"
    "ds_read_b64_tr_b16 a[212:213], v29 offset:49152\n\t"
    "ds_read_b64_tr_b16 a[214:215], v29 offset:53248\n\t"
    "v_cvt_pk_bf16_f32 v235, v254, v255\n\t"
    "v_mfma_f32_16x16x32_bf16 v[188:191], a[252:255], v[224:227], v[188:191]\n\t"
    "s_add_u32 s61, s61, 0x8000\n\t"
    "s_waitcnt lgkmcnt(8)\n\t"
    "v_mfma_f32_16x16x32_bf16 v[64:67], a[192:195], v[228:231], v[64:67]\n\t"
    "ds_read_b64_tr_b16 a[216:217], v30 offset:49152\n\t"
    "ds_read_b64_tr_b16 a[218:219], v30 offset:53248\n\t"
    "v_mfma_f32_16x16x32_bf16 v[68:71], a[192:195], v[232:235], v[68:71]\n\t"
    "s_add_u32 m0, s63, 0x10000\n\t"
    "v_mfma_f32_16x16x32_bf16 v[128:131], a[196:199], v[228:231], v[128:131]\n\t"
    "ds_read_b64_tr_b16 a[220:221], v31 offset:49152\n\t"
    "ds_read_b64_tr_b16 a[222:223], v31 offset:53248\n\t"
    "v_mfma_f32_16x16x32_bf16 v[132:135], a[196:199], v[232:235], v[132:135]\n\t"
    "buffer_load_dwordx4 v36, %[srdv], s62 offen lds\n\t"
    "s_waitcnt lgkmcnt(8)\n\t"
    "v_mfma_f32_16x16x32_bf16 v[72:75], a[200:203], v[228:231], v[72:75]\n\t"
    "ds_read_b64_tr_b16 a[224:225], v24 offset:49408\n\t"
    "ds_read_b64_tr_b16 a[226:227], v24 offset:53504\n\t"
    "v_mfma_f32_16x16x32_bf16 v[76:79], a[200:203], v[232:235], v[76:79]\n\t"
    "buffer_load_dwordx4 v37, %[srdv], s62 offen offset:1024 lds\n\t"
    "v_mfma_f32_16x16x32_bf16 v[136:139], a[204:207], v[228:231], v[136:139]\n\t"
    "ds_read_b64_tr_b16 a[228:229], v25 offset:49408\n\t"
    "ds_read_b64_tr_b16 a[230:231], v25 offset:53504\n\t"
    "v_cvt_pk_bf16_f32 v64, v64, v65\n\t"
    "v_cvt_pk_bf16_f32 v65, v66, v67\n\t"
    "v_cvt_pk_bf16_f32 v66, v128, v129\n\t"
    "v_mfma_f32_16x16x32_bf16 v[140:143], a[204:207], v[232:235], v[140:143]\n\t"
    "v_cvt_pk_bf16_f32 v67, v130, v131\n\t"
    "v_cvt_pk_bf16_f32 v68, v68, v69\n\t"
    "v_cvt_pk_bf16_f32 v69, v70, v71\n\t"
    "s_waitcnt lgkmcnt(8)\n\t"
    "v_mfma_f32_16x16x32_bf16 v[80:83], a[208:211], v[228:231], v[80:83]\n\t"
    "ds_read_b64_tr_b16 a[232:233], v26 offset:49408\n\t"
    "ds_read_b64_tr_b16 a[234:235], v26 offset:53504\n\t"
    "v_cvt_pk_bf16_f32 v70, v132, v133\n\t"
    "v_cvt_pk_bf16_f32 v71, v134, v135\n\t"
    "v_permlane16_swap_b32 v64, v66\n\t"
    "v_mfma_f32_16x16x32_bf16 v[84:87], a[208:211], v[232:235], v[84:87]\n\t"
    "buffer_load_dwordx4 v38, %[srdv], s62 offen offset:2048 lds\n\t"
    "v_permlane16_swap_b32 v65, v67\n\t"
    "v_permlane16_swap_b32 v68, v70\n\t"
    "v_permlane16_swap_b32 v69, v71\n\t"
    "v_mfma_f32_16x16x32_bf16 v[144:147], a[212:215], v[228:231], v[144:147]\n\t"
    "ds_read_b64_tr_b16 a[236:237], v27 offset:49408\n\t"
    "ds_read_b64_tr_b16 a[238:239], v27 offset:53504\n\t"
    "v_cvt_pk_bf16_f32 v72, v72, v73\n\t"
    "v_cvt_pk_bf16_f32 v73, v74, v75\n\t"
    "v_cvt_pk_bf16_f32 v74, v136, v137\n\t"
    "v_mfma_f32_16x16x32_bf16 v[148:151], a[212:215], v[232:235], v[148:151]\n\t"
    "buffer_load_dwordx4 v39, %[srdv], s62 offen offset:3072 lds\n\t"
    "v_cvt_pk_bf16_f32 v75, v138, v139\n\t"
    "v_cvt_pk_bf16_f32 v76, v76, v77\n\t"
    "v_cvt_pk_bf16_f32 v77, v78, v79\n\t"
    "s_waitcnt lgkmcnt(8)\n\t"
    "v_mfma_f32_16x16x32_bf16 v[88:91], a[216:219], v[228:231], v[88:91]\n\t"
    "ds_read_b64_tr_b16 a[240:241], v28 offset:49408\n\t"
    "ds_read_b64_tr_b16 a[242:243], v28 offset:53504\n\t"
    "v_cvt_pk_bf16_f32 v78, v140, v141\n\t"
    "v_cvt_pk_bf16_f32 v79, v142, v143\n\t"
    "v_permlane16_swap_b32 v72, v74\n\t"
    "v_mfma_f32_16x16x32_bf16 v[92:95], a[216:219], v[232:235], v[92:95]\n\t"
    "s_add_u32 m0, s63, 0x11000\n\t"
    "s_add_u32 s62, s62, 0x1000\n\t"
    "v_permlane16_swap_b32 v73, v75\n\t"
    "v_permlane16_swap_b32 v76, v78\n\t"
    "v_permlane16_swap_b32 v77, v79\n\t"
    "v_mfma_f32_16x16x32_bf16 v[152:155], a[220:223], v[228:231], v[152:155]\n\t"
    "ds_read_b64_tr_b16 a[244:245], v29 offset:49408\n\t"
    "ds_read_b64_tr_b16 a[246:247], v29 offset:53504\n\t"
    "v_cvt_pk_bf16_f32 v80, v80, v81\n\t"
    "v_cvt_pk_bf16_f32 v81, v82, v83\n\t"
    "v_cvt_pk_bf16_f32 v82, v144, v145\n\t"
    "v_mfma_f32_16x16x32_bf16 v[156:159], a[220:223], v[232:235], v[156:159]\n\t"
    "buffer_load_dwordx4 v36, %[srdv], s62 offen lds\n\t"
    "v_cvt_pk_bf16_f32 v83, v146, v147\n\t"
    "v_cvt_pk_bf16_f32 v84, v84, v85\n\t"
    "v_cvt_pk_bf16_f32 v85, v86, v87\n\t"
    "s_waitcnt lgkmcnt(8)\n\t"
    "v_mfma_f32_16x16x32_bf16 v[96:99], a[224:227], v[228:231], v[96:99]\n\t"
    "ds_read_b64_tr_b16 a[248:249], v30 offset:49408\n\t"
    "ds_read_b64_tr_b16 a[250:251], v30 offset:53504\n\t"
    "v_cvt_pk_bf16_f32 v86, v148, v149\n\t"
    "v_cvt_pk_bf16_f32 v87, v150, v151\n\t"
    "v_permlane16_swap_b32 v80, v82\n\t"
    "v_mfma_f32_16x16x32_bf16 v[100:103], a[224:227], v[232:235], v[100:103]\n\t"
    "v_permlane16_swap_b32 v81, v83\n\t"
    "v_permlane16_swap_b32 v84, v86\n\t"
    "v_permlane16_swap_b32 v85, v87\n\t"
    "v_mfma_f32_16x16x32_bf16 v[160:163], a[228:231], v[228:231], v[160:163]\n\t"
    "ds_read_b64_tr_b16 a[252:253], v31 offset:49408\n\t"
    "ds_read_b64_tr_b16 a[254:255], v31 offset:53504\n\t"
    "v_cvt_pk_bf16_f32 v88, v88, v89\n\t"
    "v_cvt_pk_bf16_f32 v89, v90, v91\n\t"
    "v_cvt_pk_bf16_f32 v90, v152, v153\n\t"
    "v_mfma_f32_16x16x32_bf16 v[164:167], a[228:231], v[232:235], v[164:167]\n\t"
    "buffer_load_dwordx4 v37, %[srdv], s62 offen offset:1024 lds\n\t"
    "v_cvt_pk_bf16_f32 v91, v154, v155\n\t"
    "v_cvt_pk_bf16_f32 v92, v92, v93\n\t"
    "v_cvt_pk_bf16_f32 v93, v94, v95\n\t"
    "s_waitcnt lgkmcnt(8)\n\t"
    "v_mfma_f32_16x16x32_bf16 v[104:107], a[232:235], v[228:231], v[104:107]\n\t"
    "v_cvt_pk_bf16_f32 v94, v156, v157\n\t"
    "v_cvt_pk_bf16_f32 v95, v158, v159\n\t"
    "v_permlane16_swap_b32 v88, v90\n\t"
    "v_mfma_f32_16x16x32_bf16 v[108:111], a[232:235], v[232:235], v[108:111]\n\t"
    "v_permlane16_swap_b32 v89, v91\n\t"
    "v_permlane16_swap_b32 v92, v94\n\t"
    "v_permlane16_swap_b32 v93, v95\n\t"
    "v_mfma_f32_16x16x32_bf16 v[168:171], a[236:239], v[228:231], v[168:171]\n\t"
    "v_cvt_pk_bf16_f32 v96, v96, v97\n\t"
    "v_cvt_pk_bf16_f32 v97, v98, v99\n\t"
    "v_cvt_pk_bf16_f32 v98, v160, v161\n\t"
    "v_mfma_f32_16x16x32_bf16 v[172:175], a[236:239], v[232:235], v[172:175]\n\t"
    "buffer_load_dwordx4 v38, %[srdv], s62 offen offset:2048 lds\n\t"
    "v_cvt_pk_bf16_f32 v99, v162, v163\n\t"
    "v_cvt_pk_bf16_f32 v100, v100, v101\n\t"
    "v_cvt_pk_bf16_f32 v101, v102, v103\n\t"
    "s_waitcnt lgkmcnt(4)\n\t"
    "v_mfma_f32_16x16x32_bf16 v[112:115], a[240:243], v[228:231], v[112:115]\n\t"
    "v_cvt_pk_bf16_f32 v102, v164, v165\n\t"
    "v_cvt_pk_bf16_f32 v103, v166, v167\n\t"
    "v_permlane16_swap_b32 v96, v98\n\t"
    "v_mfma_f32_16x16x32_bf16 v[116:119], a[240:243], v[232:235], v[116:119]\n\t"
    "buffer_load_dwordx4 v39, %[srdv], s62 offen offset:3072 lds\n\t"
    "v_permlane16_swap_b32 v97, v99\n\t"
    "v_permlane16_swap_b32 v100, v102\n\t"
    "v_permlane16_swap_b32 v101, v103\n\t"
    "v_mfma_f32_16x16x32_bf16 v[176:179], a[244:247], v[228:231], v[176:179]\n\t"
    "s_add_u32 s62, s62, 0x7000\n\t"
    "v_cvt_pk_bf16_f32 v104, v104, v105\n\t"
    "v_cvt_pk_bf16_f32 v105, v106, v107\n\t"
    "v_cvt_pk_bf16_f32 v106, v168, v169\n\t"
    "v_mfma_f32_16x16x32_bf16 v[180:183], a[244:247], v[232:235], v[180:183]\n\t"
    "v_cvt_pk_bf16_f32 v107, v170, v171\n\t"
    "v_cvt_pk_bf16_f32 v108, v108, v109\n\t"
    "v_cvt_pk_bf16_f32 v109, v110, v111\n\t"
    "s_waitcnt lgkmcnt(0)\n\t"
    "v_mfma_f32_16x16x32_bf16 v[120:123], a[248:251], v[228:231], v[120:123]\n\t"
    "v_cvt_pk_bf16_f32 v110, v172, v173\n\t"
    "v_cvt_pk_bf16_f32 v111, v174, v175\n\t"
    "v_permlane16_swap_b32 v104, v106\n\t"
    "v_mfma_f32_16x16x32_bf16 v[124:127], a[248:251], v[232:235], v[124:127]\n\t"
    "v_permlane16_swap_b32 v105, v107\n\t"
    "v_permlane16_swap_b32 v108, v110\n\t"
    "v_permlane16_swap_b32 v109, v111\n\t"
    "v_mfma_f32_16x16x32_bf16 v[184:187], a[252:255], v[228:231], v[184:187]\n\t"
    "v_cvt_pk_bf16_f32 v112, v112, v113\n\t"
    "v_cvt_pk_bf16_f32 v113, v114, v115\n\t"
    "v_cvt_pk_bf16_f32 v114, v176, v177\n\t"
    "v_mfma_f32_16x16x32_bf16 v[188:191], a[252:255], v[232:235], v[188:191]\n\t"
    "v_cvt_pk_bf16_f32 v115, v178, v179\n\t"
    "v_cvt_pk_bf16_f32 v116, v116, v117\n\t"
    "v_cvt_pk_bf16_f32 v117, v118, v119\n\t"
    "v_cvt_pk_bf16_f32 v118, v180, v181\n\t"
    "v_cvt_pk_bf16_f32 v119, v182, v183\n\t"
    "v_permlane16_swap_b32 v112, v114\n\t"
    "v_permlane16_swap_b32 v113, v115\n\t"
    "v_permlane16_swap_b32 v116, v118\n\t"
    "v_permlane16_swap_b32 v117, v119\n\t"
    "s_waitcnt vmcnt(8)\n\t"
    "s_barrier\n\t"
    "ds_read_b128 a[128:131], v32\n\t"
    "ds_read_b128 a[132:135], v33\n\t"
    "ds_read_b128 a[136:139], v34\n\t"
    "ds_read_b128 a[140:143], v35\n\t"
    "ds_read_b128 a[144:147], v32 offset:256\n\t"
    "ds_read_b128 a[148:151], v33 offset:256\n\t"
    "ds_read_b128 a[152:155], v34 offset:256\n\t"
    "ds_read_b128 a[156:159], v35 offset:256\n\t"
    "v_cvt_pk_bf16_f32 v120, v120, v121\n\t"
    "v_cvt_pk_bf16_f32 v121, v122, v123\n\t"
    "v_cvt_pk_bf16_f32 v122, v184, v185\n\t"
    "v_cvt_pk_bf16_f32 v123, v186, v187\n\t"
    "v_cvt_pk_bf16_f32 v124, v124, v125\n\t"
    "v_cvt_pk_bf16_f32 v125, v126, v127\n\t"
    "v_cvt_pk_bf16_f32 v126, v188, v189\n\t"
    "v_cvt_pk_bf16_f32 v127, v190, v191\n\t"
    "v_permlane16_swap_b32 v120, v122\n\t"
    "v_permlane16_swap_b32 v121, v123\n\t"
    "v_permlane16_swap_b32 v124, v126\n\t"
    "v_permlane16_swap_b32 v125, v127\n\t"
    "s_add_u32 m0, s63, 0x8000\n\t"
    "s_nop 0\n\t"
    "buffer_load_dwordx4 v8, %[srdk], s61 offen lds\n\t"
    "buffer_load_dwordx4 v9, %[srdk], s61 offen offset:1024 lds\n\t"
    "buffer_load_dwordx4 v10, %[srdk], s61 offen offset:2048 lds\n\t"
    "buffer_load_dwordx4 v11, %[srdk], s61 offen offset:3072 lds\n\t"
    "s_add_u32 m0, s63, 0x9000\n\t"
    "s_nop 0\n\t"
    "buffer_load_dwordx4 v12, %[srdk], s61 offen lds\n\t"
    "buffer_load_dwordx4 v13, %[srdk], s61 offen offset:1024 lds\n\t"
    "buffer_load_dwordx4 v14, %[srdk], s61 offen offset:2048 lds\n\t"
    "buffer_load_dwordx4 v15, %[srdk], s61 offen offset:3072 lds\n\t"
    "s_add_u32 s61, s61, 0x8000\n\t"
    "buffer_load_dword v7, v59, %[srdt], 0 offen\n\t"
    "s_waitcnt lgkmcnt(0)\n\t"
    "v_mfma_f32_16x16x32_bf16 v[128:131], a[128:131], v[64:67], 0\n\t"
    "v_mfma_f32_16x16x32_bf16 v[128:131], a[132:135], v[72:75], v[128:131]\n\t"
    "v_mfma_f32_16x16x32_bf16 v[128:131], a[136:139], v[80:83], v[128:131]\n\t"
    "ds_read_b128 a[160:163], v32 offset:8192\n\t"
    "ds_read_b128 a[164:167], v33 offset:8192\n\t"
    "v_mfma_f32_16x16x32_bf16 v[128:131], a[140:143], v[88:91], v[128:131]\n\t"
    "v_mfma_f32_16x16x32_bf16 v[128:131], a[144:147], v[96:99], v[128:131]\n\t"
    "ds_read_b128 a[168:171], v34 offset:8192\n\t"
    "ds_read_b128 a[172:175], v35 offset:8192\n\t"
    "v_mfma_f32_16x16x32_bf16 v[128:131], a[148:151], v[104:107], v[128:131]\n\t"
    "v_mfma_f32_16x16x32_bf16 v[128:131], a[152:155], v[112:115], v[128:131]\n\t"
    "ds_read_b128 a[176:179], v32 offset:8448\n\t"
    "ds_read_b128 a[180:183], v33 offset:8448\n\t"
    "v_mfma_f32_16x16x32_bf16 v[128:131], a[156:159], v[120:123], v[128:131]\n\t"
    "v_mfma_f32_16x16x32_bf16 v[132:135], a[128:131], v[68:71], 0\n\t"
    "ds_read_b128 a[184:187], v34 offset:8448\n\t"
    "ds_read_b128 a[188:191], v35 offset:8448\n\t"
    "v_mfma_f32_16x16x32_bf16 v[132:135], a[132:135], v[76:79], v[132:135]\n\t"
    "v_mfma_f32_16x16x32_bf16 v[132:135], a[136:139], v[84:87], v[132:135]\n\t"
    "v_mfma_f32_16x16x32_bf16 v[132:135], a[140:143], v[92:95], v[132:135]\n\t"
    "v_mfma_f32_16x16x32_bf16 v[132:135], a[144:147], v[100:103], v[132:135]\n\t"
    "v_mfma_f32_16x16x32_bf16 v[132:135], a[148:151], v[108:111], v[132:135]\n\t"
    "v_mfma_f32_16x16x32_bf16 v[132:135], a[152:155], v[116:119], v[132:135]\n\t"
    "v_mfma_f32_16x16x32_bf16 v[132:135], a[156:159], v[124:127], v[132:135]\n\t"
    "s_waitcnt lgkmcnt(0)\n\t"
    "v_mfma_f32_16x16x32_bf16 v[136:139], a[160:163], v[64:67], 0\n\t"
    "v_mfma_f32_16x16x32_bf16 v[136:139], a[164:167], v[72:75], v[136:139]\n\t"
    "v_mfma_f32_16x16x32_bf16 v[136:139], a[168:171], v[80:83], v[136:139]\n\t"
    "ds_read_b128 a[128:131], v32 offset:16384\n\t"
    "ds_read_b128 a[132:135], v33 offset:16384\n\t"
    "v_mfma_f32_16x16x32_bf16 v[136:139], a[172:175], v[88:91], v[136:139]\n\t"
    "v_mfma_f32_16x16x32_bf16 v[136:139], a[176:179], v[96:99], v[136:139]\n\t"
    "ds_read_b128 a[136:139], v34 offset:16384\n\t"
    "ds_read_b128 a[140:143], v35 offset:16384\n\t"
    "v_mfma_f32_16x16x32_bf16 v[136:139], a[180:183], v[104:107], v[136:139]\n\t"
    "v_mfma_f32_16x16x32_bf16 v[136:139], a[184:187], v[112:115], v[136:139]\n\t"
    "ds_read_b128 a[144:147], v32 offset:16640\n\t"
    "ds_read_b128 a[148:151], v33 offset:16640\n\t"
    "v_mfma_f32_16x16x32_bf16 v[136:139], a[188:191], v[120:123], v[136:139]\n\t"
    "v_mfma_f32_16x16x32_bf16 v[140:143], a[160:163], v[68:71], 0\n\t"
    "ds_read_b128 a[152:155], v34 offset:16640\n\t"
    "ds_read_b128 a[156:159], v35 offset:16640\n\t"
    "v_mfma_f32_16x16x32_bf16 v[140:143], a[164:167], v[76:79], v[140:143]\n\t"
    "v_mfma_f32_16x16x32_bf16 v[140:143], a[168:171], v[84:87], v[140:143]\n\t"
    "v_mfma_f32_16x16x32_bf16 v[140:143], a[172:175], v[92:95], v[140:143]\n\t"
    "v_mfma_f32_16x16x32_bf16 v[140:143], a[176:179], v[100:103], v[140:143]\n\t"
    "v_mfma_f32_16x16x32_bf16 v[140:143], a[180:183], v[108:111], v[140:143]\n\t"
    "v_mfma_f32_16x16x32_bf16 v[140:143], a[184:187], v[116:119], v[140:143]\n\t"
    "v_mfma_f32_16x16x32_bf16 v[140:143], a[188:191], v[124:127], v[140:143]\n\t"
    "s_waitcnt lgkmcnt(0)\n\t"
    "v_mfma_f32_16x16x32_bf16 v[144:147], a[128:131], v[64:67], 0\n\t"
    "v_mfma_f32_16x16x32_bf16 v[144:147], a[132:135], v[72:75], v[144:147]\n\t"
    "v_mfma_f32_16x16x32_bf16 v[144:147], a[136:139], v[80:83], v[144:147]\n\t"
    "ds_read_b128 a[160:163], v32 offset:24576\n\t"
    "ds_read_b128 a[164:167], v33 offset:24576\n\t"
    "v_mfma_f32_16x16x32_bf16 v[144:147], a[140:143], v[88:91], v[144:147]\n\t"
    "v_mfma_f32_16x16x32_bf16 v[144:147], a[144:147], v[96:99], v[144:147]\n\t"
    "ds_read_b128 a[168:171], v34 offset:24576\n\t"
    "ds_read_b128 a[172:175], v35 offset:24576\n\t"
    "v_mfma_f32_16x16x32_bf16 v[144:147], a[148:151], v[104:107], v[144:147]\n\t"
    "v_mfma_f32_16x16x32_bf16 v[144:147], a[152:155], v[112:115], v[144:147]\n\t"
    "ds_read_b128 a[176:179], v32 offset:24832\n\t"
    "ds_read_b128 a[180:183], v33 offset:24832\n\t"
    "v_mfma_f32_16x16x32_bf16 v[144:147], a[156:159], v[120:123], v[144:147]\n\t"
    "v_mfma_f32_16x16x32_bf16 v[148:151], a[128:131], v[68:71], 0\n\t"
    "ds_read_b128 a[184:187], v34 offset:24832\n\t"
    "ds_read_b128 a[188:191], v35 offset:24832\n\t"
    "v_mfma_f32_16x16x32_bf16 v[148:151], a[132:135], v[76:79], v[148:151]\n\t"
    "v_mfma_f32_16x16x32_bf16 v[148:151], a[136:139], v[84:87], v[148:151]\n\t"
    "v_mfma_f32_16x16x32_bf16 v[148:151], a[140:143], v[92:95], v[148:151]\n\t"
    "v_mfma_f32_16x16x32_bf16 v[148:151], a[144:147], v[100:103], v[148:151]\n\t"
    "v_mfma_f32_16x16x32_bf16 v[148:151], a[148:151], v[108:111], v[148:151]\n\t"
    "v_mfma_f32_16x16x32_bf16 v[148:151], a[152:155], v[116:119], v[148:151]\n\t"
    "v_mfma_f32_16x16x32_bf16 v[148:151], a[156:159], v[124:127], v[148:151]\n\t"
    "s_waitcnt lgkmcnt(0)\n\t"
    "v_mfma_f32_16x16x32_bf16 v[152:155], a[160:163], v[64:67], 0\n\t"
    "s_waitcnt vmcnt(1)\n\t"
    "s_barrier\n\t"
    "v_mfma_f32_16x16x32_bf16 v[152:155], a[164:167], v[72:75], v[152:155]\n\t"
    "s_add_u32 m0, s63, 0x0\n\t"
    "v_mfma_f32_16x16x32_bf16 v[152:155], a[168:171], v[80:83], v[152:155]\n\t"
    "ds_read_b128 a[128:131], v32 offset:32768\n\t"
    "ds_read_b128 a[132:135], v33 offset:32768\n\t"
    "v_mfma_f32_16x16x32_bf16 v[152:155], a[172:175], v[88:91], v[152:155]\n\t"
    "buffer_load_dwordx4 v8, %[srdk], s61 offen lds\n\t"
    "v_mfma_f32_16x16x32_bf16 v[152:155], a[176:179], v[96:99], v[152:155]\n\t"
    "ds_read_b128 a[136:139], v34 offset:32768\n\t"
    "ds_read_b128 a[140:143], v35 offset:32768\n\t"
    "v_mfma_f32_16x16x32_bf16 v[152:155], a[180:183], v[104:107], v[152:155]\n\t"
    "buffer_load_dwordx4 v9, %[srdk], s61 offen offset:1024 lds\n\t"
    "v_mfma_f32_16x16x32_bf16 v[152:155], a[184:187], v[112:115], v[152:155]\n\t"
    "ds_read_b128 a[144:147], v32 offset:33024\n\t"
    "ds_read_b128 a[148:151], v33 offset:33024\n\t"
    "v_mfma_f32_16x16x32_bf16 v[152:155], a[188:191], v[120:123], v[152:155]\n\t"
    "buffer_load_dwordx4 v10, %[srdk], s61 offen offset:2048 lds\n\t"
    "v_mfma_f32_16x16x32_bf16 v[156:159], a[160:163], v[68:71], 0\n\t"
    "ds_read_b128 a[152:155], v34 offset:33024\n\t"
    "ds_read_b128 a[156:159], v35 offset:33024\n\t"
    "v_mfma_f32_16x16x32_bf16 v[156:159], a[164:167], v[76:79], v[156:159]\n\t"
    "buffer_load_dwordx4 v11, %[srdk], s61 offen offset:3072 lds\n\t"
    "s_add_u32 m0, s63, 0x1000\n\t"
    "v_mfma_f32_16x16x32_bf16 v[156:159], a[168:171], v[84:87], v[156:159]\n\t"
    "v_mfma_f32_16x16x32_bf16 v[156:159], a[172:175], v[92:95], v[156:159]\n\t"
    "buffer_load_dwordx4 v12, %[srdk], s61 offen lds\n\t"
    "v_mfma_f32_16x16x32_bf16 v[156:159], a[176:179], v[100:103], v[156:159]\n\t"
    "buffer_load_dwordx4 v13, %[srdk], s61 offen offset:1024 lds\n\t"
    "v_mfma_f32_16x16x32_bf16 v[156:159], a[180:183], v[108:111], v[156:159]\n\t"
    "buffer_load_dwordx4 v14, %[srdk], s61 offen offset:2048 lds\n\t"
    "v_mfma_f32_16x16x32_bf16 v[156:159], a[184:187], v[116:119], v[156:159]\n\t"
    "buffer_load_dwordx4 v15, %[srdk], s61 offen offset:3072 lds\n\t"
    "v_mfma_f32_16x16x32_bf16 v[156:159], a[188:191], v[124:127], v[156:159]\n\t"
    "s_add_u32 s61, s61, 0x8000\n\t"
    "s_nop 7\n\t"
    "v_max3_f32 v216, v128, v129, v130\n\t"
    "v_max3_f32 v216, v216, v131, v136\n\t"
    "v_max3_f32 v216, v216, v137, v138\n\t"
    "v_max3_f32 v216, v216, v139, v144\n\t"
    "v_max3_f32 v216, v216, v145, v146\n\t"
    "v_max3_f32 v216, v216, v147, v152\n\t"
    "v_max3_f32 v216, v216, v153, v154\n\t"
    "v_max_f32 v216, v216, v155\n\t"
    "v_max3_f32 v217, v132, v133, v134\n\t"
    "v_max3_f32 v217, v217, v135, v140\n\t"
    "v_max3_f32 v217, v217, v141, v142\n\t"
    "v_max3_f32 v217, v217, v143, v148\n\t"
    "v_max3_f32 v217, v217, v149, v150\n\t"
    "v_max3_f32 v217, v217, v151, v156\n\t"
    "v_max3_f32 v217, v217, v157, v158\n\t"
    "v_max_f32 v217, v217, v159\n\t"
    "v_mov_b32 v218, v216\n\t"
    "v_mov_b32 v219, v217\n\t"
    "s_nop 1\n\t"
    "v_permlane32_swap_b32 v216, v218\n\t"
    "v_permlane32_swap_b32 v217, v219\n\t"
    "s_nop 1\n\t"
    "v_max_f32 v216, v216, v218\n\t"
    "v_max_f32 v217, v217, v219\n\t"
    "v_mov_b32 v218, v216\n\t"
    "v_mov_b32 v219, v217\n\t"
    "s_nop 1\n\t"
    "v_permlane16_swap_b32 v216, v218\n\t"
    "v_permlane16_swap_b32 v217, v219\n\t"
    "s_nop 1\n\t"
    "v_max_f32 v216, v216, v218\n\t"
    "v_max_f32 v217, v217, v219\n\t"
    "v_mov_b32 v212, v216\n\t"
    "v_mov_b32 v213, v217\n\t"
    "v_sub_f32 v128, v128, v216\n\t"
    "v_sub_f32 v129, v129, v216\n\t"
    "v_sub_f32 v130, v130, v216\n\t"
    "v_sub_f32 v131, v131, v216\n\t"
    "v_sub_f32 v132, v132, v217\n\t"
    "v_sub_f32 v133, v133, v217\n\t"
    "v_sub_f32 v134, v134, v217\n\t"
    "v_sub_f32 v135, v135, v217\n\t"
    "v_sub_f32 v136, v136, v216\n\t"
    "v_sub_f32 v137, v137, v216\n\t"
    "v_sub_f32 v138, v138, v216\n\t"
    "v_sub_f32 v139, v139, v216\n\t"
    "v_sub_f32 v140, v140, v217\n\t"
    "v_sub_f32 v141, v141, v217\n\t"
    "v_sub_f32 v142, v142, v217\n\t"
    "v_sub_f32 v143, v143, v217\n\t"
    "v_sub_f32 v144, v144, v216\n\t"
    "v_sub_f32 v145, v145, v216\n\t"
    "v_sub_f32 v146, v146, v216\n\t"
    "v_sub_f32 v147, v147, v216\n\t"
    "v_sub_f32 v148, v148, v217\n\t"
    "v_sub_f32 v149, v149, v217\n\t"
    "v_sub_f32 v150, v150, v217\n\t"
    "v_sub_f32 v151, v151, v217\n\t"
    "v_sub_f32 v152, v152, v216\n\t"
    "v_sub_f32 v153, v153, v216\n\t"
    "v_sub_f32 v154, v154, v216\n\t"
    "v_sub_f32 v155, v155, v216\n\t"
    "v_sub_f32 v156, v156, v217\n\t"
    "v_sub_f32 v157, v157, v217\n\t"
    "v_sub_f32 v158, v158, v217\n\t"
    "v_sub_f32 v159, v159, v217\n\t"
    "v_sub_f32 v192, 0, v212\n\t"
    "v_sub_f32 v193, 0, v212\n\t"
    "v_sub_f32 v194, 0, v212\n\t"
    "v_sub_f32 v195, 0, v212\n\t"
    "v_sub_f32 v196, 0, v213\n\t"
    "v_sub_f32 v197, 0, v213\n\t"
    "v_sub_f32 v198, 0, v213\n\t"
    "v_sub_f32 v199, 0, v213\n\t"
    "v_exp_f32 v128, v128\n\t"
    "v_exp_f32 v129, v129\n\t"
    "v_exp_f32 v130, v130\n\t"
    "v_exp_f32 v131, v131\n\t"
    "v_exp_f32 v136, v136\n\t"
    "v_exp_f32 v137, v137\n\t"
    "v_exp_f32 v138, v138\n\t"
    "v_exp_f32 v139, v139\n\t"
    "v_exp_f32 v132, v132\n\t"
    "v_exp_f32 v133, v133\n\t"
    "v_exp_f32 v134, v134\n\t"
    "v_exp_f32 v135, v135\n\t"
    "v_exp_f32 v140, v140\n\t"
    "v_exp_f32 v141, v141\n\t"
    "v_exp_f32 v142, v142\n\t"
    "v_exp_f32 v143, v143\n\t"
    "s_waitcnt lgkmcnt(0)\n\t"
    "v_mfma_f32_16x16x32_bf16 v[160:163], a[128:131], v[64:67], v[192:195]\n\t"
    "v_exp_f32 v144, v144\n\t"
    "v_mfma_f32_16x16x32_bf16 v[160:163], a[132:135], v[72:75], v[160:163]\n\t"
    "v_exp_f32 v145, v145\n\t"
    "v_mfma_f32_16x16x32_bf16 v[160:163], a[136:139], v[80:83], v[160:163]\n\t"
    "ds_read_b128 a[160:163], v32 offset:40960\n\t"
    "ds_read_b128 a[164:167], v33 offset:40960\n\t"
    "v_mfma_f32_16x16x32_bf16 v[160:163], a[140:143], v[88:91], v[160:163]\n\t"
    "v_exp_f32 v146, v146\n\t"
    "v_mfma_f32_16x16x32_bf16 v[160:163], a[144:147], v[96:99], v[160:163]\n\t"
    "ds_read_b128 a[168:171], v34 offset:40960\n\t"
    "ds_read_b128 a[172:175], v35 offset:40960\n\t"
    "v_mfma_f32_16x16x32_bf16 v[160:163], a[148:151], v[104:107], v[160:163]\n\t"
    "v_exp_f32 v147, v147\n\t"
    "v_mfma_f32_16x16x32_bf16 v[160:163], a[152:155], v[112:115], v[160:163]\n\t"
    "ds_read_b128 a[176:179], v32 offset:41216\n\t"
    "ds_read_b128 a[180:183], v33 offset:41216\n\t"
    "v_mfma_f32_16x16x32_bf16 v[160:163], a[156:159], v[120:123], v[160:163]\n\t"
    "v_exp_f32 v152, v152\n\t"
    "v_mfma_f32_16x16x32_bf16 v[164:167], a[128:131], v[68:71], v[196:199]\n\t"
    "ds_read_b128 a[184:187], v34 offset:41216\n\t"
    "ds_read_b128 a[188:191], v35 offset:41216\n\t"
    "v_mfma_f32_16x16x32_bf16 v[164:167], a[132:135], v[76:79], v[164:167]\n\t"
    "v_exp_f32 v153, v153\n\t"
    "v_mfma_f32_16x16x32_bf16 v[164:167], a[136:139], v[84:87], v[164:167]\n\t"
    "v_exp_f32 v154, v154\n\t"
    "v_mfma_f32_16x16x32_bf16 v[164:167], a[140:143], v[92:95], v[164:167]\n\t"
    "v_exp_f32 v155, v155\n\t"
    "v_mfma_f32_16x16x32_bf16 v[164:167], a[144:147], v[100:103], v[164:167]\n\t"
    "v_exp_f32 v148, v148\n\t"
    "v_mfma_f32_16x16x32_bf16 v[164:167], a[148:151], v[108:111], v[164:167]\n\t"
    "v_exp_f32 v149, v149\n\t"
    "v_mfma_f32_16x16x32_bf16 v[164:167], a[152:155], v[116:119], v[164:167]\n\t"
    "v_exp_f32 v150, v150\n\t"
    "v_mfma_f32_16x16x32_bf16 v[164:167], a[156:159], v[124:127], v[164:167]\n\t"
    "v_cvt_pk_bf16_f32 v128, v128, v129\n\t"
    "s_waitcnt lgkmcnt(0)\n\t"
    "v_mfma_f32_16x16x32_bf16 v[168:171], a[160:163], v[64:67], v[192:195]\n\t"
    "v_exp_f32 v151, v151\n\t"
    "v_mfma_f32_16x16x32_bf16 v[168:171], a[164:167], v[72:75], v[168:171]\n\t"
    "v_exp_f32 v156, v156\n\t"
    "v_mfma_f32_16x16x32_bf16 v[168:171], a[168:171], v[80:83], v[168:171]\n\t"
    "ds_read_b128 a[128:131], v32 offset:49152\n\t"
    "ds_read_b128 a[132:135], v33 offset:49152\n\t"
    "v_mfma_f32_16x16x32_bf16 v[168:171], a[172:175], v[88:91], v[168:171]\n\t"
    "v_exp_f32 v157, v157\n\t"
    "v_mfma_f32_16x16x32_bf16 v[168:171], a[176:179], v[96:99], v[168:171]\n\t"
    "ds_read_b128 a[136:139], v34 offset:49152\n\t"
    "ds_read_b128 a[140:143], v35 offset:49152\n\t"
    "v_mfma_f32_16x16x32_bf16 v[168:171], a[180:183], v[104:107], v[168:171]\n\t"
    "v_exp_f32 v158, v158\n\t"
    "v_mfma_f32_16x16x32_bf16 v[168:171], a[184:187], v[112:115], v[168:171]\n\t"
    "ds_read_b128 a[144:147], v32 offset:49408\n\t"
    "ds_read_b128 a[148:151], v33 offset:49408\n\t"
    "v_mfma_f32_16x16x32_bf16 v[168:171], a[188:191], v[120:123], v[168:171]\n\t"
    "v_exp_f32 v159, v159\n\t"
    "v_mfma_f32_16x16x32_bf16 v[172:175], a[160:163], v[68:71], v[196:199]\n\t"
    "ds_read_b128 a[152:155], v34 offset:49408\n\t"
    "ds_read_b128 a[156:159], v35 offset:49408\n\t"
    "v_mfma_f32_16x16x32_bf16 v[172:175], a[164:167], v[76:79], v[172:175]\n\t"
    "v_cvt_pk_bf16_f32 v129, v130, v131\n\t"
    "v_cvt_pk_bf16_f32 v130, v136, v137\n\t"
    "v_mfma_f32_16x16x32_bf16 v[172:175], a[168:171], v[84:87], v[172:175]\n\t"
    "v_cvt_pk_bf16_f32 v131, v138, v139\n\t"
    "v_cvt_pk_bf16_f32 v132, v132, v133\n\t"
    "v_mfma_f32_16x16x32_bf16 v[172:175], a[172:175], v[92:95], v[172:175]\n\t"
    "v_cvt_pk_bf16_f32 v133, v134, v135\n\t"
    "v_cvt_pk_bf16_f32 v134, v140, v141\n\t"
    "v_mfma_f32_16x16x32_bf16 v[172:175], a[176:179], v[100:103], v[172:175]\n\t"
    "v_cvt_pk_bf16_f32 v135, v142, v143\n\t"
    "v_cvt_pk_bf16_f32 v144, v144, v145\n\t"
    "v_mfma_f32_16x16x32_bf16 v[172:175], a[180:183], v[108:111], v[172:175]\n\t"
    "v_cvt_pk_bf16_f32 v145, v146, v147\n\t"
    "v_cvt_pk_bf16_f32 v146, v152, v153\n\t"
    "v_mfma_f32_16x16x32_bf16 v[172:175], a[184:187], v[116:119], v[172:175]\n\t"
    "v_cvt_pk_bf16_f32 v147, v154, v155\n\t"
    "v_cvt_pk_bf16_f32 v148, v148, v149\n\t"
    "v_mfma_f32_16x16x32_bf16 v[172:175], a[188:191], v[124:127], v[172:175]\n\t"
    "v_cvt_pk_bf16_f32 v149, v150, v151\n\t"
    "s_waitcnt lgkmcnt(0)\n\t"
    "v_mfma_f32_16x16x32_bf16 v[176:179], a[128:131], v[64:67], v[192:195]\n\t"
    "v_cvt_pk_bf16_f32 v150, v156, v157\n\t"
    "v_cvt_pk_bf16_f32 v151, v158, v159\n\t"
    "v_mfma_f32_16x16x32_bf16 v[176:179], a[132:135], v[72:75], v[176:179]\n\t"
    "v_max3_f32 v216, v160, v161, v162\n\t"
    "v_max3_f32 v217, v163, v164, v165\n\t"
    "v_mfma_f32_16x16x32_bf16 v[176:179], a[136:139], v[80:83], v[176:179]\n\t"
    "ds_read_b128 a[160:163], v32 offset:57344\n\t"
    "ds_read_b128 a[164:167], v33 offset:57344\n\t"
    "v_mfma_f32_16x16x32_bf16 v[176:179], a[140:143], v[88:91], v[176:179]\n\t"
    "v_max3_f32 v216, v216, v166, v167\n\t"
    "v_max3_f32 v217, v217, v168, v169\n\t"
    "v_mfma_f32_16x16x32_bf16 v[176:179], a[144:147], v[96:99], v[176:179]\n\t"
    "ds_read_b128 a[168:171], v34 offset:57344\n\t"
    "ds_read_b128 a[172:175], v35 offset:57344\n\t"
    "v_mfma_f32_16x16x32_bf16 v[176:179], a[148:151], v[104:107], v[176:179]\n\t"
    "v_max3_f32 v216, v216, v170, v171\n\t"
    "v_max3_f32 v217, v217, v172, v173\n\t"
    "v_mfma_f32_16x16x32_bf16 v[176:179], a[152:155], v[112:115], v[176:179]\n\t"
    "ds_read_b128 a[176:179], v32 offset:57600\n\t"
    "ds_read_b128 a[180:183], v33 offset:57600\n\t"
    "v_mfma_f32_16x16x32_bf16 v[176:179], a[156:159], v[120:123], v[176:179]\n\t"
    "v_max3_f32 v216, v216, v174, v175\n\t"
    "v_mfma_f32_16x16x32_bf16 v[180:183], a[128:131], v[68:71], v[196:199]\n\t"
    "ds_read_b128 a[184:187], v34 offset:57600\n\t"
    "ds_read_b128 a[188:191], v35 offset:57600\n\t"
    "v_mfma_f32_16x16x32_bf16 v[180:183], a[132:135], v[76:79], v[180:183]\n\t"
    "v_mfma_f32_16x16x32_bf16 v[180:183], a[136:139], v[84:87], v[180:183]\n\t"
    "v_mfma_f32_16x16x32_bf16 v[180:183], a[140:143], v[92:95], v[180:183]\n\t"
    "v_mfma_f32_16x16x32_bf16 v[180:183], a[144:147], v[100:103], v[180:183]\n\t"
    "v_max3_f32 v217, v217, v176, v177\n\t"
    "v_max3_f32 v216, v216, v178, v179\n\t"
    "v_mfma_f32_16x16x32_bf16 v[180:183], a[148:151], v[108:111], v[180:183]\n\t"
    "v_mfma_f32_16x16x32_bf16 v[180:183], a[152:155], v[116:119], v[180:183]\n\t"
    "v_mfma_f32_16x16x32_bf16 v[180:183], a[156:159], v[124:127], v[180:183]\n\t"
    "s_waitcnt lgkmcnt(0)\n\t"
    "v_mfma_f32_16x16x32_bf16 v[184:187], a[160:163], v[64:67], v[192:195]\n\t"
    "s_waitcnt vmcnt(0)\n\t"
    "s_barrier\n\t"
    "v_mfma_f32_16x16x32_bf16 v[184:187], a[164:167], v[72:75], v[184:187]\n\t"
    "s_add_u32 m0, s63, 0x18000\n\t"
    "v_mfma_f32_16x16x32_bf16 v[184:187], a[168:171], v[80:83], v[184:187]\n\t"
    "ds_read_b64_tr_b16 a[192:193], v16\n\t"
    "ds_read_b64_tr_b16 a[194:195], v16 offset:8192\n\t"
    "v_mfma_f32_16x16x32_bf16 v[184:187], a[172:175], v[88:91], v[184:187]\n\t"
    "buffer_load_dwordx4 v36, %[srdv], s62 offen lds\n\t"
    "v_mfma_f32_16x16x32_bf16 v[184:187], a[176:179], v[96:99], v[184:187]\n\t"
    "ds_read_b64_tr_b16 a[196:197], v16 offset:16384\n\t"
    "ds_read_b64_tr_b16 a[198:199], v16 offset:24576\n\t"
    "v_mfma_f32_16x16x32_bf16 v[184:187], a[180:183], v[104:107], v[184:187]\n\t"
    "buffer_load_dwordx4 v37, %[srdv], s62 offen offset:1024 lds\n\t"
    "v_max3_f32 v217, v217, v180, v181\n\t"
    "v_mfma_f32_16x16x32_bf16 v[184:187], a[184:187], v[112:115], v[184:187]\n\t"
    "ds_read_b64_tr_b16 a[200:201], v17\n\t"
    "ds_read_b64_tr_b16 a[202:203], v17 offset:8192\n\t"
    "v_mfma_f32_16x16x32_bf16 v[184:187], a[188:191], v[120:123], v[184:187]\n\t"
    "buffer_load_dwordx4 v38, %[srdv], s62 offen offset:2048 lds\n\t"
    "v_max3_f32 v216, v216, v182, v183\n\t"
    "v_mfma_f32_16x16x32_bf16 v[188:191], a[160:163], v[68:71], v[196:199]\n\t"
    "ds_read_b64_tr_b16 a[204:205], v17 offset:16384\n\t"
    "ds_read_b64_tr_b16 a[206:207], v17 offset:24576\n\t"
    "v_mfma_f32_16x16x32_bf16 v[188:191], a[164:167], v[76:79], v[188:191]\n\t"
    "buffer_load_dwordx4 v39, %[srdv], s62 offen offset:3072 lds\n\t"
    "v_mfma_f32_16x16x32_bf16 v[188:191], a[168:171], v[84:87], v[188:191]\n\t"
    "ds_read_b64_tr_b16 a[208:209], v18\n\t"
    "ds_read_b64_tr_b16 a[210:211], v18 offset:8192\n\t"
    "v_mfma_f32_16x16x32_bf16 v[188:191], a[172:175], v[92:95], v[188:191]\n\t"
    "s_add_u32 m0, s63, 0x19000\n\t"
    "s_add_u32 s62, s62, 0x1000\n\t"
    "v_mfma_f32_16x16x32_bf16 v[188:191], a[176:179], v[100:103], v[188:191]\n\t"
    "ds_read_b64_tr_b16 a[212:213], v18 offset:16384\n\t"
    "ds_read_b64_tr_b16 a[214:215], v18 offset:24576\n\t"
    "v_mfma_f32_16x16x32_bf16 v[188:191], a[180:183], v[108:111], v[188:191]\n\t"
    "buffer_load_dwordx4 v36, %[srdv], s62 offen lds\n\t"
    "v_max3_f32 v217, v217, v184, v185\n\t"
    "v_mfma_f32_16x16x32_bf16 v[188:191], a[184:187], v[116:119], v[188:191]\n\t"
    "ds_read_b64_tr_b16 a[216:217], v19\n\t"
    "ds_read_b64_tr_b16 a[218:219], v19 offset:8192\n\t"
    "v_mfma_f32_16x16x32_bf16 v[188:191], a[188:191], v[124:127], v[188:191]\n\t"
    "v_max3_f32 v216, v216, v186, v187\n\t"
    "s_waitcnt lgkmcnt(6)\n\t"
    "v_mfma_f32_16x16x32_bf16 a[0:3], a[192:195], v[128:131], 0\n\t"
    "ds_read_b64_tr_b16 a[220:221], v19 offset:16384\n\t"
    "ds_read_b64_tr_b16 a[222:223], v19 offset:24576\n\t"
    "v_mfma_f32_16x16x32_bf16 a[0:3], a[196:199], v[144:147], a[0:3]\n\t"
    "buffer_load_dwordx4 v37, %[srdv], s62 offen offset:1024 lds\n\t"
    "v_mfma_f32_16x16x32_bf16 a[4:7], a[192:195], v[132:135], 0\n\t"
    "ds_read_b64_tr_b16 a[224:225], v20\n\t"
    "ds_read_b64_tr_b16 a[226:227], v20 offset:8192\n\t"
    "v_mfma_f32_16x16x32_bf16 a[4:7], a[196:199], v[148:151], a[4:7]\n\t"
    "v_max3_f32 v217, v217, v188, v189\n\t"
    "v_max3_f32 v216, v216, v190, v191\n\t"
    "v_mfma_f32_16x16x32_bf16 a[8:11], a[200:203], v[128:131], 0\n\t"
    "ds_read_b64_tr_b16 a[228:229], v20 offset:16384\n\t"
    "ds_read_b64_tr_b16 a[230:231], v20 offset:24576\n\t"
    "v_mfma_f32_16x16x32_bf16 a[8:11], a[204:207], v[144:147], a[8:11]\n\t"
    "v_max_f32 v216, v216, v217\n\t"
    "v_cmp_lt_f32 vcc, s66, v216\n\t"
    "v_mfma_f32_16x16x32_bf16 a[12:15], a[200:203], v[132:135], 0\n\t"
    "ds_read_b64_tr_b16 a[232:233], v21\n\t"
    "ds_read_b64_tr_b16 a[234:235], v21 offset:8192\n\t"
    "v_mfma_f32_16x16x32_bf16 a[12:15], a[204:207], v[148:151], a[12:15]\n\t"
    "s_cbranch_vccnz Lsa2_%=\n\t"
    "Lba1_%=:\n\t"
    "s_waitcnt lgkmcnt(6)\n\t"
    "v_mfma_f32_16x16x32_bf16 a[16:19], a[208:211], v[128:131], 0\n\t"
    "ds_read_b64_tr_b16 a[236:237], v21 offset:16384\n\t"
    "ds_read_b64_tr_b16 a[238:239], v21 offset:24576\n\t"
    "v_mfma_f32_16x16x32_bf16 a[16:19], a[212:215], v[144:147], a[16:19]\n\t"
    "buffer_load_dwordx4 v38, %[srdv], s62 offen offset:2048 lds\n\t"
    "v_mfma_f32_16x16x32_bf16 a[20:23], a[208:211], v[132:135], 0\n\t"
    "ds_read_b64_tr_b16 a[240:241], v22\n\t"
    "ds_read_b64_tr_b16 a[242:243], v22 offset:8192\n\t"
    "v_mfma_f32_16x16x32_bf16 a[20:23], a[212:215], v[148:151], a[20:23]\n\t"
    "buffer_load_dwordx4 v39, %[srdv], s62 offen offset:3072 lds\n\t"
    "v_mfma_f32_16x16x32_bf16 a[24:27], a[216:219], v[128:131], 0\n\t"
    "ds_read_b64_tr_b16 a[244:245], v22 offset:16384\n\t"
    "ds_read_b64_tr_b16 a[246:247], v22 offset:24576\n\t"
    "v_mfma_f32_16x16x32_bf16 a[24:27], a[220:223], v[144:147], a[24:27]\n\t"
    "s_add_u32 s62, s62, 0x7000\n\t"
    "s_add_u32 m0, s63, 0x8000\n\t"
    "v_mfma_f32_16x16x32_bf16 a[28:31], a[216:219], v[132:135], 0\n\t"
    "ds_read_b64_tr_b16 a[248:249], v23\n\t"
    "ds_read_b64_tr_b16 a[250:251], v23 offset:8192\n\t"
    "v_mfma_f32_16x16x32_bf16 a[28:31], a[220:223], v[148:151], a[28:31]\n\t"
    "buffer_load_dwordx4 v8, %[srdk], s61 offen lds\n\t"
    "s_waitcnt lgkmcnt(6)\n\t"
    "v_mfma_f32_16x16x32_bf16 a[32:35], a[224:227], v[128:131], 0\n\t"
    "ds_read_b64_tr_b16 a[252:253], v23 offset:16384\n\t"
    "ds_read_b64_tr_b16 a[254:255], v23 offset:24576\n\t"
    "v_mfma_f32_16x16x32_bf16 a[32:35], a[228:231], v[144:147], a[32:35]\n\t"
    "v_exp_f32 v160, v160\n\t"
    "v_mfma_f32_16x16x32_bf16 a[36:39], a[224:227], v[132:135], 0\n\t"
    "ds_read_b64_tr_b16 a[192:193], v16 offset:256\n\t"
    "ds_read_b64_tr_b16 a[194:195], v16 offset:8448\n\t"
    "v_mfma_f32_16x16x32_bf16 a[36:39], a[228:231], v[148:151], a[36:39]\n\t"
    "v_exp_f32 v161, v161\n\t"
    "v_mfma_f32_16x16x32_bf16 a[40:43], a[232:235], v[128:131], 0\n\t"
    "ds_read_b64_tr_b16 a[196:197], v16 offset:16640\n\t"
    "ds_read_b64_tr_b16 a[198:199], v16 offset:24832\n\t"
    "v_mfma_f32_16x16x32_bf16 a[40:43], a[236:239], v[144:147], a[40:43]\n\t"
    "v_exp_f32 v162, v162\n\t"
    "v_mfma_f32_16x16x32_bf16 a[44:47], a[232:235], v[132:135], 0\n\t"
    "ds_read_b64_tr_b16 a[200:201], v17 offset:256\n\t"
    "ds_read_b64_tr_b16 a[202:203], v17 offset:8448\n\t"
    "v_mfma_f32_16x16x32_bf16 a[44:47], a[236:239], v[148:151], a[44:47]\n\t"
    "buffer_load_dwordx4 v9, %[srdk], s61 offen offset:1024 lds\n\t"
    "s_waitcnt lgkmcnt(6)\n\t"
    "v_mfma_f32_16x16x32_bf16 a[48:51], a[240:243], v[128:131], 0\n\t"
    "ds_read_b64_tr_b16 a[204:205], v17 offset:16640\n\t"
    "ds_read_b64_tr_b16 a[206:207], v17 offset:24832\n\t"
    "v_mfma_f32_16x16x32_bf16 a[48:51], a[244:247], v[144:147], a[48:51]\n\t"
    "v_exp_f32 v163, v163\n\t"
    "v_mfma_f32_16x16x32_bf16 a[52:55], a[240:243], v[132:135], 0\n\t"
    "ds_read_b64_tr_b16 a[208:209], v18 offset:256\n\t"
    "ds_read_b64_tr_b16 a[210:211], v18 offset:8448\n\t"
    "v_mfma_f32_16x16x32_bf16 a[52:55], a[244:247], v[148:151], a[52:55]\n\t"
    "v_exp_f32 v168, v168\n\t"
    "v_mfma_f32_16x16x32_bf16 a[56:59], a[248:251], v[128:131], 0\n\t"
    "ds_read_b64_tr_b16 a[212:213], v18 offset:16640\n\t"
    "ds_read_b64_tr_b16 a[214:215], v18 offset:24832\n\t"
    "v_mfma_f32_16x16x32_bf16 a[56:59], a[252:255], v[144:147], a[56:59]\n\t"
    "v_exp_f32 v169, v169\n\t"
    "v_mfma_f32_16x16x32_bf16 a[60:63], a[248:251], v[132:135], 0\n\t"
    "ds_read_b64_tr_b16 a[216:217], v19 offset:256\n\t"
    "ds_read_b64_tr_b16 a[218:219], v19 offset:8448\n\t"
    "v_mfma_f32_16x16x32_bf16 a[60:63], a[252:255], v[148:151], a[60:63]\n\t"
    "buffer_load_dwordx4 v10, %[srdk], s61 offen offset:2048 lds\n\t"
    "s_waitcnt lgkmcnt(6)\n\t"
    "v_mfma_f32_16x16x32_bf16 a[64:67], a[192:195], v[128:131], 0\n\t"
    "ds_read_b64_tr_b16 a[220:221], v19 offset:16640\n\t"
    "ds_read_b64_tr_b16 a[222:223], v19 offset:24832\n\t"
    "v_mfma_f32_16x16x32_bf16 a[64:67], a[196:199], v[144:147], a[64:67]\n\t"
    "v_exp_f32 v170, v170\n\t"
    "v_mfma_f32_16x16x32_bf16 a[68:71], a[192:195], v[132:135], 0\n\t"
    "ds_read_b64_tr_b16 a[224:225], v20 offset:256\n\t"
    "ds_read_b64_tr_b16 a[226:227], v20 offset:8448\n\t"
    "v_mfma_f32_16x16x32_bf16 a[68:71], a[196:199], v[148:151], a[68:71]\n\t"
    "v_exp_f32 v171, v171\n\t"
    "v_mfma_f32_16x16x32_bf16 a[72:75], a[200:203], v[128:131], 0\n\t"
    "ds_read_b64_tr_b16 a[228:229], v20 offset:16640\n\t"
    "ds_read_b64_tr_b16 a[230:231], v20 offset:24832\n\t"
    "v_mfma_f32_16x16x32_bf16 a[72:75], a[204:207], v[144:147], a[72:75]\n\t"
    "v_exp_f32 v164, v164\n\t"
    "v_mfma_f32_16x16x32_bf16 a[76:79], a[200:203], v[132:135], 0\n\t"
    "ds_read_b64_tr_b16 a[232:233], v21 offset:256\n\t"
    "ds_read_b64_tr_b16 a[234:235], v21 offset:8448\n\t"
    "v_mfma_f32_16x16x32_bf16 a[76:79], a[204:207], v[148:151], a[76:79]\n\t"
    "buffer_load_dwordx4 v11, %[srdk], s61 offen offset:3072 lds\n\t"
    "s_waitcnt lgkmcnt(6)\n\t"
    "v_mfma_f32_16x16x32_bf16 a[80:83], a[208:211], v[128:131], 0\n\t"
    "ds_read_b64_tr_b16 a[236:237], v21 offset:16640\n\t"
    "ds_read_b64_tr_b16 a[238:239], v21 offset:24832\n\t"
    "v_mfma_f32_16x16x32_bf16 a[80:83], a[212:215], v[144:147], a[80:83]\n\t"
    "v_exp_f32 v165, v165\n\t"
    "v_mfma_f32_16x16x32_bf16 a[84:87], a[208:211], v[132:135], 0\n\t"
    "ds_read_b64_tr_b16 a[240:241], v22 offset:256\n\t"
    "ds_read_b64_tr_b16 a[242:243], v22 offset:8448\n\t"
    "v_mfma_f32_16x16x32_bf16 a[84:87], a[212:215], v[148:151], a[84:87]\n\t"
    "v_exp_f32 v166, v166\n\t"
    "v_mfma_f32_16x16x32_bf16 a[88:91], a[216:219], v[128:131], 0\n\t"
    "ds_read_b64_tr_b16 a[244:245], v22 offset:16640\n\t"
    "ds_read_b64_tr_b16 a[246:247], v22 offset:24832\n\t"
    "v_mfma_f32_16x16x32_bf16 a[88:91], a[220:223], v[144:147], a[88:91]\n\t"
    "s_add_u32 m0, s63, 0x9000\n\t"
    "v_mfma_f32_16x16x32_bf16 a[92:95], a[216:219], v[132:135], 0\n\t"
    "ds_read_b64_tr_b16 a[248:249], v23 offset:256\n\t"
    "ds_read_b64_tr_b16 a[250:251], v23 offset:8448\n\t"
    "v_mfma_f32_16x16x32_bf16 a[92:95], a[220:223], v[148:151], a[92:95]\n\t"
    "buffer_load_dwordx4 v12, %[srdk], s61 offen lds\n\t"
    "s_waitcnt lgkmcnt(6)\n\t"
    "v_mfma_f32_16x16x32_bf16 a[96:99], a[224:227], v[128:131], 0\n\t"
    "ds_read_b64_tr_b16 a[252:253], v23 offset:16640\n\t"
    "ds_read_b64_tr_b16 a[254:255], v23 offset:24832\n\t"
    "v_mfma_f32_16x16x32_bf16 a[96:99], a[228:231], v[144:147], a[96:99]\n\t"
    "v_exp_f32 v167, v167\n\t"
    "v_mfma_f32_16x16x32_bf16 a[100:103], a[224:227], v[132:135], 0\n\t"
    "v_exp_f32 v172, v172\n\t"
    "v_mfma_f32_16x16x32_bf16 a[100:103], a[228:231], v[148:151], a[100:103]\n\t"
    "v_exp_f32 v173, v173\n\t"
    "v_mfma_f32_16x16x32_bf16 a[104:107], a[232:235], v[128:131], 0\n\t"
    "ds_read_b128 a[128:131], v32\n\t"
    "ds_read_b128 a[132:135], v33\n\t"
    "v_mfma_f32_16x16x32_bf16 a[104:107], a[236:239], v[144:147], a[104:107]\n\t"
    "v_exp_f32 v174, v174\n\t"
    "v_mfma_f32_16x16x32_bf16 a[108:111], a[232:235], v[132:135], 0\n\t"
    "ds_read_b128 a[136:139], v34\n\t"
    "ds_read_b128 a[140:143], v35\n\t"
    "v_mfma_f32_16x16x32_bf16 a[108:111], a[236:239], v[148:151], a[108:111]\n\t"
    "buffer_load_dwordx4 v13, %[srdk], s61 offen offset:1024 lds\n\t"
    "s_waitcnt lgkmcnt(4)\n\t"
    "v_mfma_f32_16x16x32_bf16 a[112:115], a[240:243], v[128:131], 0\n\t"
    "ds_read_b128 a[144:147], v32 offset:256\n\t"
    "ds_read_b128 a[148:151], v33 offset:256\n\t"
    "v_mfma_f32_16x16x32_bf16 a[112:115], a[244:247], v[144:147], a[112:115]\n\t"
    "v_exp_f32 v175, v175\n\t"
    "v_mfma_f32_16x16x32_bf16 a[116:119], a[240:243], v[132:135], 0\n\t"
    "ds_read_b128 a[152:155], v34 offset:256\n\t"
    "ds_read_b128 a[156:159], v35 offset:256\n\t"
    "v_mfma_f32_16x16x32_bf16 a[116:119], a[244:247], v[148:151], a[116:119]\n\t"
    "v_mfma_f32_16x16x32_bf16 a[120:123], a[248:251], v[128:131], 0\n\t"
    "v_mfma_f32_16x16x32_bf16 a[120:123], a[252:255], v[144:147], a[120:123]\n\t"
    "buffer_load_dwordx4 v14, %[srdk], s61 offen offset:2048 lds\n\t"
    "v_mfma_f32_16x16x32_bf16 a[124:127], a[248:251], v[132:135], 0\n\t"
    "v_mfma_f32_16x16x32_bf16 a[124:127], a[252:255], v[148:151], a[124:127]\n\t"
    "buffer_load_dwordx4 v15, %[srdk], s61 offen offset:3072 lds\n\t"
    "v_mfma_f32_16x16x32_bf16 v[200:203], v[208:211], v[128:131], 0\n\t"
    "s_add_u32 s61, s61, 0x8000\n\t"
    "v_mfma_f32_16x16x32_bf16 v[200:203], v[208:211], v[144:147], v[200:203]\n\t"
    "buffer_load_dword v7, v6, %[srdt], 0 offen\n\t"
    "v_mfma_f32_16x16x32_bf16 v[204:207], v[208:211], v[132:135], 0\n\t"
    "v_mfma_f32_16x16x32_bf16 v[204:207], v[208:211], v[148:151], v[204:207]\n\t"
    "s_mov_b32 s64, 14\n\t"
    "LLOOP_%=:\n\t"
    "s_waitcnt lgkmcnt(0)\n\t"
    "v_mfma_f32_16x16x32_bf16 v[128:131], a[128:131], v[64:67], v[192:195]\n\t"
    "v_exp_f32 v176, v176\n\t"
    "v_mfma_f32_16x16x32_bf16 v[128:131], a[132:135], v[72:75], v[128:131]\n\t"
    "s_cmp_lg_u32 s65, 0\n\t"
    "s_cbranch_scc1 Lsb4_%=\n\t"
    "Lbb3_%=:\n\t"
    "v_mfma_f32_16x16x32_bf16 v[128:131], a[136:139], v[80:83], v[128:131]\n\t"
    "ds_read_b128 a[160:163], v32 offset:8192\n\t"
    "ds_read_b128 a[164:167], v33 offset:8192\n\t"
    "v_mfma_f32_16x16x32_bf16 v[128:131], a[140:143], v[88:91], v[128:131]\n\t"
    "v_exp_f32 v177, v177\n\t"
    "v_mfma_f32_16x16x32_bf16 v[128:131], a[144:147], v[96:99], v[128:131]\n\t"
    "ds_read_b128 a[168:171], v34 offset:8192\n\t"
    "ds_read_b128 a[172:175], v35 offset:8192\n\t"
    "v_mfma_f32_16x16x32_bf16 v[128:131], a[148:151], v[104:107], v[128:131]\n\t"
    "v_exp_f32 v178, v178\n\t"
    "v_mfma_f32_16x16x32_bf16 v[128:131], a[152:155], v[112:115], v[128:131]\n\t"
    "ds_read_b128 a[176:179], v32 offset:8448\n\t"
    "ds_read_b128 a[180:183], v33 offset:8448\n\t"
    "v_mfma_f32_16x16x32_bf16 v[128:131], a[156:159], v[120:123], v[128:131]\n\t"
    "v_exp_f32 v179, v179\n\t"
    "v_mfma_f32_16x16x32_bf16 v[132:135], a[128:131], v[68:71], v[196:199]\n\t"
    "ds_read_b128 a[184:187], v34 offset:8448\n\t"
    "ds_read_b128 a[188:191], v35 offset:8448\n\t"
    "v_mfma_f32_16x16x32_bf16 v[132:135], a[132:135], v[76:79], v[132:135]\n\t"
    "v_exp_f32 v184, v184\n\t"
    "v_mfma_f32_16x16x32_bf16 v[132:135], a[136:139], v[84:87], v[132:135]\n\t"
    "v_exp_f32 v185, v185\n\t"
    "v_mfma_f32_16x16x32_bf16 v[132:135], a[140:143], v[92:95], v[132:135]\n\t"
    "v_exp_f32 v186, v186\n\t"
    "v_mfma_f32_16x16x32_bf16 v[132:135], a[144:147], v[100:103], v[132:135]\n\t"
    "v_exp_f32 v187, v187\n\t"
    "v_mfma_f32_16x16x32_bf16 v[132:135], a[148:151], v[108:111], v[132:135]\n\t"
    "v_exp_f32 v180, v180\n\t"
    "v_mfma_f32_16x16x32_bf16 v[132:135], a[152:155], v[116:119], v[132:135]\n\t"
    "v_exp_f32 v181, v181\n\t"
    "v_mfma_f32_16x16x32_bf16 v[132:135], a[156:159], v[124:127], v[132:135]\n\t"
    "v_cvt_pk_bf16_f32 v160, v160, v161\n\t"
    "s_waitcnt lgkmcnt(0)\n\t"
    "v_mfma_f32_16x16x32_bf16 v[136:139], a[160:163], v[64:67], v[192:195]\n\t"
    "v_exp_f32 v182, v182\n\t"
    "v_mfma_f32_16x16x32_bf16 v[136:139], a[164:167], v[72:75], v[136:139]\n\t"
    "v_exp_f32 v183, v183\n\t"
    "v_mfma_f32_16x16x32_bf16 v[136:139], a[168:171], v[80:83], v[136:139]\n\t"
    "ds_read_b128 a[128:131], v32 offset:16384\n\t"
    "ds_read_b128 a[132:135], v33 offset:16384\n\t"
    "v_mfma_f32_16x16x32_bf16 v[136:139], a[172:175], v[88:91], v[136:139]\n\t"
    "v_exp_f32 v188, v188\n\t"
    "v_mfma_f32_16x16x32_bf16 v[136:139], a[176:179], v[96:99], v[136:139]\n\t"
    "ds_read_b128 a[136:139], v34 offset:16384\n\t"
    "ds_read_b128 a[140:143], v35 offset:16384\n\t"
    "v_mfma_f32_16x16x32_bf16 v[136:139], a[180:183], v[104:107], v[136:139]\n\t"
    "v_exp_f32 v189, v189\n\t"
    "v_mfma_f32_16x16x32_bf16 v[136:139], a[184:187], v[112:115], v[136:139]\n\t"
    "ds_read_b128 a[144:147], v32 offset:16640\n\t"
    "ds_read_b128 a[148:151], v33 offset:16640\n\t"
    "v_mfma_f32_16x16x32_bf16 v[136:139], a[188:191], v[120:123], v[136:139]\n\t"
    "v_exp_f32 v190, v190\n\t"
    "v_mfma_f32_16x16x32_bf16 v[140:143], a[160:163], v[68:71], v[196:199]\n\t"
    "ds_read_b128 a[152:155], v34 offset:16640\n\t"
    "ds_read_b128 a[156:159], v35 offset:16640\n\t"
    "v_mfma_f32_16x16x32_bf16 v[140:143], a[164:167], v[76:79], v[140:143]\n\t"
    "v_exp_f32 v191, v191\n\t"
    "v_mfma_f32_16x16x32_bf16 v[140:143], a[168:171], v[84:87], v[140:143]\n\t"
    "v_cvt_pk_bf16_f32 v161, v162, v163\n\t"
    "v_cvt_pk_bf16_f32 v162, v168, v169\n\t"
    "v_mfma_f32_16x16x32_bf16 v[140:143], a[172:175], v[92:95], v[140:143]\n\t"
    "v_cvt_pk_bf16_f32 v163, v170, v171\n\t"
    "v_cvt_pk_bf16_f32 v164, v164, v165\n\t"
    "v_mfma_f32_16x16x32_bf16 v[140:143], a[176:179], v[100:103], v[140:143]\n\t"
    "v_cvt_pk_bf16_f32 v165, v166, v167\n\t"
    "v_cvt_pk_bf16_f32 v166, v172, v173\n\t"
    "v_mfma_f32_16x16x32_bf16 v[140:143], a[180:183], v[108:111], v[140:143]\n\t"
    "v_cvt_pk_bf16_f32 v167, v174, v175\n\t"
    "v_cvt_pk_bf16_f32 v176, v176, v177\n\t"
    "v_mfma_f32_16x16x32_bf16 v[140:143], a[184:187], v[116:119], v[140:143]\n\t"
    "v_cvt_pk_bf16_f32 v177, v178, v179\n\t"
    "v_cvt_pk_bf16_f32 v178, v184, v185\n\t"
    "v_mfma_f32_16x16x32_bf16 v[140:143], a[188:191], v[124:127], v[140:143]\n\t"
    "v_cvt_pk_bf16_f32 v179, v186, v187\n\t"
    "s_waitcnt lgkmcnt(0)\n\t"
    "v_mfma_f32_16x16x32_bf16 v[144:147], a[128:131], v[64:67], v[192:195]\n\t"
    "v_cvt_pk_bf16_f32 v180, v180, v181\n\t"
    "v_cvt_pk_bf16_f32 v181, v182, v183\n\t"
    "v_mfma_f32_16x16x32_bf16 v[144:147], a[132:135], v[72:75], v[144:147]\n\t"
    "v_cvt_pk_bf16_f32 v182, v188, v189\n\t"
    "v_cvt_pk_bf16_f32 v183, v190, v191\n\t"
    "v_mfma_f32_16x16x32_bf16 v[144:147], a[136:139], v[80:83], v[144:147]\n\t"
    "ds_read_b128 a[160:163], v32 offset:24576\n\t"
    "ds_read_b128 a[164:167], v33 offset:24576\n\t"
    "v_mfma_f32_16x16x32_bf16 v[144:147], a[140:143], v[88:91], v[144:147]\n\t"
    "v_max3_f32 v216, v128, v129, v130\n\t"
    "v_max3_f32 v217, v131, v132, v133\n\t"
    "v_mfma_f32_16x16x32_bf16 v[144:147], a[144:147], v[96:99], v[144:147]\n\t"
    "ds_read_b128 a[168:171], v34 offset:24576\n\t"
    "ds_read_b128 a[172:175], v35 offset:24576\n\t"
    "v_mfma_f32_16x16x32_bf16 v[144:147], a[148:151], v[104:107], v[144:147]\n\t"
    "v_max3_f32 v216, v216, v134, v135\n\t"
    "v_max3_f32 v217, v217, v136, v137\n\t"
    "v_mfma_f32_16x16x32_bf16 v[144:147], a[152:155], v[112:115], v[144:147]\n\t"
    "ds_read_b128 a[176:179], v32 offset:24832\n\t"
    "ds_read_b128 a[180:183], v33 offset:24832\n\t"
    "v_mfma_f32_16x16x32_bf16 v[144:147], a[156:159], v[120:123], v[144:147]\n\t"
    "v_max3_f32 v216, v216, v138, v139\n\t"
    "v_max3_f32 v217, v217, v140, v141\n\t"
    "v_mfma_f32_16x16x32_bf16 v[148:151], a[128:131], v[68:71], v[196:199]\n\t"
    "ds_read_b128 a[184:187], v34 offset:24832\n\t"
    "ds_read_b128 a[188:191], v35 offset:24832\n\t"
    "v_mfma_f32_16x16x32_bf16 v[148:151], a[132:135], v[76:79], v[148:151]\n\t"
    "v_max3_f32 v216, v216, v142, v143\n\t"
    "v_mfma_f32_16x16x32_bf16 v[148:151], a[136:139], v[84:87], v[148:151]\n\t"
    "v_mfma_f32_16x16x32_bf16 v[148:151], a[140:143], v[92:95], v[148:151]\n\t"
    "v_mfma_f32_16x16x32_bf16 v[148:151], a[144:147], v[100:103], v[148:151]\n\t"
    "v_max3_f32 v217, v217, v144, v145\n\t"
    "v_max3_f32 v216, v216, v146, v147\n\t"
    "v_mfma_f32_16x16x32_bf16 v[148:151], a[148:151], v[108:111], v[148:151]\n\t"
    "v_mfma_f32_16x16x32_bf16 v[148:151], a[152:155], v[116:119], v[148:151]\n\t"
    "v_add_u32 v6, %[tstr], v6\n\t"
    "v_mfma_f32_16x16x32_bf16 v[148:151], a[156:159], v[124:127], v[148:151]\n\t"
    "s_waitcnt lgkmcnt(0)\n\t"
    "v_mfma_f32_16x16x32_bf16 v[152:155], a[160:163], v[64:67], v[192:195]\n\t"
    "s_waitcnt vmcnt(1)\n\t"
    "s_barrier\n\t"
    "v_mfma_f32_16x16x32_bf16 v[152:155], a[164:167], v[72:75], v[152:155]\n\t"
    "s_add_u32 m0, s63, 0x10000\n\t"
    "v_mfma_f32_16x16x32_bf16 v[152:155], a[168:171], v[80:83], v[152:155]\n\t"
    "ds_read_b64_tr_b16 a[192:193], v16 offset:32768\n\t"
    "ds_read_b64_tr_b16 a[194:195], v16 offset:40960\n\t"
    "v_mfma_f32_16x16x32_bf16 v[152:155], a[172:175], v[88:91], v[152:155]\n\t"
    "buffer_load_dwordx4 v36, %[srdv], s62 offen lds\n\t"
    "v_mfma_f32_16x16x32_bf16 v[152:155], a[176:179], v[96:99], v[152:155]\n\t"
    "ds_read_b64_tr_b16 a[196:197], v16 offset:49152\n\t"
    "ds_read_b64_tr_b16 a[198:199], v16 offset:57344\n\t"
    "v_mfma_f32_16x16x32_bf16 v[152:155], a[180:183], v[104:107], v[152:155]\n\t"
    "buffer_load_dwordx4 v37, %[srdv], s62 offen offset:1024 lds\n\t"
    "v_max3_f32 v217, v217, v148, v149\n\t"
    "v_mfma_f32_16x16x32_bf16 v[152:155], a[184:187], v[112:115], v[152:155]\n\t"
    "ds_read_b64_tr_b16 a[200:201], v17 offset:32768\n\t"
    "ds_read_b64_tr_b16 a[202:203], v17 offset:40960\n\t"
    "v_mfma_f32_16x16x32_bf16 v[152:155], a[188:191], v[120:123], v[152:155]\n\t"
    "buffer_load_dwordx4 v38, %[srdv], s62 offen offset:2048 lds\n\t"
    "v_max3_f32 v216, v216, v150, v151\n\t"
    "v_mfma_f32_16x16x32_bf16 v[156:159], a[160:163], v[68:71], v[196:199]\n\t"
    "ds_read_b64_tr_b16 a[204:205], v17 offset:49152\n\t"
    "ds_read_b64_tr_b16 a[206:207], v17 offset:57344\n\t"
    "v_mfma_f32_16x16x32_bf16 v[156:159], a[164:167], v[76:79], v[156:159]\n\t"
    "buffer_load_dwordx4 v39, %[srdv], s62 offen offset:3072 lds\n\t"
    "v_mfma_f32_16x16x32_bf16 v[156:159], a[168:171], v[84:87], v[156:159]\n\t"
    "ds_read_b64_tr_b16 a[208:209], v18 offset:32768\n\t"
    "ds_read_b64_tr_b16 a[210:211], v18 offset:40960\n\t"
    "v_mfma_f32_16x16x32_bf16 v[156:159], a[172:175], v[92:95], v[156:159]\n\t"
    "s_add_u32 m0, s63, 0x11000\n\t"
    "s_add_u32 s62, s62, 0x1000\n\t"
    "v_mfma_f32_16x16x32_bf16 v[156:159], a[176:179], v[100:103], v[156:159]\n\t"
    "ds_read_b64_tr_b16 a[212:213], v18 offset:49152\n\t"
    "ds_read_b64_tr_b16 a[214:215], v18 offset:57344\n\t"
    "v_mfma_f32_16x16x32_bf16 v[156:159], a[180:183], v[108:111], v[156:159]\n\t"
    "buffer_load_dwordx4 v36, %[srdv], s62 offen lds\n\t"
    "v_max3_f32 v217, v217, v152, v153\n\t"
    "v_mfma_f32_16x16x32_bf16 v[156:159], a[184:187], v[116:119], v[156:159]\n\t"
    "ds_read_b64_tr_b16 a[216:217], v19 offset:32768\n\t"
    "ds_read_b64_tr_b16 a[218:219], v19 offset:40960\n\t"
    "v_mfma_f32_16x16x32_bf16 v[156:159], a[188:191], v[124:127], v[156:159]\n\t"
    "v_max3_f32 v216, v216, v154, v155\n\t"
    "s_waitcnt lgkmcnt(6)\n\t"
    "v_mfma_f32_16x16x32_bf16 a[0:3], a[192:195], v[160:163], a[0:3]\n\t"
    "ds_read_b64_tr_b16 a[220:221], v19 offset:49152\n\t"
    "ds_read_b64_tr_b16 a[222:223], v19 offset:57344\n\t"
    "v_mfma_f32_16x16x32_bf16 a[0:3], a[196:199], v[176:179], a[0:3]\n\t"
    "buffer_load_dwordx4 v37, %[srdv], s62 offen offset:1024 lds\n\t"
    "v_mfma_f32_16x16x32_bf16 a[4:7], a[192:195], v[164:167], a[4:7]\n\t"
    "ds_read_b64_tr_b16 a[224:225], v20 offset:32768\n\t"
    "ds_read_b64_tr_b16 a[226:227], v20 offset:40960\n\t"
    "v_mfma_f32_16x16x32_bf16 a[4:7], a[196:199], v[180:183], a[4:7]\n\t"
    "v_max3_f32 v217, v217, v156, v157\n\t"
    "v_max3_f32 v216, v216, v158, v159\n\t"
    "v_mfma_f32_16x16x32_bf16 a[8:11], a[200:203], v[160:163], a[8:11]\n\t"
    "ds_read_b64_tr_b16 a[228:229], v20 offset:49152\n\t"
    "ds_read_b64_tr_b16 a[230:231], v20 offset:57344\n\t"
    "v_mfma_f32_16x16x32_bf16 a[8:11], a[204:207], v[176:179], a[8:11]\n\t"
    "v_max_f32 v216, v216, v217\n\t"
    "v_cmp_lt_f32 vcc, s66, v216\n\t"
    "v_mfma_f32_16x16x32_bf16 a[12:15], a[200:203], v[164:167], a[12:15]\n\t"
    "ds_read_b64_tr_b16 a[232:233], v21 offset:32768\n\t"
    "ds_read_b64_tr_b16 a[234:235], v21 offset:40960\n\t"
    "v_mfma_f32_16x16x32_bf16 a[12:15], a[204:207], v[180:183], a[12:15]\n\t"
    "s_cbranch_vccnz Lsa6_%=\n\t"
    "Lba5_%=:\n\t"
    "s_waitcnt lgkmcnt(6)\n\t"
    "v_mfma_f32_16x16x32_bf16 a[16:19], a[208:211], v[160:163], a[16:19]\n\t"
    "ds_read_b64_tr_b16 a[236:237], v21 offset:49152\n\t"
    "ds_read_b64_tr_b16 a[238:239], v21 offset:57344\n\t"
    "v_mfma_f32_16x16x32_bf16 a[16:19], a[212:215], v[176:179], a[16:19]\n\t"
    "buffer_load_dwordx4 v38, %[srdv], s62 offen offset:2048 lds\n\t"
    "v_mfma_f32_16x16x32_bf16 a[20:23], a[208:211], v[164:167], a[20:23]\n\t"
    "ds_read_b64_tr_b16 a[240:241], v22 offset:32768\n\t"
    "ds_read_b64_tr_b16 a[242:243], v22 offset:40960\n\t"
    "v_mfma_f32_16x16x32_bf16 a[20:23], a[212:215], v[180:183], a[20:23]\n\t"
    "buffer_load_dwordx4 v39, %[srdv], s62 offen offset:3072 lds\n\t"
    "v_mfma_f32_16x16x32_bf16 a[24:27], a[216:219], v[160:163], a[24:27]\n\t"
    "ds_read_b64_tr_b16 a[244:245], v22 offset:49152\n\t"
    "ds_read_b64_tr_b16 a[246:247], v22 offset:57344\n\t"
    "v_mfma_f32_16x16x32_bf16 a[24:27], a[220:223], v[176:179], a[24:27]\n\t"
    "s_add_u32 s62, s62, 0x7000\n\t"
    "s_add_u32 m0, s63, 0x0\n\t"
    "v_mfma_f32_16x16x32_bf16 a[28:31], a[216:219], v[164:167], a[28:31]\n\t"
    "ds_read_b64_tr_b16 a[248:249], v23 offset:32768\n\t"
    "ds_read_b64_tr_b16 a[250:251], v23 offset:40960\n\t"
    "v_mfma_f32_16x16x32_bf16 a[28:31], a[220:223], v[180:183], a[28:31]\n\t"
    "buffer_load_dwordx4 v8, %[srdk], s61 offen lds\n\t"
    "s_waitcnt lgkmcnt(6)\n\t"
    "v_mfma_f32_16x16x32_bf16 a[32:35], a[224:227], v[160:163], a[32:35]\n\t"
    "ds_read_b64_tr_b16 a[252:253], v23 offset:49152\n\t"
    "ds_read_b64_tr_b16 a[254:255], v23 offset:57344\n\t"
    "v_mfma_f32_16x16x32_bf16 a[32:35], a[228:231], v[176:179], a[32:35]\n\t"
    "v_exp_f32 v128, v128\n\t"
    "v_mfma_f32_16x16x32_bf16 a[36:39], a[224:227], v[164:167], a[36:39]\n\t"
    "ds_read_b64_tr_b16 a[192:193], v16 offset:33024\n\t"
    "ds_read_b64_tr_b16 a[194:195], v16 offset:41216\n\t"
    "v_mfma_f32_16x16x32_bf16 a[36:39], a[228:231], v[180:183], a[36:39]\n\t"
    "v_exp_f32 v129, v129\n\t"
    "v_mfma_f32_16x16x32_bf16 a[40:43], a[232:235], v[160:163], a[40:43]\n\t"
    "ds_read_b64_tr_b16 a[196:197], v16 offset:49408\n\t"
    "ds_read_b64_tr_b16 a[198:199], v16 offset:57600\n\t"
    "v_mfma_f32_16x16x32_bf16 a[40:43], a[236:239], v[176:179], a[40:43]\n\t"
    "v_exp_f32 v130, v130\n\t"
    "v_mfma_f32_16x16x32_bf16 a[44:47], a[232:235], v[164:167], a[44:47]\n\t"
    "ds_read_b64_tr_b16 a[200:201], v17 offset:33024\n\t"
    "ds_read_b64_tr_b16 a[202:203], v17 offset:41216\n\t"
    "v_mfma_f32_16x16x32_bf16 a[44:47], a[236:239], v[180:183], a[44:47]\n\t"
    "buffer_load_dwordx4 v9, %[srdk], s61 offen offset:1024 lds\n\t"
    "s_waitcnt lgkmcnt(6)\n\t"
    "v_mfma_f32_16x16x32_bf16 a[48:51], a[240:243], v[160:163], a[48:51]\n\t"
    "ds_read_b64_tr_b16 a[204:205], v17 offset:49408\n\t"
    "ds_read_b64_tr_b16 a[206:207], v17 offset:57600\n\t"
    "v_mfma_f32_16x16x32_bf16 a[48:51], a[244:247], v[176:179], a[48:51]\n\t"
    "v_exp_f32 v131, v131\n\t"
    "v_mfma_f32_16x16x32_bf16 a[52:55], a[240:243], v[164:167], a[52:55]\n\t"
    "ds_read_b64_tr_b16 a[208:209], v18 offset:33024\n\t"
    "ds_read_b64_tr_b16 a[210:211], v18 offset:41216\n\t"
    "v_mfma_f32_16x16x32_bf16 a[52:55], a[244:247], v[180:183], a[52:55]\n\t"
    "v_exp_f32 v136, v136\n\t"
    "v_mfma_f32_16x16x32_bf16 a[56:59], a[248:251], v[160:163], a[56:59]\n\t"
    "ds_read_b64_tr_b16 a[212:213], v18 offset:49408\n\t"
    "ds_read_b64_tr_b16 a[214:215], v18 offset:57600\n\t"
    "v_mfma_f32_16x16x32_bf16 a[56:59], a[252:255], v[176:179], a[56:59]\n\t"
    "v_exp_f32 v137, v137\n\t"
    "v_mfma_f32_16x16x32_bf16 a[60:63], a[248:251], v[164:167], a[60:63]\n\t"
    "ds_read_b64_tr_b16 a[216:217], v19 offset:33024\n\t"
    "ds_read_b64_tr_b16 a[218:219], v19 offset:41216\n\t"
    "v_mfma_f32_16x16x32_bf16 a[60:63], a[252:255], v[180:183], a[60:63]\n\t"
    "buffer_load_dwordx4 v10, %[srdk], s61 offen offset:2048 lds\n\t"
    "s_waitcnt lgkmcnt(6)\n\t"
    "v_mfma_f32_16x16x32_bf16 a[64:67], a[192:195], v[160:163], a[64:67]\n\t"
    "ds_read_b64_tr_b16 a[220:221], v19 offset:49408\n\t"
    "ds_read_b64_tr_b16 a[222:223], v19 offset:57600\n\t"
    "v_mfma_f32_16x16x32_bf16 a[64:67], a[196:199], v[176:179], a[64:67]\n\t"
    "v_exp_f32 v138, v138\n\t"
    "v_mfma_f32_16x16x32_bf16 a[68:71], a[192:195], v[164:167], a[68:71]\n\t"
    "ds_read_b64_tr_b16 a[224:225], v20 offset:33024\n\t"
    "ds_read_b64_tr_b16 a[226:227], v20 offset:41216\n\t"
    "v_mfma_f32_16x16x32_bf16 a[68:71], a[196:199], v[180:183], a[68:71]\n\t"
    "v_exp_f32 v139, v139\n\t"
    "v_mfma_f32_16x16x32_bf16 a[72:75], a[200:203], v[160:163], a[72:75]\n\t"
    "ds_read_b64_tr_b16 a[228:229], v20 offset:49408\n\t"
    "ds_read_b64_tr_b16 a[230:231], v20 offset:57600\n\t"
    "v_mfma_f32_16x16x32_bf16 a[72:75], a[204:207], v[176:179], a[72:75]\n\t"
    "v_exp_f32 v132, v132\n\t"
    "v_mfma_f32_16x16x32_bf16 a[76:79], a[200:203], v[164:167], a[76:79]\n\t"
    "ds_read_b64_tr_b16 a[232:233], v21 offset:33024\n\t"
    "ds_read_b64_tr_b16 a[234:235], v21 offset:41216\n\t"
    "v_mfma_f32_16x16x32_bf16 a[76:79], a[204:207], v[180:183], a[76:79]\n\t"
    "buffer_load_dwordx4 v11, %[srdk], s61 offen offset:3072 lds\n\t"
    "s_waitcnt lgkmcnt(6)\n\t"
    "v_mfma_f32_16x16x32_bf16 a[80:83], a[208:211], v[160:163], a[80:83]\n\t"
    "ds_read_b64_tr_b16 a[236:237], v21 offset:49408\n\t"
    "ds_read_b64_tr_b16 a[238:239], v21 offset:57600\n\t"
    "v_mfma_f32_16x16x32_bf16 a[80:83], a[212:215], v[176:179], a[80:83]\n\t"
    "v_exp_f32 v133, v133\n\t"
    "v_mfma_f32_16x16x32_bf16 a[84:87], a[208:211], v[164:167], a[84:87]\n\t"
    "ds_read_b64_tr_b16 a[240:241], v22 offset:33024\n\t"
    "ds_read_b64_tr_b16 a[242:243], v22 offset:41216\n\t"
    "v_mfma_f32_16x16x32_bf16 a[84:87], a[212:215], v[180:183], a[84:87]\n\t"
    "v_exp_f32 v134, v134\n\t"
    "v_mfma_f32_16x16x32_bf16 a[88:91], a[216:219], v[160:163], a[88:91]\n\t"
    "ds_read_b64_tr_b16 a[244:245], v22 offset:49408\n\t"
    "ds_read_b64_tr_b16 a[246:247], v22 offset:57600\n\t"
    "v_mfma_f32_16x16x32_bf16 a[88:91], a[220:223], v[176:179], a[88:91]\n\t"
    "s_add_u32 m0, s63, 0x1000\n\t"
    "v_mfma_f32_16x16x32_bf16 a[92:95], a[216:219], v[164:167], a[92:95]\n\t"
    "ds_read_b64_tr_b16 a[248:249], v23 offset:33024\n\t"
    "ds_read_b64_tr_b16 a[250:251], v23 offset:41216\n\t"
    "v_mfma_f32_16x16x32_bf16 a[92:95], a[220:223], v[180:183], a[92:95]\n\t"
    "buffer_load_dwordx4 v12, %[srdk], s61 offen lds\n\t"
    "s_waitcnt lgkmcnt(6)\n\t"
    "v_mfma_f32_16x16x32_bf16 a[96:99], a[224:227], v[160:163], a[96:99]\n\t"
    "ds_read_b64_tr_b16 a[252:253], v23 offset:49408\n\t"
    "ds_read_b64_tr_b16 a[254:255], v23 offset:57600\n\t"
    "v_mfma_f32_16x16x32_bf16 a[96:99], a[228:231], v[176:179], a[96:99]\n\t"
    "v_exp_f32 v135, v135\n\t"
    "v_mfma_f32_16x16x32_bf16 a[100:103], a[224:227], v[164:167], a[100:103]\n\t"
    "v_exp_f32 v140, v140\n\t"
    "v_mfma_f32_16x16x32_bf16 a[100:103], a[228:231], v[180:183], a[100:103]\n\t"
    "v_exp_f32 v141, v141\n\t"
    "v_mfma_f32_16x16x32_bf16 a[104:107], a[232:235], v[160:163], a[104:107]\n\t"
    "ds_read_b128 a[128:131], v32 offset:32768\n\t"
    "ds_read_b128 a[132:135], v33 offset:32768\n\t"
    "v_mfma_f32_16x16x32_bf16 a[104:107], a[236:239], v[176:179], a[104:107]\n\t"
    "v_exp_f32 v142, v142\n\t"
    "v_mfma_f32_16x16x32_bf16 a[108:111], a[232:235], v[164:167], a[108:111]\n\t"
    "ds_read_b128 a[136:139], v34 offset:32768\n\t"
    "ds_read_b128 a[140:143], v35 offset:32768\n\t"
    "v_mfma_f32_16x16x32_bf16 a[108:111], a[236:239], v[180:183], a[108:111]\n\t"
    "buffer_load_dwordx4 v13, %[srdk], s61 offen offset:1024 lds\n\t"
    "s_waitcnt lgkmcnt(4)\n\t"
    "v_mfma_f32_16x16x32_bf16 a[112:115], a[240:243], v[160:163], a[112:115]\n\t"
    "ds_read_b128 a[144:147], v32 offset:33024\n\t"
    "ds_read_b128 a[148:151], v33 offset:33024\n\t"
    "v_mfma_f32_16x16x32_bf16 a[112:115], a[244:247], v[176:179], a[112:115]\n\t"
    "v_exp_f32 v143, v143\n\t"
    "v_mfma_f32_16x16x32_bf16 a[116:119], a[240:243], v[164:167], a[116:119]\n\t"
    "ds_read_b128 a[152:155], v34 offset:33024\n\t"
    "ds_read_b128 a[156:159], v35 offset:33024\n\t"
    "v_mfma_f32_16x16x32_bf16 a[116:119], a[244:247], v[180:183], a[116:119]\n\t"
    "v_mfma_f32_16x16x32_bf16 a[120:123], a[248:251], v[160:163], a[120:123]\n\t"
    "v_mfma_f32_16x16x32_bf16 a[120:123], a[252:255], v[176:179], a[120:123]\n\t"
    "buffer_load_dwordx4 v14, %[srdk], s61 offen offset:2048 lds\n\t"
    "v_mfma_f32_16x16x32_bf16 a[124:127], a[248:251], v[164:167], a[124:127]\n\t"
    "v_mfma_f32_16x16x32_bf16 a[124:127], a[252:255], v[180:183], a[124:127]\n\t"
    "buffer_load_dwordx4 v15, %[srdk], s61 offen offset:3072 lds\n\t"
    "v_mfma_f32_16x16x32_bf16 v[200:203], v[208:211], v[160:163], v[200:203]\n\t"
    "s_add_u32 s61, s61, 0x8000\n\t"
    "v_mfma_f32_16x16x32_bf16 v[200:203], v[208:211], v[176:179], v[200:203]\n\t"
    "buffer_load_dword v7, v6, %[srdt], 0 offen\n\t"
    "v_mfma_f32_16x16x32_bf16 v[204:207], v[208:211], v[164:167], v[204:207]\n\t"
    "v_mfma_f32_16x16x32_bf16 v[204:207], v[208:211], v[180:183], v[204:207]\n\t"
    "s_waitcnt lgkmcnt(0)\n\t"
    "v_mfma_f32_16x16x32_bf16 v[160:163], a[128:131], v[64:67], v[192:195]\n\t"
    "v_exp_f32 v144, v144\n\t"
    "v_mfma_f32_16x16x32_bf16 v[160:163], a[132:135], v[72:75], v[160:163]\n\t"
    "s_cmp_lg_u32 s65, 0\n\t"
    "s_cbranch_scc1 Lsb8_%=\n\t"
    "Lbb7_%=:\n\t"
    "v_mfma_f32_16x16x32_bf16 v[160:163], a[136:139], v[80:83], v[160:163]\n\t"
    "ds_read_b128 a[160:163], v32 offset:40960\n\t"
    "ds_read_b128 a[164:167], v33 offset:40960\n\t"
    "v_mfma_f32_16x16x32_bf16 v[160:163], a[140:143], v[88:91], v[160:163]\n\t"
    "v_exp_f32 v145, v145\n\t"
    "v_mfma_f32_16x16x32_bf16 v[160:163], a[144:147], v[96:99], v[160:163]\n\t"
    "ds_read_b128 a[168:171], v34 offset:40960\n\t"
    "ds_read_b128 a[172:175], v35 offset:40960\n\t"
    "v_mfma_f32_16x16x32_bf16 v[160:163], a[148:151], v[104:107], v[160:163]\n\t"
    "v_exp_f32 v146, v146\n\t"
    "v_mfma_f32_16x16x32_bf16 v[160:163], a[152:155], v[112:115], v[160:163]\n\t"
    "ds_read_b128 a[176:179], v32 offset:41216\n\t"
    "ds_read_b128 a[180:183], v33 offset:41216\n\t"
    "v_mfma_f32_16x16x32_bf16 v[160:163], a[156:159], v[120:123], v[160:163]\n\t"
    "v_exp_f32 v147, v147\n\t"
    "v_mfma_f32_16x16x32_bf16 v[164:167], a[128:131], v[68:71], v[196:199]\n\t"
    "ds_read_b128 a[184:187], v34 offset:41216\n\t"
    "ds_read_b128 a[188:191], v35 offset:41216\n\t"
    "v_mfma_f32_16x16x32_bf16 v[164:167], a[132:135], v[76:79], v[164:167]\n\t"
    "v_exp_f32 v152, v152\n\t"
    "v_mfma_f32_16x16x32_bf16 v[164:167], a[136:139], v[84:87], v[164:167]\n\t"
    "v_exp_f32 v153, v153\n\t"
    "v_mfma_f32_16x16x32_bf16 v[164:167], a[140:143], v[92:95], v[164:167]\n\t"
    "v_exp_f32 v154, v154\n\t"
    "v_mfma_f32_16x16x32_bf16 v[164:167], a[144:147], v[100:103], v[164:167]\n\t"
    "v_exp_f32 v155, v155\n\t"
    "v_mfma_f32_16x16x32_bf16 v[164:167], a[148:151], v[108:111], v[164:167]\n\t"
    "v_exp_f32 v148, v148\n\t"
    "v_mfma_f32_16x16x32_bf16 v[164:167], a[152:155], v[116:119], v[164:167]\n\t"
    "v_exp_f32 v149, v149\n\t"
    "v_mfma_f32_16x16x32_bf16 v[164:167], a[156:159], v[124:127], v[164:167]\n\t"
    "v_cvt_pk_bf16_f32 v128, v128, v129\n\t"
    "s_waitcnt lgkmcnt(0)\n\t"
    "v_mfma_f32_16x16x32_bf16 v[168:171], a[160:163], v[64:67], v[192:195]\n\t"
    "v_exp_f32 v150, v150\n\t"
    "v_mfma_f32_16x16x32_bf16 v[168:171], a[164:167], v[72:75], v[168:171]\n\t"
    "v_exp_f32 v151, v151\n\t"
    "v_mfma_f32_16x16x32_bf16 v[168:171], a[168:171], v[80:83], v[168:171]\n\t"
    "ds_read_b128 a[128:131], v32 offset:49152\n\t"
    "ds_read_b128 a[132:135], v33 offset:49152\n\t"
    "v_mfma_f32_16x16x32_bf16 v[168:171], a[172:175], v[88:91], v[168:171]\n\t"
    "v_exp_f32 v156, v156\n\t"
    "v_mfma_f32_16x16x32_bf16 v[168:171], a[176:179], v[96:99], v[168:171]\n\t"
    "ds_read_b128 a[136:139], v34 offset:49152\n\t"
    "ds_read_b128 a[140:143], v35 offset:49152\n\t"
    "v_mfma_f32_16x16x32_bf16 v[168:171], a[180:183], v[104:107], v[168:171]\n\t"
    "v_exp_f32 v157, v157\n\t"
    "v_mfma_f32_16x16x32_bf16 v[168:171], a[184:187], v[112:115], v[168:171]\n\t"
    "ds_read_b128 a[144:147], v32 offset:49408\n\t"
    "ds_read_b128 a[148:151], v33 offset:49408\n\t"
    "v_mfma_f32_16x16x32_bf16 v[168:171], a[188:191], v[120:123], v[168:171]\n\t"
    "v_exp_f32 v158, v158\n\t"
    "v_mfma_f32_16x16x32_bf16 v[172:175], a[160:163], v[68:71], v[196:199]\n\t"
    "ds_read_b128 a[152:155], v34 offset:49408\n\t"
    "ds_read_b128 a[156:159], v35 offset:49408\n\t"
    "v_mfma_f32_16x16x32_bf16 v[172:175], a[164:167], v[76:79], v[172:175]\n\t"
    "v_exp_f32 v159, v159\n\t"
    "v_mfma_f32_16x16x32_bf16 v[172:175], a[168:171], v[84:87], v[172:175]\n\t"
    "v_cvt_pk_bf16_f32 v129, v130, v131\n\t"
    "v_cvt_pk_bf16_f32 v130, v136, v137\n\t"
    "v_mfma_f32_16x16x32_bf16 v[172:175], a[172:175], v[92:95], v[172:175]\n\t"
    "v_cvt_pk_bf16_f32 v131, v138, v139\n\t"
    "v_cvt_pk_bf16_f32 v132, v132, v133\n\t"
    "v_mfma_f32_16x16x32_bf16 v[172:175], a[176:179], v[100:103], v[172:175]\n\t"
    "v_cvt_pk_bf16_f32 v133, v134, v135\n\t"
    "v_cvt_pk_bf16_f32 v134, v140, v141\n\t"
    "v_mfma_f32_16x16x32_bf16 v[172:175], a[180:183], v[108:111], v[172:175]\n\t"
    "v_cvt_pk_bf16_f32 v135, v142, v143\n\t"
    "v_cvt_pk_bf16_f32 v144, v144, v145\n\t"
    "v_mfma_f32_16x16x32_bf16 v[172:175], a[184:187], v[116:119], v[172:175]\n\t"
    "v_cvt_pk_bf16_f32 v145, v146, v147\n\t"
    "v_cvt_pk_bf16_f32 v146, v152, v153\n\t"
    "v_mfma_f32_16x16x32_bf16 v[172:175], a[188:191], v[124:127], v[172:175]\n\t"
    "v_cvt_pk_bf16_f32 v147, v154, v155\n\t"
    "s_waitcnt lgkmcnt(0)\n\t"
    "v_mfma_f32_16x16x32_bf16 v[176:179], a[128:131], v[64:67], v[192:195]\n\t"
    "v_cvt_pk_bf16_f32 v148, v148, v149\n\t"
    "v_cvt_pk_bf16_f32 v149, v150, v151\n\t"
    "v_mfma_f32_16x16x32_bf16 v[176:179], a[132:135], v[72:75], v[176:179]\n\t"
    "v_cvt_pk_bf16_f32 v150, v156, v157\n\t"
    "v_cvt_pk_bf16_f32 v151, v158, v159\n\t"
    "v_mfma_f32_16x16x32_bf16 v[176:179], a[136:139], v[80:83], v[176:179]\n\t"
    "ds_read_b128 a[160:163], v32 offset:57344\n\t"
    "ds_read_b128 a[164:167], v33 offset:57344\n\t"
    "v_mfma_f32_16x16x32_bf16 v[176:179], a[140:143], v[88:91], v[176:179]\n\t"
    "v_max3_f32 v216, v160, v161, v162\n\t"
    "v_max3_f32 v217, v163, v164, v165\n\t"
    "v_mfma_f32_16x16x32_bf16 v[176:179], a[144:147], v[96:99], v[176:179]\n\t"
    "ds_read_b128 a[168:171], v34 offset:57344\n\t"
    "ds_read_b128 a[172:175], v35 offset:57344\n\t"
    "v_mfma_f32_16x16x32_bf16 v[176:179], a[148:151], v[104:107], v[176:179]\n\t"
    "v_max3_f32 v216, v216, v166, v167\n\t"
    "v_max3_f32 v217, v217, v168, v169\n\t"
    "v_mfma_f32_16x16x32_bf16 v[176:179], a[152:155], v[112:115], v[176:179]\n\t"
    "ds_read_b128 a[176:179], v32 offset:57600\n\t"
    "ds_read_b128 a[180:183], v33 offset:57600\n\t"
    "v_mfma_f32_16x16x32_bf16 v[176:179], a[156:159], v[120:123], v[176:179]\n\t"
    "v_max3_f32 v216, v216, v170, v171\n\t"
    "v_max3_f32 v217, v217, v172, v173\n\t"
    "v_mfma_f32_16x16x32_bf16 v[180:183], a[128:131], v[68:71], v[196:199]\n\t"
    "ds_read_b128 a[184:187], v34 offset:57600\n\t"
    "ds_read_b128 a[188:191], v35 offset:57600\n\t"
    "v_mfma_f32_16x16x32_bf16 v[180:183], a[132:135], v[76:79], v[180:183]\n\t"
    "v_max3_f32 v216, v216, v174, v175\n\t"
    "v_mfma_f32_16x16x32_bf16 v[180:183], a[136:139], v[84:87], v[180:183]\n\t"
    "v_mfma_f32_16x16x32_bf16 v[180:183], a[140:143], v[92:95], v[180:183]\n\t"
    "v_mfma_f32_16x16x32_bf16 v[180:183], a[144:147], v[100:103], v[180:183]\n\t"
    "v_max3_f32 v217, v217, v176, v177\n\t"
    "v_max3_f32 v216, v216, v178, v179\n\t"
    "v_mfma_f32_16x16x32_bf16 v[180:183], a[148:151], v[108:111], v[180:183]\n\t"
    "v_mfma_f32_16x16x32_bf16 v[180:183], a[152:155], v[116:119], v[180:183]\n\t"
    "v_add_u32 v6, %[tstr], v6\n\t"
    "v_mfma_f32_16x16x32_bf16 v[180:183], a[156:159], v[124:127], v[180:183]\n\t"
    "s_waitcnt lgkmcnt(0)\n\t"
    "v_mfma_f32_16x16x32_bf16 v[184:187], a[160:163], v[64:67], v[192:195]\n\t"
    "s_waitcnt vmcnt(1)\n\t"
    "s_barrier\n\t"
    "v_mfma_f32_16x16x32_bf16 v[184:187], a[164:167], v[72:75], v[184:187]\n\t"
    "s_add_u32 m0, s63, 0x18000\n\t"
    "v_mfma_f32_16x16x32_bf16 v[184:187], a[168:171], v[80:83], v[184:187]\n\t"
    "ds_read_b64_tr_b16 a[192:193], v16\n\t"
    "ds_read_b64_tr_b16 a[194:195], v16 offset:8192\n\t"
    "v_mfma_f32_16x16x32_bf16 v[184:187], a[172:175], v[88:91], v[184:187]\n\t"
    "buffer_load_dwordx4 v36, %[srdv], s62 offen lds\n\t"
    "v_mfma_f32_16x16x32_bf16 v[184:187], a[176:179], v[96:99], v[184:187]\n\t"
    "ds_read_b64_tr_b16 a[196:197], v16 offset:16384\n\t"
    "ds_read_b64_tr_b16 a[198:199], v16 offset:24576\n\t"
    "v_mfma_f32_16x16x32_bf16 v[184:187], a[180:183], v[104:107], v[184:187]\n\t"
    "buffer_load_dwordx4 v37, %[srdv], s62 offen offset:1024 lds\n\t"
    "v_max3_f32 v217, v217, v180, v181\n\t"
    "v_mfma_f32_16x16x32_bf16 v[184:187], a[184:187], v[112:115], v[184:187]\n\t"
    "ds_read_b64_tr_b16 a[200:201], v17\n\t"
    "ds_read_b64_tr_b16 a[202:203], v17 offset:8192\n\t"
    "v_mfma_f32_16x16x32_bf16 v[184:187], a[188:191], v[120:123], v[184:187]\n\t"
    "buffer_load_dwordx4 v38, %[srdv], s62 offen offset:2048 lds\n\t"
    "v_max3_f32 v216, v216, v182, v183\n\t"
    "v_mfma_f32_16x16x32_bf16 v[188:191], a[160:163], v[68:71], v[196:199]\n\t"
    "ds_read_b64_tr_b16 a[204:205], v17 offset:16384\n\t"
    "ds_read_b64_tr_b16 a[206:207], v17 offset:24576\n\t"
    "v_mfma_f32_16x16x32_bf16 v[188:191], a[164:167], v[76:79], v[188:191]\n\t"
    "buffer_load_dwordx4 v39, %[srdv], s62 offen offset:3072 lds\n\t"
    "v_mfma_f32_16x16x32_bf16 v[188:191], a[168:171], v[84:87], v[188:191]\n\t"
    "ds_read_b64_tr_b16 a[208:209], v18\n\t"
    "ds_read_b64_tr_b16 a[210:211], v18 offset:8192\n\t"
    "v_mfma_f32_16x16x32_bf16 v[188:191], a[172:175], v[92:95], v[188:191]\n\t"
    "s_add_u32 m0, s63, 0x19000\n\t"
    "s_add_u32 s62, s62, 0x1000\n\t"
    "v_mfma_f32_16x16x32_bf16 v[188:191], a[176:179], v[100:103], v[188:191]\n\t"
    "ds_read_b64_tr_b16 a[212:213], v18 offset:16384\n\t"
    "ds_read_b64_tr_b16 a[214:215], v18 offset:24576\n\t"
    "v_mfma_f32_16x16x32_bf16 v[188:191], a[180:183], v[108:111], v[188:191]\n\t"
    "buffer_load_dwordx4 v36, %[srdv], s62 offen lds\n\t"
    "v_max3_f32 v217, v217, v184, v185\n\t"
    "v_mfma_f32_16x16x32_bf16 v[188:191], a[184:187], v[116:119], v[188:191]\n\t"
    "ds_read_b64_tr_b16 a[216:217], v19\n\t"
    "ds_read_b64_tr_b16 a[218:219], v19 offset:8192\n\t"
    "v_mfma_f32_16x16x32_bf16 v[188:191], a[188:191], v[124:127], v[188:191]\n\t"
    "v_max3_f32 v216, v216, v186, v187\n\t"
    "s_waitcnt lgkmcnt(6)\n\t"
    "v_mfma_f32_16x16x32_bf16 a[0:3], a[192:195], v[128:131], a[0:3]\n\t"
    "ds_read_b64_tr_b16 a[220:221], v19 offset:16384\n\t"
    "ds_read_b64_tr_b16 a[222:223], v19 offset:24576\n\t"
    "v_mfma_f32_16x16x32_bf16 a[0:3], a[196:199], v[144:147], a[0:3]\n\t"
    "buffer_load_dwordx4 v37, %[srdv], s62 offen offset:1024 lds\n\t"
    "v_mfma_f32_16x16x32_bf16 a[4:7], a[192:195], v[132:135], a[4:7]\n\t"
    "ds_read_b64_tr_b16 a[224:225], v20\n\t"
    "ds_read_b64_tr_b16 a[226:227], v20 offset:8192\n\t"
    "v_mfma_f32_16x16x32_bf16 a[4:7], a[196:199], v[148:151], a[4:7]\n\t"
    "v_max3_f32 v217, v217, v188, v189\n\t"
    "v_max3_f32 v216, v216, v190, v191\n\t"
    "v_mfma_f32_16x16x32_bf16 a[8:11], a[200:203], v[128:131], a[8:11]\n\t"
    "ds_read_b64_tr_b16 a[228:229], v20 offset:16384\n\t"
    "ds_read_b64_tr_b16 a[230:231], v20 offset:24576\n\t"
    "v_mfma_f32_16x16x32_bf16 a[8:11], a[204:207], v[144:147], a[8:11]\n\t"
    "v_max_f32 v216, v216, v217\n\t"
    "v_cmp_lt_f32 vcc, s66, v216\n\t"
    "v_mfma_f32_16x16x32_bf16 a[12:15], a[200:203], v[132:135], a[12:15]\n\t"
    "ds_read_b64_tr_b16 a[232:233], v21\n\t"
    "ds_read_b64_tr_b16 a[234:235], v21 offset:8192\n\t"
    "v_mfma_f32_16x16x32_bf16 a[12:15], a[204:207], v[148:151], a[12:15]\n\t"
    "s_cbranch_vccnz Lsa10_%=\n\t"
    "Lba9_%=:\n\t"
    "s_waitcnt lgkmcnt(6)\n\t"
    "v_mfma_f32_16x16x32_bf16 a[16:19], a[208:211], v[128:131], a[16:19]\n\t"
    "ds_read_b64_tr_b16 a[236:237], v21 offset:16384\n\t"
    "ds_read_b64_tr_b16 a[238:239], v21 offset:24576\n\t"
    "v_mfma_f32_16x16x32_bf16 a[16:19], a[212:215], v[144:147], a[16:19]\n\t"
    "buffer_load_dwordx4 v38, %[srdv], s62 offen offset:2048 lds\n\t"
    "v_mfma_f32_16x16x32_bf16 a[20:23], a[208:211], v[132:135], a[20:23]\n\t"
    "ds_read_b64_tr_b16 a[240:241], v22\n\t"
    "ds_read_b64_tr_b16 a[242:243], v22 offset:8192\n\t"
    "v_mfma_f32_16x16x32_bf16 a[20:23], a[212:215], v[148:151], a[20:23]\n\t"
    "buffer_load_dwordx4 v39, %[srdv], s62 offen offset:3072 lds\n\t"
    "v_mfma_f32_16x16x32_bf16 a[24:27], a[216:219], v[128:131], a[24:27]\n\t"
    "ds_read_b64_tr_b16 a[244:245], v22 offset:16384\n\t"
    "ds_read_b64_tr_b16 a[246:247], v22 offset:24576\n\t"
    "v_mfma_f32_16x16x32_bf16 a[24:27], a[220:223], v[144:147], a[24:27]\n\t"
    "s_add_u32 s62, s62, 0x7000\n\t"
    "s_add_u32 m0, s63, 0x8000\n\t"
    "v_mfma_f32_16x16x32_bf16 a[28:31], a[216:219], v[132:135], a[28:31]\n\t"
    "ds_read_b64_tr_b16 a[248:249], v23\n\t"
    "ds_read_b64_tr_b16 a[250:251], v23 offset:8192\n\t"
    "v_mfma_f32_16x16x32_bf16 a[28:31], a[220:223], v[148:151], a[28:31]\n\t"
    "buffer_load_dwordx4 v8, %[srdk], s61 offen lds\n\t"
    "s_waitcnt lgkmcnt(6)\n\t"
    "v_mfma_f32_16x16x32_bf16 a[32:35], a[224:227], v[128:131], a[32:35]\n\t"
    "ds_read_b64_tr_b16 a[252:253], v23 offset:16384\n\t"
    "ds_read_b64_tr_b16 a[254:255], v23 offset:24576\n\t"
    "v_mfma_f32_16x16x32_bf16 a[32:35], a[228:231], v[144:147], a[32:35]\n\t"
    "v_exp_f32 v160, v160\n\t"
    "v_mfma_f32_16x16x32_bf16 a[36:39], a[224:227], v[132:135], a[36:39]\n\t"
    "ds_read_b64_tr_b16 a[192:193], v16 offset:256\n\t"
    "ds_read_b64_tr_b16 a[194:195], v16 offset:8448\n\t"
    "v_mfma_f32_16x16x32_bf16 a[36:39], a[228:231], v[148:151], a[36:39]\n\t"
    "v_exp_f32 v161, v161\n\t"
    "v_mfma_f32_16x16x32_bf16 a[40:43], a[232:235], v[128:131], a[40:43]\n\t"
    "ds_read_b64_tr_b16 a[196:197], v16 offset:16640\n\t"
    "ds_read_b64_tr_b16 a[198:199], v16 offset:24832\n\t"
    "v_mfma_f32_16x16x32_bf16 a[40:43], a[236:239], v[144:147], a[40:43]\n\t"
    "v_exp_f32 v162, v162\n\t"
    "v_mfma_f32_16x16x32_bf16 a[44:47], a[232:235], v[132:135], a[44:47]\n\t"
    "ds_read_b64_tr_b16 a[200:201], v17 offset:256\n\t"
    "ds_read_b64_tr_b16 a[202:203], v17 offset:8448\n\t"
    "v_mfma_f32_16x16x32_bf16 a[44:47], a[236:239], v[148:151], a[44:47]\n\t"
    "buffer_load_dwordx4 v9, %[srdk], s61 offen offset:1024 lds\n\t"
    "s_waitcnt lgkmcnt(6)\n\t"
    "v_mfma_f32_16x16x32_bf16 a[48:51], a[240:243], v[128:131], a[48:51]\n\t"
    "ds_read_b64_tr_b16 a[204:205], v17 offset:16640\n\t"
    "ds_read_b64_tr_b16 a[206:207], v17 offset:24832\n\t"
    "v_mfma_f32_16x16x32_bf16 a[48:51], a[244:247], v[144:147], a[48:51]\n\t"
    "v_exp_f32 v163, v163\n\t"
    "v_mfma_f32_16x16x32_bf16 a[52:55], a[240:243], v[132:135], a[52:55]\n\t"
    "ds_read_b64_tr_b16 a[208:209], v18 offset:256\n\t"
    "ds_read_b64_tr_b16 a[210:211], v18 offset:8448\n\t"
    "v_mfma_f32_16x16x32_bf16 a[52:55], a[244:247], v[148:151], a[52:55]\n\t"
    "v_exp_f32 v168, v168\n\t"
    "v_mfma_f32_16x16x32_bf16 a[56:59], a[248:251], v[128:131], a[56:59]\n\t"
    "ds_read_b64_tr_b16 a[212:213], v18 offset:16640\n\t"
    "ds_read_b64_tr_b16 a[214:215], v18 offset:24832\n\t"
    "v_mfma_f32_16x16x32_bf16 a[56:59], a[252:255], v[144:147], a[56:59]\n\t"
    "v_exp_f32 v169, v169\n\t"
    "v_mfma_f32_16x16x32_bf16 a[60:63], a[248:251], v[132:135], a[60:63]\n\t"
    "ds_read_b64_tr_b16 a[216:217], v19 offset:256\n\t"
    "ds_read_b64_tr_b16 a[218:219], v19 offset:8448\n\t"
    "v_mfma_f32_16x16x32_bf16 a[60:63], a[252:255], v[148:151], a[60:63]\n\t"
    "buffer_load_dwordx4 v10, %[srdk], s61 offen offset:2048 lds\n\t"
    "s_waitcnt lgkmcnt(6)\n\t"
    "v_mfma_f32_16x16x32_bf16 a[64:67], a[192:195], v[128:131], a[64:67]\n\t"
    "ds_read_b64_tr_b16 a[220:221], v19 offset:16640\n\t"
    "ds_read_b64_tr_b16 a[222:223], v19 offset:24832\n\t"
    "v_mfma_f32_16x16x32_bf16 a[64:67], a[196:199], v[144:147], a[64:67]\n\t"
    "v_exp_f32 v170, v170\n\t"
    "v_mfma_f32_16x16x32_bf16 a[68:71], a[192:195], v[132:135], a[68:71]\n\t"
    "ds_read_b64_tr_b16 a[224:225], v20 offset:256\n\t"
    "ds_read_b64_tr_b16 a[226:227], v20 offset:8448\n\t"
    "v_mfma_f32_16x16x32_bf16 a[68:71], a[196:199], v[148:151], a[68:71]\n\t"
    "v_exp_f32 v171, v171\n\t"
    "v_mfma_f32_16x16x32_bf16 a[72:75], a[200:203], v[128:131], a[72:75]\n\t"
    "ds_read_b64_tr_b16 a[228:229], v20 offset:16640\n\t"
    "ds_read_b64_tr_b16 a[230:231], v20 offset:24832\n\t"
    "v_mfma_f32_16x16x32_bf16 a[72:75], a[204:207], v[144:147], a[72:75]\n\t"
    "v_exp_f32 v164, v164\n\t"
    "v_mfma_f32_16x16x32_bf16 a[76:79], a[200:203], v[132:135], a[76:79]\n\t"
    "ds_read_b64_tr_b16 a[232:233], v21 offset:256\n\t"
    "ds_read_b64_tr_b16 a[234:235], v21 offset:8448\n\t"
    "v_mfma_f32_16x16x32_bf16 a[76:79], a[204:207], v[148:151], a[76:79]\n\t"
    "buffer_load_dwordx4 v11, %[srdk], s61 offen offset:3072 lds\n\t"
    "s_waitcnt lgkmcnt(6)\n\t"
    "v_mfma_f32_16x16x32_bf16 a[80:83], a[208:211], v[128:131], a[80:83]\n\t"
    "ds_read_b64_tr_b16 a[236:237], v21 offset:16640\n\t"
    "ds_read_b64_tr_b16 a[238:239], v21 offset:24832\n\t"
    "v_mfma_f32_16x16x32_bf16 a[80:83], a[212:215], v[144:147], a[80:83]\n\t"
    "v_exp_f32 v165, v165\n\t"
    "v_mfma_f32_16x16x32_bf16 a[84:87], a[208:211], v[132:135], a[84:87]\n\t"
    "ds_read_b64_tr_b16 a[240:241], v22 offset:256\n\t"
    "ds_read_b64_tr_b16 a[242:243], v22 offset:8448\n\t"
    "v_mfma_f32_16x16x32_bf16 a[84:87], a[212:215], v[148:151], a[84:87]\n\t"
    "v_exp_f32 v166, v166\n\t"
    "v_mfma_f32_16x16x32_bf16 a[88:91], a[216:219], v[128:131], a[88:91]\n\t"
    "ds_read_b64_tr_b16 a[244:245], v22 offset:16640\n\t"
    "ds_read_b64_tr_b16 a[246:247], v22 offset:24832\n\t"
    "v_mfma_f32_16x16x32_bf16 a[88:91], a[220:223], v[144:147], a[88:91]\n\t"
    "s_add_u32 m0, s63, 0x9000\n\t"
    "v_mfma_f32_16x16x32_bf16 a[92:95], a[216:219], v[132:135], a[92:95]\n\t"
    "ds_read_b64_tr_b16 a[248:249], v23 offset:256\n\t"
    "ds_read_b64_tr_b16 a[250:251], v23 offset:8448\n\t"
    "v_mfma_f32_16x16x32_bf16 a[92:95], a[220:223], v[148:151], a[92:95]\n\t"
    "buffer_load_dwordx4 v12, %[srdk], s61 offen lds\n\t"
    "s_waitcnt lgkmcnt(6)\n\t"
    "v_mfma_f32_16x16x32_bf16 a[96:99], a[224:227], v[128:131], a[96:99]\n\t"
    "ds_read_b64_tr_b16 a[252:253], v23 offset:16640\n\t"
    "ds_read_b64_tr_b16 a[254:255], v23 offset:24832\n\t"
    "v_mfma_f32_16x16x32_bf16 a[96:99], a[228:231], v[144:147], a[96:99]\n\t"
    "v_exp_f32 v167, v167\n\t"
    "v_mfma_f32_16x16x32_bf16 a[100:103], a[224:227], v[132:135], a[100:103]\n\t"
    "v_exp_f32 v172, v172\n\t"
    "v_mfma_f32_16x16x32_bf16 a[100:103], a[228:231], v[148:151], a[100:103]\n\t"
    "v_exp_f32 v173, v173\n\t"
    "v_mfma_f32_16x16x32_bf16 a[104:107], a[232:235], v[128:131], a[104:107]\n\t"
    "ds_read_b128 a[128:131], v32\n\t"
    "ds_read_b128 a[132:135], v33\n\t"
    "v_mfma_f32_16x16x32_bf16 a[104:107], a[236:239], v[144:147], a[104:107]\n\t"
    "v_exp_f32 v174, v174\n\t"
    "v_mfma_f32_16x16x32_bf16 a[108:111], a[232:235], v[132:135], a[108:111]\n\t"
    "ds_read_b128 a[136:139], v34\n\t"
    "ds_read_b128 a[140:143], v35\n\t"
    "v_mfma_f32_16x16x32_bf16 a[108:111], a[236:239], v[148:151], a[108:111]\n\t"
    "buffer_load_dwordx4 v13, %[srdk], s61 offen offset:1024 lds\n\t"
    "s_waitcnt lgkmcnt(4)\n\t"
    "v_mfma_f32_16x16x32_bf16 a[112:115], a[240:243], v[128:131], a[112:115]\n\t"
    "ds_read_b128 a[144:147], v32 offset:256\n\t"
    "ds_read_b128 a[148:151], v33 offset:256\n\t"
    "v_mfma_f32_16x16x32_bf16 a[112:115], a[244:247], v[144:147], a[112:115]\n\t"
    "v_exp_f32 v175, v175\n\t"
    "v_mfma_f32_16x16x32_bf16 a[116:119], a[240:243], v[132:135], a[116:119]\n\t"
    "ds_read_b128 a[152:155], v34 offset:256\n\t"
    "ds_read_b128 a[156:159], v35 offset:256\n\t"
    "v_mfma_f32_16x16x32_bf16 a[116:119], a[244:247], v[148:151], a[116:119]\n\t"
    "v_mfma_f32_16x16x32_bf16 a[120:123], a[248:251], v[128:131], a[120:123]\n\t"
    "v_mfma_f32_16x16x32_bf16 a[120:123], a[252:255], v[144:147], a[120:123]\n\t"
    "buffer_load_dwordx4 v14, %[srdk], s61 offen offset:2048 lds\n\t"
    "v_mfma_f32_16x16x32_bf16 a[124:127], a[248:251], v[132:135], a[124:127]\n\t"
    "v_mfma_f32_16x16x32_bf16 a[124:127], a[252:255], v[148:151], a[124:127]\n\t"
    "buffer_load_dwordx4 v15, %[srdk], s61 offen offset:3072 lds\n\t"
    "v_mfma_f32_16x16x32_bf16 v[200:203], v[208:211], v[128:131], v[200:203]\n\t"
    "s_add_u32 s61, s61, 0x8000\n\t"
    "s_sub_u32 s64, s64, 1\n\t"
    "v_mfma_f32_16x16x32_bf16 v[200:203], v[208:211], v[144:147], v[200:203]\n\t"
    "buffer_load_dword v7, v6, %[srdt], 0 offen\n\t"
    "v_mfma_f32_16x16x32_bf16 v[204:207], v[208:211], v[132:135], v[204:207]\n\t"
    "v_mfma_f32_16x16x32_bf16 v[204:207], v[208:211], v[148:151], v[204:207]\n\t"
    "s_cmp_lg_u32 s64, 0\n\t"
    "s_cbranch_scc1 LLOOP_%=\n\t"
    "s_waitcnt lgkmcnt(0)\n\t"
    "v_mfma_f32_16x16x32_bf16 v[128:131], a[128:131], v[64:67], v[192:195]\n\t"
    "v_exp_f32 v176, v176\n\t"
    "v_mfma_f32_16x16x32_bf16 v[128:131], a[132:135], v[72:75], v[128:131]\n\t"
    "s_cmp_lg_u32 s65, 0\n\t"
    "s_cbranch_scc1 Lsb12_%=\n\t"
    "Lbb11_%=:\n\t"
    "v_mfma_f32_16x16x32_bf16 v[128:131], a[136:139], v[80:83], v[128:131]\n\t"
    "ds_read_b128 a[160:163], v32 offset:8192\n\t"
    "ds_read_b128 a[164:167], v33 offset:8192\n\t"
    "v_mfma_f32_16x16x32_bf16 v[128:131], a[140:143], v[88:91], v[128:131]\n\t"
    "v_exp_f32 v177, v177\n\t"
    "v_mfma_f32_16x16x32_bf16 v[128:131], a[144:147], v[96:99], v[128:131]\n\t"
    "ds_read_b128 a[168:171], v34 offset:8192\n\t"
    "ds_read_b128 a[172:175], v35 offset:8192\n\t"
    "v_mfma_f32_16x16x32_bf16 v[128:131], a[148:151], v[104:107], v[128:131]\n\t"
    "v_exp_f32 v178, v178\n\t"
    "v_mfma_f32_16x16x32_bf16 v[128:131], a[152:155], v[112:115], v[128:131]\n\t"
    "ds_read_b128 a[176:179], v32 offset:8448\n\t"
    "ds_read_b128 a[180:183], v33 offset:8448\n\t"
    "v_mfma_f32_16x16x32_bf16 v[128:131], a[156:159], v[120:123], v[128:131]\n\t"
    "v_exp_f32 v179, v179\n\t"
    "v_mfma_f32_16x16x32_bf16 v[132:135], a[128:131], v[68:71], v[196:199]\n\t"
    "ds_read_b128 a[184:187], v34 offset:8448\n\t"
    "ds_read_b128 a[188:191], v35 offset:8448\n\t"
    "v_mfma_f32_16x16x32_bf16 v[132:135], a[132:135], v[76:79], v[132:135]\n\t"
    "v_exp_f32 v184, v184\n\t"
    "v_mfma_f32_16x16x32_bf16 v[132:135], a[136:139], v[84:87], v[132:135]\n\t"
    "v_exp_f32 v185, v185\n\t"
    "v_mfma_f32_16x16x32_bf16 v[132:135], a[140:143], v[92:95], v[132:135]\n\t"
    "v_exp_f32 v186, v186\n\t"
    "v_mfma_f32_16x16x32_bf16 v[132:135], a[144:147], v[100:103], v[132:135]\n\t"
    "v_exp_f32 v187, v187\n\t"
    "v_mfma_f32_16x16x32_bf16 v[132:135], a[148:151], v[108:111], v[132:135]\n\t"
    "v_exp_f32 v180, v180\n\t"
    "v_mfma_f32_16x16x32_bf16 v[132:135], a[152:155], v[116:119], v[132:135]\n\t"
    "v_exp_f32 v181, v181\n\t"
    "v_mfma_f32_16x16x32_bf16 v[132:135], a[156:159], v[124:127], v[132:135]\n\t"
    "v_cvt_pk_bf16_f32 v160, v160, v161\n\t"
    "s_waitcnt lgkmcnt(0)\n\t"
    "v_mfma_f32_16x16x32_bf16 v[136:139], a[160:163], v[64:67], v[192:195]\n\t"
    "v_exp_f32 v182, v182\n\t"
    "v_mfma_f32_16x16x32_bf16 v[136:139], a[164:167], v[72:75], v[136:139]\n\t"
    "v_exp_f32 v183, v183\n\t"
    "v_mfma_f32_16x16x32_bf16 v[136:139], a[168:171], v[80:83], v[136:139]\n\t"
    "ds_read_b128 a[128:131], v32 offset:16384\n\t"
    "ds_read_b128 a[132:135], v33 offset:16384\n\t"
    "v_mfma_f32_16x16x32_bf16 v[136:139], a[172:175], v[88:91], v[136:139]\n\t"
    "v_exp_f32 v188, v188\n\t"
    "v_mfma_f32_16x16x32_bf16 v[136:139], a[176:179], v[96:99], v[136:139]\n\t"
    "ds_read_b128 a[136:139], v34 offset:16384\n\t"
    "ds_read_b128 a[140:143], v35 offset:16384\n\t"
    "v_mfma_f32_16x16x32_bf16 v[136:139], a[180:183], v[104:107], v[136:139]\n\t"
    "v_exp_f32 v189, v189\n\t"
    "v_mfma_f32_16x16x32_bf16 v[136:139], a[184:187], v[112:115], v[136:139]\n\t"
    "ds_read_b128 a[144:147], v32 offset:16640\n\t"
    "ds_read_b128 a[148:151], v33 offset:16640\n\t"
    "v_mfma_f32_16x16x32_bf16 v[136:139], a[188:191], v[120:123], v[136:139]\n\t"
    "v_exp_f32 v190, v190\n\t"
    "v_mfma_f32_16x16x32_bf16 v[140:143], a[160:163], v[68:71], v[196:199]\n\t"
    "ds_read_b128 a[152:155], v34 offset:16640\n\t"
    "ds_read_b128 a[156:159], v35 offset:16640\n\t"
    "v_mfma_f32_16x16x32_bf16 v[140:143], a[164:167], v[76:79], v[140:143]\n\t"
    "v_exp_f32 v191, v191\n\t"
    "v_mfma_f32_16x16x32_bf16 v[140:143], a[168:171], v[84:87], v[140:143]\n\t"
    "v_cvt_pk_bf16_f32 v161, v162, v163\n\t"
    "v_cvt_pk_bf16_f32 v162, v168, v169\n\t"
    "v_mfma_f32_16x16x32_bf16 v[140:143], a[172:175], v[92:95], v[140:143]\n\t"
    "v_cvt_pk_bf16_f32 v163, v170, v171\n\t"
    "v_cvt_pk_bf16_f32 v164, v164, v165\n\t"
    "v_mfma_f32_16x16x32_bf16 v[140:143], a[176:179], v[100:103], v[140:143]\n\t"
    "v_cvt_pk_bf16_f32 v165, v166, v167\n\t"
    "v_cvt_pk_bf16_f32 v166, v172, v173\n\t"
    "v_mfma_f32_16x16x32_bf16 v[140:143], a[180:183], v[108:111], v[140:143]\n\t"
    "v_cvt_pk_bf16_f32 v167, v174, v175\n\t"
    "v_cvt_pk_bf16_f32 v176, v176, v177\n\t"
    "v_mfma_f32_16x16x32_bf16 v[140:143], a[184:187], v[116:119], v[140:143]\n\t"
    "v_cvt_pk_bf16_f32 v177, v178, v179\n\t"
    "v_cvt_pk_bf16_f32 v178, v184, v185\n\t"
    "v_mfma_f32_16x16x32_bf16 v[140:143], a[188:191], v[124:127], v[140:143]\n\t"
    "v_cvt_pk_bf16_f32 v179, v186, v187\n\t"
    "s_waitcnt lgkmcnt(0)\n\t"
    "v_mfma_f32_16x16x32_bf16 v[144:147], a[128:131], v[64:67], v[192:195]\n\t"
    "v_cvt_pk_bf16_f32 v180, v180, v181\n\t"
    "v_cvt_pk_bf16_f32 v181, v182, v183\n\t"
    "v_mfma_f32_16x16x32_bf16 v[144:147], a[132:135], v[72:75], v[144:147]\n\t"
    "v_cvt_pk_bf16_f32 v182, v188, v189\n\t"
    "v_cvt_pk_bf16_f32 v183, v190, v191\n\t"
    "v_mfma_f32_16x16x32_bf16 v[144:147], a[136:139], v[80:83], v[144:147]\n\t"
    "ds_read_b128 a[160:163], v32 offset:24576\n\t"
    "ds_read_b128 a[164:167], v33 offset:24576\n\t"
    "v_mfma_f32_16x16x32_bf16 v[144:147], a[140:143], v[88:91], v[144:147]\n\t"
    "v_max3_f32 v216, v128, v129, v130\n\t"
    "v_max3_f32 v217, v131, v132, v133\n\t"
    "v_mfma_f32_16x16x32_bf16 v[144:147], a[144:147], v[96:99], v[144:147]\n\t"
    "ds_read_b128 a[168:171], v34 offset:24576\n\t"
    "ds_read_b128 a[172:175], v35 offset:24576\n\t"
    "v_mfma_f32_16x16x32_bf16 v[144:147], a[148:151], v[104:107], v[144:147]\n\t"
    "v_max3_f32 v216, v216, v134, v135\n\t"
    "v_max3_f32 v217, v217, v136, v137\n\t"
    "v_mfma_f32_16x16x32_bf16 v[144:147], a[152:155], v[112:115], v[144:147]\n\t"
    "ds_read_b128 a[176:179], v32 offset:24832\n\t"
    "ds_read_b128 a[180:183], v33 offset:24832\n\t"
    "v_mfma_f32_16x16x32_bf16 v[144:147], a[156:159], v[120:123], v[144:147]\n\t"
    "v_max3_f32 v216, v216, v138, v139\n\t"
    "v_max3_f32 v217, v217, v140, v141\n\t"
    "v_mfma_f32_16x16x32_bf16 v[148:151], a[128:131], v[68:71], v[196:199]\n\t"
    "ds_read_b128 a[184:187], v34 offset:24832\n\t"
    "ds_read_b128 a[188:191], v35 offset:24832\n\t"
    "v_mfma_f32_16x16x32_bf16 v[148:151], a[132:135], v[76:79], v[148:151]\n\t"
    "v_max3_f32 v216, v216, v142, v143\n\t"
    "v_mfma_f32_16x16x32_bf16 v[148:151], a[136:139], v[84:87], v[148:151]\n\t"
    "v_mfma_f32_16x16x32_bf16 v[148:151], a[140:143], v[92:95], v[148:151]\n\t"
    "v_mfma_f32_16x16x32_bf16 v[148:151], a[144:147], v[100:103], v[148:151]\n\t"
    "v_max3_f32 v217, v217, v144, v145\n\t"
    "v_max3_f32 v216, v216, v146, v147\n\t"
    "v_mfma_f32_16x16x32_bf16 v[148:151], a[148:151], v[108:111], v[148:151]\n\t"
    "v_mfma_f32_16x16x32_bf16 v[148:151], a[152:155], v[116:119], v[148:151]\n\t"
    "v_mfma_f32_16x16x32_bf16 v[148:151], a[156:159], v[124:127], v[148:151]\n\t"
    "s_waitcnt lgkmcnt(0)\n\t"
    "v_mfma_f32_16x16x32_bf16 v[152:155], a[160:163], v[64:67], v[192:195]\n\t"
    "s_waitcnt vmcnt(1)\n\t"
    "s_barrier\n\t"
    "v_mfma_f32_16x16x32_bf16 v[152:155], a[164:167], v[72:75], v[152:155]\n\t"
    "s_add_u32 m0, s63, 0x10000\n\t"
    "v_mfma_f32_16x16x32_bf16 v[152:155], a[168:171], v[80:83], v[152:155]\n\t"
    "ds_read_b64_tr_b16 a[192:193], v16 offset:32768\n\t"
    "ds_read_b64_tr_b16 a[194:195], v16 offset:40960\n\t"
    "v_mfma_f32_16x16x32_bf16 v[152:155], a[172:175], v[88:91], v[152:155]\n\t"
    "buffer_load_dwordx4 v36, %[srdv], s62 offen lds\n\t"
    "v_mfma_f32_16x16x32_bf16 v[152:155], a[176:179], v[96:99], v[152:155]\n\t"
    "ds_read_b64_tr_b16 a[196:197], v16 offset:49152\n\t"
    "ds_read_b64_tr_b16 a[198:199], v16 offset:57344\n\t"
    "v_mfma_f32_16x16x32_bf16 v[152:155], a[180:183], v[104:107], v[152:155]\n\t"
    "buffer_load_dwordx4 v37, %[srdv], s62 offen offset:1024 lds\n\t"
    "v_max3_f32 v217, v217, v148, v149\n\t"
    "v_mfma_f32_16x16x32_bf16 v[152:155], a[184:187], v[112:115], v[152:155]\n\t"
    "ds_read_b64_tr_b16 a[200:201], v17 offset:32768\n\t"
    "ds_read_b64_tr_b16 a[202:203], v17 offset:40960\n\t"
    "v_mfma_f32_16x16x32_bf16 v[152:155], a[188:191], v[120:123], v[152:155]\n\t"
    "buffer_load_dwordx4 v38, %[srdv], s62 offen offset:2048 lds\n\t"
    "v_max3_f32 v216, v216, v150, v151\n\t"
    "v_mfma_f32_16x16x32_bf16 v[156:159], a[160:163], v[68:71], v[196:199]\n\t"
    "ds_read_b64_tr_b16 a[204:205], v17 offset:49152\n\t"
    "ds_read_b64_tr_b16 a[206:207], v17 offset:57344\n\t"
    "v_mfma_f32_16x16x32_bf16 v[156:159], a[164:167], v[76:79], v[156:159]\n\t"
    "buffer_load_dwordx4 v39, %[srdv], s62 offen offset:3072 lds\n\t"
    "v_mfma_f32_16x16x32_bf16 v[156:159], a[168:171], v[84:87], v[156:159]\n\t"
    "ds_read_b64_tr_b16 a[208:209], v18 offset:32768\n\t"
    "ds_read_b64_tr_b16 a[210:211], v18 offset:40960\n\t"
    "v_mfma_f32_16x16x32_bf16 v[156:159], a[172:175], v[92:95], v[156:159]\n\t"
    "s_add_u32 m0, s63, 0x11000\n\t"
    "s_add_u32 s62, s62, 0x1000\n\t"
    "v_mfma_f32_16x16x32_bf16 v[156:159], a[176:179], v[100:103], v[156:159]\n\t"
    "ds_read_b64_tr_b16 a[212:213], v18 offset:49152\n\t"
    "ds_read_b64_tr_b16 a[214:215], v18 offset:57344\n\t"
    "v_mfma_f32_16x16x32_bf16 v[156:159], a[180:183], v[108:111], v[156:159]\n\t"
    "buffer_load_dwordx4 v36, %[srdv], s62 offen lds\n\t"
    "v_max3_f32 v217, v217, v152, v153\n\t"
    "v_mfma_f32_16x16x32_bf16 v[156:159], a[184:187], v[116:119], v[156:159]\n\t"
    "ds_read_b64_tr_b16 a[216:217], v19 offset:32768\n\t"
    "ds_read_b64_tr_b16 a[218:219], v19 offset:40960\n\t"
    "v_mfma_f32_16x16x32_bf16 v[156:159], a[188:191], v[124:127], v[156:159]\n\t"
    "v_max3_f32 v216, v216, v154, v155\n\t"
    "s_waitcnt lgkmcnt(6)\n\t"
    "v_mfma_f32_16x16x32_bf16 a[0:3], a[192:195], v[160:163], a[0:3]\n\t"
    "ds_read_b64_tr_b16 a[220:221], v19 offset:49152\n\t"
    "ds_read_b64_tr_b16 a[222:223], v19 offset:57344\n\t"
    "v_mfma_f32_16x16x32_bf16 a[0:3], a[196:199], v[176:179], a[0:3]\n\t"
    "buffer_load_dwordx4 v37, %[srdv], s62 offen offset:1024 lds\n\t"
    "v_mfma_f32_16x16x32_bf16 a[4:7], a[192:195], v[164:167], a[4:7]\n\t"
    "ds_read_b64_tr_b16 a[224:225], v20 offset:32768\n\t"
    "ds_read_b64_tr_b16 a[226:227], v20 offset:40960\n\t"
    "v_mfma_f32_16x16x32_bf16 a[4:7], a[196:199], v[180:183], a[4:7]\n\t"
    "v_max3_f32 v217, v217, v156, v157\n\t"
    "v_max3_f32 v216, v216, v158, v159\n\t"
    "v_mfma_f32_16x16x32_bf16 a[8:11], a[200:203], v[160:163], a[8:11]\n\t"
    "ds_read_b64_tr_b16 a[228:229], v20 offset:49152\n\t"
    "ds_read_b64_tr_b16 a[230:231], v20 offset:57344\n\t"
    "v_mfma_f32_16x16x32_bf16 a[8:11], a[204:207], v[176:179], a[8:11]\n\t"
    "v_max_f32 v216, v216, v217\n\t"
    "v_cmp_lt_f32 vcc, s66, v216\n\t"
    "v_mfma_f32_16x16x32_bf16 a[12:15], a[200:203], v[164:167], a[12:15]\n\t"
    "ds_read_b64_tr_b16 a[232:233], v21 offset:32768\n\t"
    "ds_read_b64_tr_b16 a[234:235], v21 offset:40960\n\t"
    "v_mfma_f32_16x16x32_bf16 a[12:15], a[204:207], v[180:183], a[12:15]\n\t"
    "s_cbranch_vccnz Lsa14_%=\n\t"
    "Lba13_%=:\n\t"
    "s_waitcnt lgkmcnt(6)\n\t"
    "v_mfma_f32_16x16x32_bf16 a[16:19], a[208:211], v[160:163], a[16:19]\n\t"
    "ds_read_b64_tr_b16 a[236:237], v21 offset:49152\n\t"
    "ds_read_b64_tr_b16 a[238:239], v21 offset:57344\n\t"
    "v_mfma_f32_16x16x32_bf16 a[16:19], a[212:215], v[176:179], a[16:19]\n\t"
    "buffer_load_dwordx4 v38, %[srdv], s62 offen offset:2048 lds\n\t"
    "v_mfma_f32_16x16x32_bf16 a[20:23], a[208:211], v[164:167], a[20:23]\n\t"
    "ds_read_b64_tr_b16 a[240:241], v22 offset:32768\n\t"
    "ds_read_b64_tr_b16 a[242:243], v22 offset:40960\n\t"
    "v_mfma_f32_16x16x32_bf16 a[20:23], a[212:215], v[180:183], a[20:23]\n\t"
    "buffer_load_dwordx4 v39, %[srdv], s62 offen offset:3072 lds\n\t"
    "v_mfma_f32_16x16x32_bf16 a[24:27], a[216:219], v[160:163], a[24:27]\n\t"
    "ds_read_b64_tr_b16 a[244:245], v22 offset:49152\n\t"
    "ds_read_b64_tr_b16 a[246:247], v22 offset:57344\n\t"
    "v_mfma_f32_16x16x32_bf16 a[24:27], a[220:223], v[176:179], a[24:27]\n\t"
    "s_add_u32 s62, s62, 0x7000\n\t"
    "s_add_u32 m0, s63, 0x0\n\t"
    "s_add_u32 s68, s72, 0x0\n\t"
    "v_mfma_f32_16x16x32_bf16 a[28:31], a[216:219], v[164:167], a[28:31]\n\t"
    "ds_read_b64_tr_b16 a[248:249], v23 offset:32768\n\t"
    "ds_read_b64_tr_b16 a[250:251], v23 offset:40960\n\t"
    "v_mfma_f32_16x16x32_bf16 a[28:31], a[220:223], v[180:183], a[28:31]\n\t"
    "buffer_load_dwordx4 v48, %[srdwo], s68 offen lds\n\t"
    "s_waitcnt lgkmcnt(6)\n\t"
    "v_mfma_f32_16x16x32_bf16 a[32:35], a[224:227], v[160:163], a[32:35]\n\t"
    "ds_read_b64_tr_b16 a[252:253], v23 offset:49152\n\t"
    "ds_read_b64_tr_b16 a[254:255], v23 offset:57344\n\t"
    "v_mfma_f32_16x16x32_bf16 a[32:35], a[228:231], v[176:179], a[32:35]\n\t"
    "v_exp_f32 v128, v128\n\t"
    "v_mfma_f32_16x16x32_bf16 a[36:39], a[224:227], v[164:167], a[36:39]\n\t"
    "ds_read_b64_tr_b16 a[192:193], v16 offset:33024\n\t"
    "ds_read_b64_tr_b16 a[194:195], v16 offset:41216\n\t"
    "v_mfma_f32_16x16x32_bf16 a[36:39], a[228:231], v[180:183], a[36:39]\n\t"
    "v_exp_f32 v129, v129\n\t"
    "v_mfma_f32_16x16x32_bf16 a[40:43], a[232:235], v[160:163], a[40:43]\n\t"
    "ds_read_b64_tr_b16 a[196:197], v16 offset:49408\n\t"
    "ds_read_b64_tr_b16 a[198:199], v16 offset:57600\n\t"
    "v_mfma_f32_16x16x32_bf16 a[40:43], a[236:239], v[176:179], a[40:43]\n\t"
    "v_exp_f32 v130, v130\n\t"
    "v_mfma_f32_16x16x32_bf16 a[44:47], a[232:235], v[164:167], a[44:47]\n\t"
    "ds_read_b64_tr_b16 a[200:201], v17 offset:33024\n\t"
    "ds_read_b64_tr_b16 a[202:203], v17 offset:41216\n\t"
    "v_mfma_f32_16x16x32_bf16 a[44:47], a[236:239], v[180:183], a[44:47]\n\t"
    "buffer_load_dwordx4 v49, %[srdwo], s68 offen offset:1024 lds\n\t"
    "s_waitcnt lgkmcnt(6)\n\t"
    "v_mfma_f32_16x16x32_bf16 a[48:51], a[240:243], v[160:163], a[48:51]\n\t"
    "ds_read_b64_tr_b16 a[204:205], v17 offset:49408\n\t"
    "ds_read_b64_tr_b16 a[206:207], v17 offset:57600\n\t"
    "v_mfma_f32_16x16x32_bf16 a[48:51], a[244:247], v[176:179], a[48:51]\n\t"
    "v_exp_f32 v131, v131\n\t"
    "v_mfma_f32_16x16x32_bf16 a[52:55], a[240:243], v[164:167], a[52:55]\n\t"
    "ds_read_b64_tr_b16 a[208:209], v18 offset:33024\n\t"
    "ds_read_b64_tr_b16 a[210:211], v18 offset:41216\n\t"
    "v_mfma_f32_16x16x32_bf16 a[52:55], a[244:247], v[180:183], a[52:55]\n\t"
    "v_exp_f32 v136, v136\n\t"
    "v_mfma_f32_16x16x32_bf16 a[56:59], a[248:251], v[160:163], a[56:59]\n\t"
    "ds_read_b64_tr_b16 a[212:213], v18 offset:49408\n\t"
    "ds_read_b64_tr_b16 a[214:215], v18 offset:57600\n\t"
    "v_mfma_f32_16x16x32_bf16 a[56:59], a[252:255], v[176:179], a[56:59]\n\t"
    "v_exp_f32 v137, v137\n\t"
    "v_mfma_f32_16x16x32_bf16 a[60:63], a[248:251], v[164:167], a[60:63]\n\t"
    "ds_read_b64_tr_b16 a[216:217], v19 offset:33024\n\t"
    "ds_read_b64_tr_b16 a[218:219], v19 offset:41216\n\t"
    "v_mfma_f32_16x16x32_bf16 a[60:63], a[252:255], v[180:183], a[60:63]\n\t"
    "buffer_load_dwordx4 v50, %[srdwo], s68 offen offset:2048 lds\n\t"
    "s_waitcnt lgkmcnt(6)\n\t"
    "v_mfma_f32_16x16x32_bf16 a[64:67], a[192:195], v[160:163], a[64:67]\n\t"
    "ds_read_b64_tr_b16 a[220:221], v19 offset:49408\n\t"
    "ds_read_b64_tr_b16 a[222:223], v19 offset:57600\n\t"
    "v_mfma_f32_16x16x32_bf16 a[64:67], a[196:199], v[176:179], a[64:67]\n\t"
    "v_exp_f32 v138, v138\n\t"
    "v_mfma_f32_16x16x32_bf16 a[68:71], a[192:195], v[164:167], a[68:71]\n\t"
    "ds_read_b64_tr_b16 a[224:225], v20 offset:33024\n\t"
    "ds_read_b64_tr_b16 a[226:227], v20 offset:41216\n\t"
    "v_mfma_f32_16x16x32_bf16 a[68:71], a[196:199], v[180:183], a[68:71]\n\t"
    "v_exp_f32 v139, v139\n\t"
    "v_mfma_f32_16x16x32_bf16 a[72:75], a[200:203], v[160:163], a[72:75]\n\t"
    "ds_read_b64_tr_b16 a[228:229], v20 offset:49408\n\t"
    "ds_read_b64_tr_b16 a[230:231], v20 offset:57600\n\t"
    "v_mfma_f32_16x16x32_bf16 a[72:75], a[204:207], v[176:179], a[72:75]\n\t"
    "v_exp_f32 v132, v132\n\t"
    "v_mfma_f32_16x16x32_bf16 a[76:79], a[200:203], v[164:167], a[76:79]\n\t"
    "ds_read_b64_tr_b16 a[232:233], v21 offset:33024\n\t"
    "ds_read_b64_tr_b16 a[234:235], v21 offset:41216\n\t"
    "v_mfma_f32_16x16x32_bf16 a[76:79], a[204:207], v[180:183], a[76:79]\n\t"
    "buffer_load_dwordx4 v51, %[srdwo], s68 offen offset:3072 lds\n\t"
    "s_waitcnt lgkmcnt(6)\n\t"
    "v_mfma_f32_16x16x32_bf16 a[80:83], a[208:211], v[160:163], a[80:83]\n\t"
    "ds_read_b64_tr_b16 a[236:237], v21 offset:49408\n\t"
    "ds_read_b64_tr_b16 a[238:239], v21 offset:57600\n\t"
    "v_mfma_f32_16x16x32_bf16 a[80:83], a[212:215], v[176:179], a[80:83]\n\t"
    "v_exp_f32 v133, v133\n\t"
    "v_mfma_f32_16x16x32_bf16 a[84:87], a[208:211], v[164:167], a[84:87]\n\t"
    "ds_read_b64_tr_b16 a[240:241], v22 offset:33024\n\t"
    "ds_read_b64_tr_b16 a[242:243], v22 offset:41216\n\t"
    "v_mfma_f32_16x16x32_bf16 a[84:87], a[212:215], v[180:183], a[84:87]\n\t"
    "v_exp_f32 v134, v134\n\t"
    "v_mfma_f32_16x16x32_bf16 a[88:91], a[216:219], v[160:163], a[88:91]\n\t"
    "ds_read_b64_tr_b16 a[244:245], v22 offset:49408\n\t"
    "ds_read_b64_tr_b16 a[246:247], v22 offset:57600\n\t"
    "v_mfma_f32_16x16x32_bf16 a[88:91], a[220:223], v[176:179], a[88:91]\n\t"
    "s_add_u32 m0, s63, 0x1000\n\t"
    "s_add_u32 s68, s72, 0x7000\n\t"
    "v_mfma_f32_16x16x32_bf16 a[92:95], a[216:219], v[164:167], a[92:95]\n\t"
    "ds_read_b64_tr_b16 a[248:249], v23 offset:33024\n\t"
    "ds_read_b64_tr_b16 a[250:251], v23 offset:41216\n\t"
    "v_mfma_f32_16x16x32_bf16 a[92:95], a[220:223], v[180:183], a[92:95]\n\t"
    "buffer_load_dwordx4 v48, %[srdwo], s68 offen lds\n\t"
    "s_waitcnt lgkmcnt(6)\n\t"
    "v_mfma_f32_16x16x32_bf16 a[96:99], a[224:227], v[160:163], a[96:99]\n\t"
    "ds_read_b64_tr_b16 a[252:253], v23 offset:49408\n\t"
    "ds_read_b64_tr_b16 a[254:255], v23 offset:57600\n\t"
    "v_mfma_f32_16x16x32_bf16 a[96:99], a[228:231], v[176:179], a[96:99]\n\t"
    "v_exp_f32 v135, v135\n\t"
    "v_mfma_f32_16x16x32_bf16 a[100:103], a[224:227], v[164:167], a[100:103]\n\t"
    "v_exp_f32 v140, v140\n\t"
    "v_mfma_f32_16x16x32_bf16 a[100:103], a[228:231], v[180:183], a[100:103]\n\t"
    "v_exp_f32 v141, v141\n\t"
    "v_mfma_f32_16x16x32_bf16 a[104:107], a[232:235], v[160:163], a[104:107]\n\t"
    "ds_read_b128 a[128:131], v32 offset:32768\n\t"
    "ds_read_b128 a[132:135], v33 offset:32768\n\t"
    "v_mfma_f32_16x16x32_bf16 a[104:107], a[236:239], v[176:179], a[104:107]\n\t"
    "v_exp_f32 v142, v142\n\t"
    "v_mfma_f32_16x16x32_bf16 a[108:111], a[232:235], v[164:167], a[108:111]\n\t"
    "ds_read_b128 a[136:139], v34 offset:32768\n\t"
    "ds_read_b128 a[140:143], v35 offset:32768\n\t"
    "v_mfma_f32_16x16x32_bf16 a[108:111], a[236:239], v[180:183], a[108:111]\n\t"
    "buffer_load_dwordx4 v49, %[srdwo], s68 offen offset:1024 lds\n\t"
    "s_waitcnt lgkmcnt(4)\n\t"
    "v_mfma_f32_16x16x32_bf16 a[112:115], a[240:243], v[160:163], a[112:115]\n\t"
    "ds_read_b128 a[144:147], v32 offset:33024\n\t"
    "ds_read_b128 a[148:151], v33 offset:33024\n\t"
    "v_mfma_f32_16x16x32_bf16 a[112:115], a[244:247], v[176:179], a[112:115]\n\t"
    "v_exp_f32 v143, v143\n\t"
    "v_mfma_f32_16x16x32_bf16 a[116:119], a[240:243], v[164:167], a[116:119]\n\t"
    "ds_read_b128 a[152:155], v34 offset:33024\n\t"
    "ds_read_b128 a[156:159], v35 offset:33024\n\t"
    "v_mfma_f32_16x16x32_bf16 a[116:119], a[244:247], v[180:183], a[116:119]\n\t"
    "v_mfma_f32_16x16x32_bf16 a[120:123], a[248:251], v[160:163], a[120:123]\n\t"
    "v_mfma_f32_16x16x32_bf16 a[120:123], a[252:255], v[176:179], a[120:123]\n\t"
    "buffer_load_dwordx4 v50, %[srdwo], s68 offen offset:2048 lds\n\t"
    "v_mfma_f32_16x16x32_bf16 a[124:127], a[248:251], v[164:167], a[124:127]\n\t"
    "v_mfma_f32_16x16x32_bf16 a[124:127], a[252:255], v[180:183], a[124:127]\n\t"
    "buffer_load_dwordx4 v51, %[srdwo], s68 offen offset:3072 lds\n\t"
    "v_mfma_f32_16x16x32_bf16 v[200:203], v[208:211], v[160:163], v[200:203]\n\t"
    "v_mfma_f32_16x16x32_bf16 v[200:203], v[208:211], v[176:179], v[200:203]\n\t"
    "v_mfma_f32_16x16x32_bf16 v[204:207], v[208:211], v[164:167], v[204:207]\n\t"
    "v_mfma_f32_16x16x32_bf16 v[204:207], v[208:211], v[180:183], v[204:207]\n\t"
    "s_waitcnt lgkmcnt(0)\n\t"
    "v_mfma_f32_16x16x32_bf16 v[160:163], a[128:131], v[64:67], v[192:195]\n\t"
    "v_exp_f32 v144, v144\n\t"
    "v_mfma_f32_16x16x32_bf16 v[160:163], a[132:135], v[72:75], v[160:163]\n\t"
    "s_cmp_lg_u32 s65, 0\n\t"
    "s_cbranch_scc1 Lsb16_%=\n\t"
    "Lbb15_%=:\n\t"
    "v_mfma_f32_16x16x32_bf16 v[160:163], a[136:139], v[80:83], v[160:163]\n\t"
    "ds_read_b128 a[160:163], v32 offset:40960\n\t"
    "ds_read_b128 a[164:167], v33 offset:40960\n\t"
    "v_mfma_f32_16x16x32_bf16 v[160:163], a[140:143], v[88:91], v[160:163]\n\t"
    "v_exp_f32 v145, v145\n\t"
    "v_mfma_f32_16x16x32_bf16 v[160:163], a[144:147], v[96:99], v[160:163]\n\t"
    "ds_read_b128 a[168:171], v34 offset:40960\n\t"
    "ds_read_b128 a[172:175], v35 offset:40960\n\t"
    "v_mfma_f32_16x16x32_bf16 v[160:163], a[148:151], v[104:107], v[160:163]\n\t"
    "v_exp_f32 v146, v146\n\t"
    "v_mfma_f32_16x16x32_bf16 v[160:163], a[152:155], v[112:115], v[160:163]\n\t"
    "ds_read_b128 a[176:179], v32 offset:41216\n\t"
    "ds_read_b128 a[180:183], v33 offset:41216\n\t"
    "v_mfma_f32_16x16x32_bf16 v[160:163], a[156:159], v[120:123], v[160:163]\n\t"
    "v_exp_f32 v147, v147\n\t"
    "v_mfma_f32_16x16x32_bf16 v[164:167], a[128:131], v[68:71], v[196:199]\n\t"
    "ds_read_b128 a[184:187], v34 offset:41216\n\t"
    "ds_read_b128 a[188:191], v35 offset:41216\n\t"
    "v_mfma_f32_16x16x32_bf16 v[164:167], a[132:135], v[76:79], v[164:167]\n\t"
    "v_exp_f32 v152, v152\n\t"
    "v_mfma_f32_16x16x32_bf16 v[164:167], a[136:139], v[84:87], v[164:167]\n\t"
    "v_exp_f32 v153, v153\n\t"
    "v_mfma_f32_16x16x32_bf16 v[164:167], a[140:143], v[92:95], v[164:167]\n\t"
    "v_exp_f32 v154, v154\n\t"
    "v_mfma_f32_16x16x32_bf16 v[164:167], a[144:147], v[100:103], v[164:167]\n\t"
    "v_exp_f32 v155, v155\n\t"
    "v_mfma_f32_16x16x32_bf16 v[164:167], a[148:151], v[108:111], v[164:167]\n\t"
    "v_exp_f32 v148, v148\n\t"
    "v_mfma_f32_16x16x32_bf16 v[164:167], a[152:155], v[116:119], v[164:167]\n\t"
    "v_exp_f32 v149, v149\n\t"
    "v_mfma_f32_16x16x32_bf16 v[164:167], a[156:159], v[124:127], v[164:167]\n\t"
    "v_cvt_pk_bf16_f32 v128, v128, v129\n\t"
    "s_waitcnt lgkmcnt(0)\n\t"
    "v_mfma_f32_16x16x32_bf16 v[168:171], a[160:163], v[64:67], v[192:195]\n\t"
    "v_exp_f32 v150, v150\n\t"
    "v_mfma_f32_16x16x32_bf16 v[168:171], a[164:167], v[72:75], v[168:171]\n\t"
    "v_exp_f32 v151, v151\n\t"
    "v_mfma_f32_16x16x32_bf16 v[168:171], a[168:171], v[80:83], v[168:171]\n\t"
    "ds_read_b128 a[128:131], v32 offset:49152\n\t"
    "ds_read_b128 a[132:135], v33 offset:49152\n\t"
    "v_mfma_f32_16x16x32_bf16 v[168:171], a[172:175], v[88:91], v[168:171]\n\t"
    "v_exp_f32 v156, v156\n\t"
    "v_mfma_f32_16x16x32_bf16 v[168:171], a[176:179], v[96:99], v[168:171]\n\t"
    "ds_read_b128 a[136:139], v34 offset:49152\n\t"
    "ds_read_b128 a[140:143], v35 offset:49152\n\t"
    "v_mfma_f32_16x16x32_bf16 v[168:171], a[180:183], v[104:107], v[168:171]\n\t"
    "v_exp_f32 v157, v157\n\t"
    "v_mfma_f32_16x16x32_bf16 v[168:171], a[184:187], v[112:115], v[168:171]\n\t"
    "ds_read_b128 a[144:147], v32 offset:49408\n\t"
    "ds_read_b128 a[148:151], v33 offset:49408\n\t"
    "v_mfma_f32_16x16x32_bf16 v[168:171], a[188:191], v[120:123], v[168:171]\n\t"
    "v_exp_f32 v158, v158\n\t"
    "v_mfma_f32_16x16x32_bf16 v[172:175], a[160:163], v[68:71], v[196:199]\n\t"
    "ds_read_b128 a[152:155], v34 offset:49408\n\t"
    "ds_read_b128 a[156:159], v35 offset:49408\n\t"
    "v_mfma_f32_16x16x32_bf16 v[172:175], a[164:167], v[76:79], v[172:175]\n\t"
    "v_exp_f32 v159, v159\n\t"
    "v_mfma_f32_16x16x32_bf16 v[172:175], a[168:171], v[84:87], v[172:175]\n\t"
    "v_cvt_pk_bf16_f32 v129, v130, v131\n\t"
    "v_cvt_pk_bf16_f32 v130, v136, v137\n\t"
    "v_mfma_f32_16x16x32_bf16 v[172:175], a[172:175], v[92:95], v[172:175]\n\t"
    "v_cvt_pk_bf16_f32 v131, v138, v139\n\t"
    "v_cvt_pk_bf16_f32 v132, v132, v133\n\t"
    "v_mfma_f32_16x16x32_bf16 v[172:175], a[176:179], v[100:103], v[172:175]\n\t"
    "v_cvt_pk_bf16_f32 v133, v134, v135\n\t"
    "v_cvt_pk_bf16_f32 v134, v140, v141\n\t"
    "v_mfma_f32_16x16x32_bf16 v[172:175], a[180:183], v[108:111], v[172:175]\n\t"
    "v_cvt_pk_bf16_f32 v135, v142, v143\n\t"
    "v_cvt_pk_bf16_f32 v144, v144, v145\n\t"
    "v_mfma_f32_16x16x32_bf16 v[172:175], a[184:187], v[116:119], v[172:175]\n\t"
    "v_cvt_pk_bf16_f32 v145, v146, v147\n\t"
    "v_cvt_pk_bf16_f32 v146, v152, v153\n\t"
    "v_mfma_f32_16x16x32_bf16 v[172:175], a[188:191], v[124:127], v[172:175]\n\t"
    "v_cvt_pk_bf16_f32 v147, v154, v155\n\t"
    "s_waitcnt lgkmcnt(0)\n\t"
    "v_mfma_f32_16x16x32_bf16 v[176:179], a[128:131], v[64:67], v[192:195]\n\t"
    "v_cvt_pk_bf16_f32 v148, v148, v149\n\t"
    "v_cvt_pk_bf16_f32 v149, v150, v151\n\t"
    "v_mfma_f32_16x16x32_bf16 v[176:179], a[132:135], v[72:75], v[176:179]\n\t"
    "v_cvt_pk_bf16_f32 v150, v156, v157\n\t"
    "v_cvt_pk_bf16_f32 v151, v158, v159\n\t"
    "v_mfma_f32_16x16x32_bf16 v[176:179], a[136:139], v[80:83], v[176:179]\n\t"
    "ds_read_b128 a[160:163], v32 offset:57344\n\t"
    "ds_read_b128 a[164:167], v33 offset:57344\n\t"
    "v_mfma_f32_16x16x32_bf16 v[176:179], a[140:143], v[88:91], v[176:179]\n\t"
    "v_max3_f32 v216, v160, v161, v162\n\t"
    "v_max3_f32 v217, v163, v164, v165\n\t"
    "v_mfma_f32_16x16x32_bf16 v[176:179], a[144:147], v[96:99], v[176:179]\n\t"
    "ds_read_b128 a[168:171], v34 offset:57344\n\t"
    "ds_read_b128 a[172:175], v35 offset:57344\n\t"
    "v_mfma_f32_16x16x32_bf16 v[176:179], a[148:151], v[104:107], v[176:179]\n\t"
    "v_max3_f32 v216, v216, v166, v167\n\t"
    "v_max3_f32 v217, v217, v168, v169\n\t"
    "v_mfma_f32_16x16x32_bf16 v[176:179], a[152:155], v[112:115], v[176:179]\n\t"
    "ds_read_b128 a[176:179], v32 offset:57600\n\t"
    "ds_read_b128 a[180:183], v33 offset:57600\n\t"
    "v_mfma_f32_16x16x32_bf16 v[176:179], a[156:159], v[120:123], v[176:179]\n\t"
    "v_max3_f32 v216, v216, v170, v171\n\t"
    "v_max3_f32 v217, v217, v172, v173\n\t"
    "v_mfma_f32_16x16x32_bf16 v[180:183], a[128:131], v[68:71], v[196:199]\n\t"
    "ds_read_b128 a[184:187], v34 offset:57600\n\t"
    "ds_read_b128 a[188:191], v35 offset:57600\n\t"
    "v_mfma_f32_16x16x32_bf16 v[180:183], a[132:135], v[76:79], v[180:183]\n\t"
    "v_max3_f32 v216, v216, v174, v175\n\t"
    "v_mfma_f32_16x16x32_bf16 v[180:183], a[136:139], v[84:87], v[180:183]\n\t"
    "v_mfma_f32_16x16x32_bf16 v[180:183], a[140:143], v[92:95], v[180:183]\n\t"
    "v_mfma_f32_16x16x32_bf16 v[180:183], a[144:147], v[100:103], v[180:183]\n\t"
    "v_max3_f32 v217, v217, v176, v177\n\t"
    "v_max3_f32 v216, v216, v178, v179\n\t"
    "v_mfma_f32_16x16x32_bf16 v[180:183], a[148:151], v[108:111], v[180:183]\n\t"
    "v_mfma_f32_16x16x32_bf16 v[180:183], a[152:155], v[116:119], v[180:183]\n\t"
    "v_mfma_f32_16x16x32_bf16 v[180:183], a[156:159], v[124:127], v[180:183]\n\t"
    "s_waitcnt lgkmcnt(0)\n\t"
    "v_mfma_f32_16x16x32_bf16 v[184:187], a[160:163], v[64:67], v[192:195]\n\t"
    "s_waitcnt vmcnt(0)\n\t"
    "s_barrier\n\t"
    "v_mfma_f32_16x16x32_bf16 v[184:187], a[164:167], v[72:75], v[184:187]\n\t"
    "s_add_u32 m0, s63, 0x18000\n\t"
    "v_mfma_f32_16x16x32_bf16 v[184:187], a[168:171], v[80:83], v[184:187]\n\t"
    "ds_read_b64_tr_b16 a[192:193], v16\n\t"
    "ds_read_b64_tr_b16 a[194:195], v16 offset:8192\n\t"
    "v_mfma_f32_16x16x32_bf16 v[184:187], a[172:175], v[88:91], v[184:187]\n\t"
    "buffer_load_dwordx4 v36, %[srdv], s62 offen lds\n\t"
    "v_mfma_f32_16x16x32_bf16 v[184:187], a[176:179], v[96:99], v[184:187]\n\t"
    "ds_read_b64_tr_b16 a[196:197], v16 offset:16384\n\t"
    "ds_read_b64_tr_b16 a[198:199], v16 offset:24576\n\t"
    "v_mfma_f32_16x16x32_bf16 v[184:187], a[180:183], v[104:107], v[184:187]\n\t"
    "buffer_load_dwordx4 v37, %[srdv], s62 offen offset:1024 lds\n\t"
    "v_max3_f32 v217, v217, v180, v181\n\t"
    "v_mfma_f32_16x16x32_bf16 v[184:187], a[184:187], v[112:115], v[184:187]\n\t"
    "ds_read_b64_tr_b16 a[200:201], v17\n\t"
    "ds_read_b64_tr_b16 a[202:203], v17 offset:8192\n\t"
    "v_mfma_f32_16x16x32_bf16 v[184:187], a[188:191], v[120:123], v[184:187]\n\t"
    "buffer_load_dwordx4 v38, %[srdv], s62 offen offset:2048 lds\n\t"
    "v_max3_f32 v216, v216, v182, v183\n\t"
    "v_mfma_f32_16x16x32_bf16 v[188:191], a[160:163], v[68:71], v[196:199]\n\t"
    "ds_read_b64_tr_b16 a[204:205], v17 offset:16384\n\t"
    "ds_read_b64_tr_b16 a[206:207], v17 offset:24576\n\t"
    "v_mfma_f32_16x16x32_bf16 v[188:191], a[164:167], v[76:79], v[188:191]\n\t"
    "buffer_load_dwordx4 v39, %[srdv], s62 offen offset:3072 lds\n\t"
    "v_mfma_f32_16x16x32_bf16 v[188:191], a[168:171], v[84:87], v[188:191]\n\t"
    "ds_read_b64_tr_b16 a[208:209], v18\n\t"
    "ds_read_b64_tr_b16 a[210:211], v18 offset:8192\n\t"
    "v_mfma_f32_16x16x32_bf16 v[188:191], a[172:175], v[92:95], v[188:191]\n\t"
    "s_add_u32 m0, s63, 0x19000\n\t"
    "s_add_u32 s62, s62, 0x1000\n\t"
    "v_mfma_f32_16x16x32_bf16 v[188:191], a[176:179], v[100:103], v[188:191]\n\t"
    "ds_read_b64_tr_b16 a[212:213], v18 offset:16384\n\t"
    "ds_read_b64_tr_b16 a[214:215], v18 offset:24576\n\t"
    "v_mfma_f32_16x16x32_bf16 v[188:191], a[180:183], v[108:111], v[188:191]\n\t"
    "buffer_load_dwordx4 v36, %[srdv], s62 offen lds\n\t"
    "v_max3_f32 v217, v217, v184, v185\n\t"
    "v_mfma_f32_16x16x32_bf16 v[188:191], a[184:187], v[116:119], v[188:191]\n\t"
    "ds_read_b64_tr_b16 a[216:217], v19\n\t"
    "ds_read_b64_tr_b16 a[218:219], v19 offset:8192\n\t"
    "v_mfma_f32_16x16x32_bf16 v[188:191], a[188:191], v[124:127], v[188:191]\n\t"
    "v_max3_f32 v216, v216, v186, v187\n\t"
    "s_waitcnt lgkmcnt(6)\n\t"
    "v_mfma_f32_16x16x32_bf16 a[0:3], a[192:195], v[128:131], a[0:3]\n\t"
    "ds_read_b64_tr_b16 a[220:221], v19 offset:16384\n\t"
    "ds_read_b64_tr_b16 a[222:223], v19 offset:24576\n\t"
    "v_mfma_f32_16x16x32_bf16 a[0:3], a[196:199], v[144:147], a[0:3]\n\t"
    "buffer_load_dwordx4 v37, %[srdv], s62 offen offset:1024 lds\n\t"
    "v_mfma_f32_16x16x32_bf16 a[4:7], a[192:195], v[132:135], a[4:7]\n\t"
    "ds_read_b64_tr_b16 a[224:225], v20\n\t"
    "ds_read_b64_tr_b16 a[226:227], v20 offset:8192\n\t"
    "v_mfma_f32_16x16x32_bf16 a[4:7], a[196:199], v[148:151], a[4:7]\n\t"
    "v_max3_f32 v217, v217, v188, v189\n\t"
    "v_max3_f32 v216, v216, v190, v191\n\t"
    "v_mfma_f32_16x16x32_bf16 a[8:11], a[200:203], v[128:131], a[8:11]\n\t"
    "ds_read_b64_tr_b16 a[228:229], v20 offset:16384\n\t"
    "ds_read_b64_tr_b16 a[230:231], v20 offset:24576\n\t"
    "v_mfma_f32_16x16x32_bf16 a[8:11], a[204:207], v[144:147], a[8:11]\n\t"
    "v_max_f32 v216, v216, v217\n\t"
    "v_cmp_lt_f32 vcc, s66, v216\n\t"
    "v_mfma_f32_16x16x32_bf16 a[12:15], a[200:203], v[132:135], a[12:15]\n\t"
    "ds_read_b64_tr_b16 a[232:233], v21\n\t"
    "ds_read_b64_tr_b16 a[234:235], v21 offset:8192\n\t"
    "v_mfma_f32_16x16x32_bf16 a[12:15], a[204:207], v[148:151], a[12:15]\n\t"
    "s_cbranch_vccnz Lsa18_%=\n\t"
    "Lba17_%=:\n\t"
    "s_waitcnt lgkmcnt(6)\n\t"
    "v_mfma_f32_16x16x32_bf16 a[16:19], a[208:211], v[128:131], a[16:19]\n\t"
    "ds_read_b64_tr_b16 a[236:237], v21 offset:16384\n\t"
    "ds_read_b64_tr_b16 a[238:239], v21 offset:24576\n\t"
    "v_mfma_f32_16x16x32_bf16 a[16:19], a[212:215], v[144:147], a[16:19]\n\t"
    "buffer_load_dwordx4 v38, %[srdv], s62 offen offset:2048 lds\n\t"
    "v_mfma_f32_16x16x32_bf16 a[20:23], a[208:211], v[132:135], a[20:23]\n\t"
    "ds_read_b64_tr_b16 a[240:241], v22\n\t"
    "ds_read_b64_tr_b16 a[242:243], v22 offset:8192\n\t"
    "v_mfma_f32_16x16x32_bf16 a[20:23], a[212:215], v[148:151], a[20:23]\n\t"
    "buffer_load_dwordx4 v39, %[srdv], s62 offen offset:3072 lds\n\t"
    "v_mfma_f32_16x16x32_bf16 a[24:27], a[216:219], v[128:131], a[24:27]\n\t"
    "ds_read_b64_tr_b16 a[244:245], v22 offset:16384\n\t"
    "ds_read_b64_tr_b16 a[246:247], v22 offset:24576\n\t"
    "v_mfma_f32_16x16x32_bf16 a[24:27], a[220:223], v[144:147], a[24:27]\n\t"
    "s_add_u32 s62, s62, 0x7000\n\t"
    "s_add_u32 m0, s63, 0x8000\n\t"
    "s_add_u32 s68, s72, 0x1c000\n\t"
    "v_mfma_f32_16x16x32_bf16 a[28:31], a[216:219], v[132:135], a[28:31]\n\t"
    "ds_read_b64_tr_b16 a[248:249], v23\n\t"
    "ds_read_b64_tr_b16 a[250:251], v23 offset:8192\n\t"
    "v_mfma_f32_16x16x32_bf16 a[28:31], a[220:223], v[148:151], a[28:31]\n\t"
    "buffer_load_dwordx4 v48, %[srdwo], s68 offen lds\n\t"
    "s_waitcnt lgkmcnt(6)\n\t"
    "v_mfma_f32_16x16x32_bf16 a[32:35], a[224:227], v[128:131], a[32:35]\n\t"
    "ds_read_b64_tr_b16 a[252:253], v23 offset:16384\n\t"
    "ds_read_b64_tr_b16 a[254:255], v23 offset:24576\n\t"
    "v_mfma_f32_16x16x32_bf16 a[32:35], a[228:231], v[144:147], a[32:35]\n\t"
    "v_exp_f32 v160, v160\n\t"
    "v_mfma_f32_16x16x32_bf16 a[36:39], a[224:227], v[132:135], a[36:39]\n\t"
    "ds_read_b64_tr_b16 a[192:193], v16 offset:256\n\t"
    "ds_read_b64_tr_b16 a[194:195], v16 offset:8448\n\t"
    "v_mfma_f32_16x16x32_bf16 a[36:39], a[228:231], v[148:151], a[36:39]\n\t"
    "v_exp_f32 v161, v161\n\t"
    "v_mfma_f32_16x16x32_bf16 a[40:43], a[232:235], v[128:131], a[40:43]\n\t"
    "ds_read_b64_tr_b16 a[196:197], v16 offset:16640\n\t"
    "ds_read_b64_tr_b16 a[198:199], v16 offset:24832\n\t"
    "v_mfma_f32_16x16x32_bf16 a[40:43], a[236:239], v[144:147], a[40:43]\n\t"
    "v_exp_f32 v162, v162\n\t"
    "v_mfma_f32_16x16x32_bf16 a[44:47], a[232:235], v[132:135], a[44:47]\n\t"
    "ds_read_b64_tr_b16 a[200:201], v17 offset:256\n\t"
    "ds_read_b64_tr_b16 a[202:203], v17 offset:8448\n\t"
    "v_mfma_f32_16x16x32_bf16 a[44:47], a[236:239], v[148:151], a[44:47]\n\t"
    "buffer_load_dwordx4 v49, %[srdwo], s68 offen offset:1024 lds\n\t"
    "s_waitcnt lgkmcnt(6)\n\t"
    "v_mfma_f32_16x16x32_bf16 a[48:51], a[240:243], v[128:131], a[48:51]\n\t"
    "ds_read_b64_tr_b16 a[204:205], v17 offset:16640\n\t"
    "ds_read_b64_tr_b16 a[206:207], v17 offset:24832\n\t"
    "v_mfma_f32_16x16x32_bf16 a[48:51], a[244:247], v[144:147], a[48:51]\n\t"
    "v_exp_f32 v163, v163\n\t"
    "v_mfma_f32_16x16x32_bf16 a[52:55], a[240:243], v[132:135], a[52:55]\n\t"
    "ds_read_b64_tr_b16 a[208:209], v18 offset:256\n\t"
    "ds_read_b64_tr_b16 a[210:211], v18 offset:8448\n\t"
    "v_mfma_f32_16x16x32_bf16 a[52:55], a[244:247], v[148:151], a[52:55]\n\t"
    "v_exp_f32 v168, v168\n\t"
    "v_mfma_f32_16x16x32_bf16 a[56:59], a[248:251], v[128:131], a[56:59]\n\t"
    "ds_read_b64_tr_b16 a[212:213], v18 offset:16640\n\t"
    "ds_read_b64_tr_b16 a[214:215], v18 offset:24832\n\t"
    "v_mfma_f32_16x16x32_bf16 a[56:59], a[252:255], v[144:147], a[56:59]\n\t"
    "v_exp_f32 v169, v169\n\t"
    "v_mfma_f32_16x16x32_bf16 a[60:63], a[248:251], v[132:135], a[60:63]\n\t"
    "ds_read_b64_tr_b16 a[216:217], v19 offset:256\n\t"
    "ds_read_b64_tr_b16 a[218:219], v19 offset:8448\n\t"
    "v_mfma_f32_16x16x32_bf16 a[60:63], a[252:255], v[148:151], a[60:63]\n\t"
    "buffer_load_dwordx4 v50, %[srdwo], s68 offen offset:2048 lds\n\t"
    "s_waitcnt lgkmcnt(6)\n\t"
    "v_mfma_f32_16x16x32_bf16 a[64:67], a[192:195], v[128:131], a[64:67]\n\t"
    "ds_read_b64_tr_b16 a[220:221], v19 offset:16640\n\t"
    "ds_read_b64_tr_b16 a[222:223], v19 offset:24832\n\t"
    "v_mfma_f32_16x16x32_bf16 a[64:67], a[196:199], v[144:147], a[64:67]\n\t"
    "v_exp_f32 v170, v170\n\t"
    "v_mfma_f32_16x16x32_bf16 a[68:71], a[192:195], v[132:135], a[68:71]\n\t"
    "ds_read_b64_tr_b16 a[224:225], v20 offset:256\n\t"
    "ds_read_b64_tr_b16 a[226:227], v20 offset:8448\n\t"
    "v_mfma_f32_16x16x32_bf16 a[68:71], a[196:199], v[148:151], a[68:71]\n\t"
    "v_exp_f32 v171, v171\n\t"
    "v_mfma_f32_16x16x32_bf16 a[72:75], a[200:203], v[128:131], a[72:75]\n\t"
    "ds_read_b64_tr_b16 a[228:229], v20 offset:16640\n\t"
    "ds_read_b64_tr_b16 a[230:231], v20 offset:24832\n\t"
    "v_mfma_f32_16x16x32_bf16 a[72:75], a[204:207], v[144:147], a[72:75]\n\t"
    "v_exp_f32 v164, v164\n\t"
    "v_mfma_f32_16x16x32_bf16 a[76:79], a[200:203], v[132:135], a[76:79]\n\t"
    "ds_read_b64_tr_b16 a[232:233], v21 offset:256\n\t"
    "ds_read_b64_tr_b16 a[234:235], v21 offset:8448\n\t"
    "v_mfma_f32_16x16x32_bf16 a[76:79], a[204:207], v[148:151], a[76:79]\n\t"
    "buffer_load_dwordx4 v51, %[srdwo], s68 offen offset:3072 lds\n\t"
    "s_waitcnt lgkmcnt(6)\n\t"
    "v_mfma_f32_16x16x32_bf16 a[80:83], a[208:211], v[128:131], a[80:83]\n\t"
    "ds_read_b64_tr_b16 a[236:237], v21 offset:16640\n\t"
    "ds_read_b64_tr_b16 a[238:239], v21 offset:24832\n\t"
    "v_mfma_f32_16x16x32_bf16 a[80:83], a[212:215], v[144:147], a[80:83]\n\t"
    "v_exp_f32 v165, v165\n\t"
    "v_mfma_f32_16x16x32_bf16 a[84:87], a[208:211], v[132:135], a[84:87]\n\t"
    "ds_read_b64_tr_b16 a[240:241], v22 offset:256\n\t"
    "ds_read_b64_tr_b16 a[242:243], v22 offset:8448\n\t"
    "v_mfma_f32_16x16x32_bf16 a[84:87], a[212:215], v[148:151], a[84:87]\n\t"
    "v_exp_f32 v166, v166\n\t"
    "v_mfma_f32_16x16x32_bf16 a[88:91], a[216:219], v[128:131], a[88:91]\n\t"
    "ds_read_b64_tr_b16 a[244:245], v22 offset:16640\n\t"
    "ds_read_b64_tr_b16 a[246:247], v22 offset:24832\n\t"
    "v_mfma_f32_16x16x32_bf16 a[88:91], a[220:223], v[144:147], a[88:91]\n\t"
    "s_add_u32 m0, s63, 0x9000\n\t"
    "s_add_u32 s68, s72, 0x23000\n\t"
    "v_mfma_f32_16x16x32_bf16 a[92:95], a[216:219], v[132:135], a[92:95]\n\t"
    "ds_read_b64_tr_b16 a[248:249], v23 offset:256\n\t"
    "ds_read_b64_tr_b16 a[250:251], v23 offset:8448\n\t"
    "v_mfma_f32_16x16x32_bf16 a[92:95], a[220:223], v[148:151], a[92:95]\n\t"
    "buffer_load_dwordx4 v48, %[srdwo], s68 offen lds\n\t"
    "s_waitcnt lgkmcnt(6)\n\t"
    "v_mfma_f32_16x16x32_bf16 a[96:99], a[224:227], v[128:131], a[96:99]\n\t"
    "ds_read_b64_tr_b16 a[252:253], v23 offset:16640\n\t"
    "ds_read_b64_tr_b16 a[254:255], v23 offset:24832\n\t"
    "v_mfma_f32_16x16x32_bf16 a[96:99], a[228:231], v[144:147], a[96:99]\n\t"
    "v_exp_f32 v167, v167\n\t"
    "v_mfma_f32_16x16x32_bf16 a[100:103], a[224:227], v[132:135], a[100:103]\n\t"
    "v_exp_f32 v172, v172\n\t"
    "v_mfma_f32_16x16x32_bf16 a[100:103], a[228:231], v[148:151], a[100:103]\n\t"
    "v_exp_f32 v173, v173\n\t"
    "v_mfma_f32_16x16x32_bf16 a[104:107], a[232:235], v[128:131], a[104:107]\n\t"
    "v_exp_f32 v174, v174\n\t"
    "v_mfma_f32_16x16x32_bf16 a[104:107], a[236:239], v[144:147], a[104:107]\n\t"
    "v_exp_f32 v175, v175\n\t"
    "v_mfma_f32_16x16x32_bf16 a[108:111], a[232:235], v[132:135], a[108:111]\n\t"
    "v_mfma_f32_16x16x32_bf16 a[108:111], a[236:239], v[148:151], a[108:111]\n\t"
    "buffer_load_dwordx4 v49, %[srdwo], s68 offen offset:1024 lds\n\t"
    "s_waitcnt lgkmcnt(0)\n\t"
    "v_mfma_f32_16x16x32_bf16 a[112:115], a[240:243], v[128:131], a[112:115]\n\t"
    "v_mfma_f32_16x16x32_bf16 a[112:115], a[244:247], v[144:147], a[112:115]\n\t"
    "v_mfma_f32_16x16x32_bf16 a[116:119], a[240:243], v[132:135], a[116:119]\n\t"
    "v_mfma_f32_16x16x32_bf16 a[116:119], a[244:247], v[148:151], a[116:119]\n\t"
    "v_mfma_f32_16x16x32_bf16 a[120:123], a[248:251], v[128:131], a[120:123]\n\t"
    "v_mfma_f32_16x16x32_bf16 a[120:123], a[252:255], v[144:147], a[120:123]\n\t"
    "buffer_load_dwordx4 v50, %[srdwo], s68 offen offset:2048 lds\n\t"
    "v_mfma_f32_16x16x32_bf16 a[124:127], a[248:251], v[132:135], a[124:127]\n\t"
    "v_mfma_f32_16x16x32_bf16 a[124:127], a[252:255], v[148:151], a[124:127]\n\t"
    "buffer_load_dwordx4 v51, %[srdwo], s68 offen offset:3072 lds\n\t"
    "v_mfma_f32_16x16x32_bf16 v[200:203], v[208:211], v[128:131], v[200:203]\n\t"
    "v_mfma_f32_16x16x32_bf16 v[200:203], v[208:211], v[144:147], v[200:203]\n\t"
    "v_mfma_f32_16x16x32_bf16 v[204:207], v[208:211], v[132:135], v[204:207]\n\t"
    "v_mfma_f32_16x16x32_bf16 v[204:207], v[208:211], v[148:151], v[204:207]\n\t"
    "s_waitcnt vmcnt(8)\n\t"
    "s_barrier\n\t"
    "s_cmp_lg_u32 s65, 0\n\t"
    "s_cbranch_scc1 Lsb20_%=\n\t"
    "Lbb19_%=:\n\t"
    "ds_read_b64_tr_b16 a[192:193], v16 offset:32768\n\t"
    "ds_read_b64_tr_b16 a[194:195], v16 offset:40960\n\t"
    "ds_read_b64_tr_b16 a[196:197], v16 offset:49152\n\t"
    "ds_read_b64_tr_b16 a[198:199], v16 offset:57344\n\t"
    "ds_read_b64_tr_b16 a[200:201], v17 offset:32768\n\t"
    "ds_read_b64_tr_b16 a[202:203], v17 offset:40960\n\t"
    "ds_read_b64_tr_b16 a[204:205], v17 offset:49152\n\t"
    "ds_read_b64_tr_b16 a[206:207], v17 offset:57344\n\t"
    "ds_read_b64_tr_b16 a[208:209], v18 offset:32768\n\t"
    "ds_read_b64_tr_b16 a[210:211], v18 offset:40960\n\t"
    "ds_read_b64_tr_b16 a[212:213], v18 offset:49152\n\t"
    "ds_read_b64_tr_b16 a[214:215], v18 offset:57344\n\t"
    "ds_read_b64_tr_b16 a[216:217], v19 offset:32768\n\t"
    "ds_read_b64_tr_b16 a[218:219], v19 offset:40960\n\t"
    "v_exp_f32 v176, v176\n\t"
    "v_exp_f32 v177, v177\n\t"
    "v_exp_f32 v178, v178\n\t"
    "v_exp_f32 v179, v179\n\t"
    "v_exp_f32 v184, v184\n\t"
    "v_exp_f32 v185, v185\n\t"
    "v_exp_f32 v186, v186\n\t"
    "v_exp_f32 v187, v187\n\t"
    "v_exp_f32 v180, v180\n\t"
    "v_exp_f32 v181, v181\n\t"
    "v_exp_f32 v182, v182\n\t"
    "v_exp_f32 v183, v183\n\t"
    "v_exp_f32 v188, v188\n\t"
    "v_exp_f32 v189, v189\n\t"
    "v_exp_f32 v190, v190\n\t"
    "v_exp_f32 v191, v191\n\t"
    "v_cvt_pk_bf16_f32 v160, v160, v161\n\t"
    "v_cvt_pk_bf16_f32 v161, v162, v163\n\t"
    "v_cvt_pk_bf16_f32 v162, v168, v169\n\t"
    "v_cvt_pk_bf16_f32 v163, v170, v171\n\t"
    "v_cvt_pk_bf16_f32 v164, v164, v165\n\t"
    "v_cvt_pk_bf16_f32 v165, v166, v167\n\t"
    "v_cvt_pk_bf16_f32 v166, v172, v173\n\t"
    "v_cvt_pk_bf16_f32 v167, v174, v175\n\t"
    "v_cvt_pk_bf16_f32 v176, v176, v177\n\t"
    "v_cvt_pk_bf16_f32 v177, v178, v179\n\t"
    "v_cvt_pk_bf16_f32 v178, v184, v185\n\t"
    "v_cvt_pk_bf16_f32 v179, v186, v187\n\t"
    "v_cvt_pk_bf16_f32 v180, v180, v181\n\t"
    "v_cvt_pk_bf16_f32 v181, v182, v183\n\t"
    "v_cvt_pk_bf16_f32 v182, v188, v189\n\t"
    "v_cvt_pk_bf16_f32 v183, v190, v191\n\t"
    "s_nop 0\n\t"
    "v_mfma_f32_16x16x32_bf16 v[200:203], v[208:211], v[160:163], v[200:203]\n\t"
    "ds_read_b64_tr_b16 a[220:221], v19 offset:49152\n\t"
    "ds_read_b64_tr_b16 a[222:223], v19 offset:57344\n\t"
    "v_mfma_f32_16x16x32_bf16 v[200:203], v[208:211], v[176:179], v[200:203]\n\t"
    "v_mfma_f32_16x16x32_bf16 v[204:207], v[208:211], v[164:167], v[204:207]\n\t"
    "ds_read_b64_tr_b16 a[224:225], v20 offset:32768\n\t"
    "ds_read_b64_tr_b16 a[226:227], v20 offset:40960\n\t"
    "v_mfma_f32_16x16x32_bf16 v[204:207], v[208:211], v[180:183], v[204:207]\n\t"
    "s_waitcnt lgkmcnt(10)\n\t"
    "v_mfma_f32_16x16x32_bf16 a[0:3], a[192:195], v[160:163], a[0:3]\n\t"
    "ds_read_b64_tr_b16 a[228:229], v20 offset:49152\n\t"
    "ds_read_b64_tr_b16 a[230:231], v20 offset:57344\n\t"
    "v_mfma_f32_16x16x32_bf16 a[0:3], a[196:199], v[176:179], a[0:3]\n\t"
    "v_mfma_f32_16x16x32_bf16 a[4:7], a[192:195], v[164:167], a[4:7]\n\t"
    "ds_read_b64_tr_b16 a[232:233], v21 offset:32768\n\t"
    "ds_read_b64_tr_b16 a[234:235], v21 offset:40960\n\t"
    "v_mfma_f32_16x16x32_bf16 a[4:7], a[196:199], v[180:183], a[4:7]\n\t"
    "v_rcp_f32 v214, v200\n\t"
    "v_rcp_f32 v215, v204\n\t"
    "v_mfma_f32_16x16x32_bf16 a[8:11], a[200:203], v[160:163], a[8:11]\n\t"
    "ds_read_b64_tr_b16 a[236:237], v21 offset:49152\n\t"
    "ds_read_b64_tr_b16 a[238:239], v21 offset:57344\n\t"
    "v_mfma_f32_16x16x32_bf16 a[8:11], a[204:207], v[176:179], a[8:11]\n\t"
    "v_accvgpr_read_b32 v220, a0\n\t"
    "v_accvgpr_read_b32 v221, a1\n\t"
    "v_accvgpr_read_b32 v222, a2\n\t"
    "v_accvgpr_read_b32 v223, a3\n\t"
    "v_accvgpr_read_b32 v224, a4\n\t"
    "v_mfma_f32_16x16x32_bf16 a[12:15], a[200:203], v[164:167], a[12:15]\n\t"
    "ds_read_b64_tr_b16 a[240:241], v22 offset:32768\n\t"
    "ds_read_b64_tr_b16 a[242:243], v22 offset:40960\n\t"
    "v_accvgpr_read_b32 v225, a5\n\t"
    "v_accvgpr_read_b32 v226, a6\n\t"
    "v_accvgpr_read_b32 v227, a7\n\t"
    "v_mul_f32 v220, v220, v214\n\t"
    "v_mul_f32 v221, v221, v214\n\t"
    "v_mfma_f32_16x16x32_bf16 a[12:15], a[204:207], v[180:183], a[12:15]\n\t"
    "v_mul_f32 v222, v222, v214\n\t"
    "v_mul_f32 v223, v223, v214\n\t"
    "v_mul_f32 v224, v224, v215\n\t"
    "v_mul_f32 v225, v225, v215\n\t"
    "v_mul_f32 v226, v226, v215\n\t"
    "s_waitcnt lgkmcnt(10)\n\t"
    "v_mfma_f32_16x16x32_bf16 a[16:19], a[208:211], v[160:163], a[16:19]\n\t"
    "ds_read_b64_tr_b16 a[244:245], v22 offset:49152\n\t"
    "ds_read_b64_tr_b16 a[246:247], v22 offset:57344\n\t"
    "v_mul_f32 v227, v227, v215\n\t"
    "v_cvt_pk_bf16_f32 v64, v220, v221\n\t"
    "v_cvt_pk_bf16_f32 v65, v222, v223\n\t"
    "v_cvt_pk_bf16_f32 v68, v224, v225\n\t"
    "v_cvt_pk_bf16_f32 v69, v226, v227\n\t"
    "v_mfma_f32_16x16x32_bf16 a[16:19], a[212:215], v[176:179], a[16:19]\n\t"
    "v_accvgpr_read_b32 v228, a8\n\t"
    "v_accvgpr_read_b32 v229, a9\n\t"
    "v_accvgpr_read_b32 v230, a10\n\t"
    "v_accvgpr_read_b32 v231, a11\n\t"
    "v_accvgpr_read_b32 v232, a12\n\t"
    "v_accvgpr_read_b32 v233, a13\n\t"
    "v_mfma_f32_16x16x32_bf16 a[20:23], a[208:211], v[164:167], a[20:23]\n\t"
    "ds_read_b64_tr_b16 a[248:249], v23 offset:32768\n\t"
    "ds_read_b64_tr_b16 a[250:251], v23 offset:40960\n\t"
    "v_accvgpr_read_b32 v234, a14\n\t"
    "v_accvgpr_read_b32 v235, a15\n\t"
    "v_mul_f32 v228, v228, v214\n\t"
    "v_mul_f32 v229, v229, v214\n\t"
    "v_mul_f32 v230, v230, v214\n\t"
    "v_mfma_f32_16x16x32_bf16 a[20:23], a[212:215], v[180:183], a[20:23]\n\t"
    "v_mul_f32 v231, v231, v214\n\t"
    "v_mul_f32 v232, v232, v215\n\t"
    "v_mul_f32 v233, v233, v215\n\t"
    "v_mul_f32 v234, v234, v215\n\t"
    "v_mul_f32 v235, v235, v215\n\t"
    "v_cvt_pk_bf16_f32 v66, v228, v229\n\t"
    "v_mfma_f32_16x16x32_bf16 a[24:27], a[216:219], v[160:163], a[24:27]\n\t"
    "ds_read_b64_tr_b16 a[252:253], v23 offset:49152\n\t"
    "ds_read_b64_tr_b16 a[254:255], v23 offset:57344\n\t"
    "v_cvt_pk_bf16_f32 v67, v230, v231\n\t"
    "v_cvt_pk_bf16_f32 v70, v232, v233\n\t"
    "v_cvt_pk_bf16_f32 v71, v234, v235\n\t"
    "ds_write_b128 v56, v[64:67]\n\t"
    "ds_write_b128 v56, v[68:71] offset:1024\n\t"
    "v_mfma_f32_16x16x32_bf16 a[24:27], a[220:223], v[176:179], a[24:27]\n\t"
    "v_accvgpr_read_b32 v236, a16\n\t"
    "v_accvgpr_read_b32 v237, a17\n\t"
    "v_accvgpr_read_b32 v238, a18\n\t"
    "v_accvgpr_read_b32 v239, a19\n\t"
    "v_accvgpr_read_b32 v240, a20\n\t"
    "v_mfma_f32_16x16x32_bf16 a[28:31], a[216:219], v[164:167], a[28:31]\n\t"
    "ds_read_b64_tr_b16 a[192:193], v16 offset:33024\n\t"
    "ds_read_b64_tr_b16 a[194:195], v16 offset:41216\n\t"
    "v_accvgpr_read_b32 v241, a21\n\t"
    "v_accvgpr_read_b32 v242, a22\n\t"
    "v_accvgpr_read_b32 v243, a23\n\t"
    "v_mul_f32 v236, v236, v214\n\t"
    "v_mul_f32 v237, v237, v214\n\t"
    "v_mfma_f32_16x16x32_bf16 a[28:31], a[220:223], v[180:183], a[28:31]\n\t"
    "v_mul_f32 v238, v238, v214\n\t"
    "v_mul_f32 v239, v239, v214\n\t"
    "v_mul_f32 v240, v240, v215\n\t"
    "v_mul_f32 v241, v241, v215\n\t"
    "v_mul_f32 v242, v242, v215\n\t"
    "s_waitcnt lgkmcnt(12)\n\t"
    "v_mfma_f32_16x16x32_bf16 a[32:35], a[224:227], v[160:163], a[32:35]\n\t"
    "ds_read_b64_tr_b16 a[196:197], v16 offset:49408\n\t"
    "ds_read_b64_tr_b16 a[198:199], v16 offset:57600\n\t"
    "v_mul_f32 v243, v243, v215\n\t"
    "v_cvt_pk_bf16_f32 v72, v236, v237\n\t"
    "v_cvt_pk_bf16_f32 v73, v238, v239\n\t"
    "v_cvt_pk_bf16_f32 v76, v240, v241\n\t"
    "v_cvt_pk_bf16_f32 v77, v242, v243\n\t"
    "v_mfma_f32_16x16x32_bf16 a[32:35], a[228:231], v[176:179], a[32:35]\n\t"
    "v_accvgpr_read_b32 v244, a24\n\t"
    "v_accvgpr_read_b32 v245, a25\n\t"
    "v_accvgpr_read_b32 v246, a26\n\t"
    "v_accvgpr_read_b32 v247, a27\n\t"
    "v_accvgpr_read_b32 v248, a28\n\t"
    "v_accvgpr_read_b32 v249, a29\n\t"
    "v_mfma_f32_16x16x32_bf16 a[36:39], a[224:227], v[164:167], a[36:39]\n\t"
    "ds_read_b64_tr_b16 a[200:201], v17 offset:33024\n\t"
    "ds_read_b64_tr_b16 a[202:203], v17 offset:41216\n\t"
    "v_accvgpr_read_b32 v250, a30\n\t"
    "v_accvgpr_read_b32 v251, a31\n\t"
    "v_mul_f32 v244, v244, v214\n\t"
    "v_mul_f32 v245, v245, v214\n\t"
    "v_mul_f32 v246, v246, v214\n\t"
    "v_mfma_f32_16x16x32_bf16 a[36:39], a[228:231], v[180:183], a[36:39]\n\t"
    "v_mul_f32 v247, v247, v214\n\t"
    "v_mul_f32 v248, v248, v215\n\t"
    "v_mul_f32 v249, v249, v215\n\t"
    "v_mul_f32 v250, v250, v215\n\t"
    "v_mul_f32 v251, v251, v215\n\t"
    "v_cvt_pk_bf16_f32 v74, v244, v245\n\t"
    "v_mfma_f32_16x16x32_bf16 a[40:43], a[232:235], v[160:163], a[40:43]\n\t"
    "ds_read_b64_tr_b16 a[204:205], v17 offset:49408\n\t"
    "ds_read_b64_tr_b16 a[206:207], v17 offset:57600\n\t"
    "v_cvt_pk_bf16_f32 v75, v246, v247\n\t"
    "v_cvt_pk_bf16_f32 v78, v248, v249\n\t"
    "v_cvt_pk_bf16_f32 v79, v250, v251\n\t"
    "ds_write_b128 v56, v[72:75] offset:2048\n\t"
    "ds_write_b128 v56, v[76:79] offset:3072\n\t"
    "v_mfma_f32_16x16x32_bf16 a[40:43], a[236:239], v[176:179], a[40:43]\n\t"
    "v_accvgpr_read_b32 v220, a32\n\t"
    "v_accvgpr_read_b32 v221, a33\n\t"
    "v_accvgpr_read_b32 v222, a34\n\t"
    "v_accvgpr_read_b32 v223, a35\n\t"
    "v_accvgpr_read_b32 v224, a36\n\t"
    "v_mfma_f32_16x16x32_bf16 a[44:47], a[232:235], v[164:167], a[44:47]\n\t"
    "ds_read_b64_tr_b16 a[208:209], v18 offset:33024\n\t"
    "ds_read_b64_tr_b16 a[210:211], v18 offset:41216\n\t"
    "v_accvgpr_read_b32 v225, a37\n\t"
    "v_accvgpr_read_b32 v226, a38\n\t"
    "v_accvgpr_read_b32 v227, a39\n\t"
    "v_mul_f32 v220, v220, v214\n\t"
    "v_mul_f32 v221, v221, v214\n\t"
    "v_mfma_f32_16x16x32_bf16 a[44:47], a[236:239], v[180:183], a[44:47]\n\t"
    "v_mul_f32 v222, v222, v214\n\t"
    "v_mul_f32 v223, v223, v214\n\t"
    "v_mul_f32 v224, v224, v215\n\t"
    "v_mul_f32 v225, v225, v215\n\t"
    "v_mul_f32 v226, v226, v215\n\t"
    "s_waitcnt lgkmcnt(14)\n\t"
    "v_mfma_f32_16x16x32_bf16 a[48:51], a[240:243], v[160:163], a[48:51]\n\t"
    "ds_read_b64_tr_b16 a[212:213], v18 offset:49408\n\t"
    "ds_read_b64_tr_b16 a[214:215], v18 offset:57600\n\t"
    "v_mul_f32 v227, v227, v215\n\t"
    "v_cvt_pk_bf16_f32 v80, v220, v221\n\t"
    "v_cvt_pk_bf16_f32 v81, v222, v223\n\t"
    "v_cvt_pk_bf16_f32 v84, v224, v225\n\t"
    "v_cvt_pk_bf16_f32 v85, v226, v227\n\t"
    "v_mfma_f32_16x16x32_bf16 a[48:51], a[244:247], v[176:179], a[48:51]\n\t"
    "v_accvgpr_read_b32 v228, a40\n\t"
    "v_accvgpr_read_b32 v229, a41\n\t"
    "v_accvgpr_read_b32 v230, a42\n\t"
    "v_accvgpr_read_b32 v231, a43\n\t"
    "v_accvgpr_read_b32 v232, a44\n\t"
    "v_accvgpr_read_b32 v233, a45\n\t"
    "v_mfma_f32_16x16x32_bf16 a[52:55], a[240:243], v[164:167], a[52:55]\n\t"
    "ds_read_b64_tr_b16 a[216:217], v19 offset:33024\n\t"
    "ds_read_b64_tr_b16 a[218:219], v19 offset:41216\n\t"
    "v_accvgpr_read_b32 v234, a46\n\t"
    "v_accvgpr_read_b32 v235, a47\n\t"
    "v_mul_f32 v228, v228, v214\n\t"
    "v_mul_f32 v229, v229, v214\n\t"
    "v_mul_f32 v230, v230, v214\n\t"
    "v_mfma_f32_16x16x32_bf16 a[52:55], a[244:247], v[180:183], a[52:55]\n\t"
    "v_mul_f32 v231, v231, v214\n\t"
    "v_mul_f32 v232, v232, v215\n\t"
    "v_mul_f32 v233, v233, v215\n\t"
    "v_mul_f32 v234, v234, v215\n\t"
    "v_mul_f32 v235, v235, v215\n\t"
    "v_cvt_pk_bf16_f32 v82, v228, v229\n\t"
    "v_mfma_f32_16x16x32_bf16 a[56:59], a[248:251], v[160:163], a[56:59]\n\t"
    "ds_read_b64_tr_b16 a[220:221], v19 offset:49408\n\t"
    "ds_read_b64_tr_b16 a[222:223], v19 offset:57600\n\t"
    "v_cvt_pk_bf16_f32 v83, v230, v231\n\t"
    "v_cvt_pk_bf16_f32 v86, v232, v233\n\t"
    "v_cvt_pk_bf16_f32 v87, v234, v235\n\t"
    "ds_write_b128 v56, v[80:83] offset:4096\n\t"
    "ds_write_b128 v56, v[84:87] offset:5120\n\t"
    "v_mfma_f32_16x16x32_bf16 a[56:59], a[252:255], v[176:179], a[56:59]\n\t"
    "v_accvgpr_read_b32 v236, a48\n\t"
    "v_accvgpr_read_b32 v237, a49\n\t"
    "v_accvgpr_read_b32 v238, a50\n\t"
    "v_accvgpr_read_b32 v239, a51\n\t"
    "v_accvgpr_read_b32 v240, a52\n\t"
    "v_mfma_f32_16x16x32_bf16 a[60:63], a[248:251], v[164:167], a[60:63]\n\t"
    "ds_read_b64_tr_b16 a[224:225], v20 offset:33024\n\t"
    "ds_read_b64_tr_b16 a[226:227], v20 offset:41216\n\t"
    "v_accvgpr_read_b32 v241, a53\n\t"
    "v_accvgpr_read_b32 v242, a54\n\t"
    "v_accvgpr_read_b32 v243, a55\n\t"
    "v_mul_f32 v236, v236, v214\n\t"
    "v_mul_f32 v237, v237, v214\n\t"
    "v_mfma_f32_16x16x32_bf16 a[60:63], a[252:255], v[180:183], a[60:63]\n\t"
    "v_mul_f32 v238, v238, v214\n\t"
    "v_mul_f32 v239, v239, v214\n\t"
    "v_mul_f32 v240, v240, v215\n\t"
    "v_mul_f32 v241, v241, v215\n\t"
    "v_mul_f32 v242, v242, v215\n\t"
    "s_waitcnt lgkmcnt(14)\n\t"
    "v_mfma_f32_16x16x32_bf16 a[64:67], a[192:195], v[160:163], a[64:67]\n\t"
    "ds_read_b64_tr_b16 a[228:229], v20 offset:49408\n\t"
    "ds_read_b64_tr_b16 a[230:231], v20 offset:57600\n\t"
    "v_mul_f32 v243, v243, v215\n\t"
    "v_cvt_pk_bf16_f32 v88, v236, v237\n\t"
    "v_cvt_pk_bf16_f32 v89, v238, v239\n\t"
    "v_cvt_pk_bf16_f32 v92, v240, v241\n\t"
    "v_cvt_pk_bf16_f32 v93, v242, v243\n\t"
    "v_mfma_f32_16x16x32_bf16 a[64:67], a[196:199], v[176:179], a[64:67]\n\t"
    "v_accvgpr_read_b32 v244, a56\n\t"
    "v_accvgpr_read_b32 v245, a57\n\t"
    "v_accvgpr_read_b32 v246, a58\n\t"
    "v_accvgpr_read_b32 v247, a59\n\t"
    "v_accvgpr_read_b32 v248, a60\n\t"
    "v_accvgpr_read_b32 v249, a61\n\t"
    "v_mfma_f32_16x16x32_bf16 a[68:71], a[192:195], v[164:167], a[68:71]\n\t"
    "ds_read_b64_tr_b16 a[232:233], v21 offset:33024\n\t"
    "ds_read_b64_tr_b16 a[234:235], v21 offset:41216\n\t"
    "v_accvgpr_read_b32 v250, a62\n\t"
    "v_accvgpr_read_b32 v251, a63\n\t"
    "v_mul_f32 v244, v244, v214\n\t"
    "v_mul_f32 v245, v245, v214\n\t"
    "v_mul_f32 v246, v246, v214\n\t"
    "v_mfma_f32_16x16x32_bf16 a[68:71], a[196:199], v[180:183], a[68:71]\n\t"
    "v_mul_f32 v247, v247, v214\n\t"
    "v_mul_f32 v248, v248, v215\n\t"
    "v_mul_f32 v249, v249, v215\n\t"
    "v_mul_f32 v250, v250, v215\n\t"
    "v_mul_f32 v251, v251, v215\n\t"
    "v_cvt_pk_bf16_f32 v90, v244, v245\n\t"
    "v_mfma_f32_16x16x32_bf16 a[72:75], a[200:203], v[160:163], a[72:75]\n\t"
    "ds_read_b64_tr_b16 a[236:237], v21 offset:49408\n\t"
    "ds_read_b64_tr_b16 a[238:239], v21 offset:57600\n\t"
    "v_cvt_pk_bf16_f32 v91, v246, v247\n\t"
    "v_cvt_pk_bf16_f32 v94, v248, v249\n\t"
    "v_cvt_pk_bf16_f32 v95, v250, v251\n\t"
    "ds_write_b128 v56, v[88:91] offset:6144\n\t"
    "ds_write_b128 v56, v[92:95] offset:7168\n\t"
    "v_mfma_f32_16x16x32_bf16 a[72:75], a[204:207], v[176:179], a[72:75]\n\t"
    "v_accvgpr_read_b32 v220, a64\n\t"
    "v_accvgpr_read_b32 v221, a65\n\t"
    "v_accvgpr_read_b32 v222, a66\n\t"
    "v_accvgpr_read_b32 v223, a67\n\t"
    "v_accvgpr_read_b32 v224, a68\n\t"
    "v_mfma_f32_16x16x32_bf16 a[76:79], a[200:203], v[164:167], a[76:79]\n\t"
    "ds_read_b64_tr_b16 a[240:241], v22 offset:33024\n\t"
    "ds_read_b64_tr_b16 a[242:243], v22 offset:41216\n\t"
    "v_accvgpr_read_b32 v225, a69\n\t"
    "v_accvgpr_read_b32 v226, a70\n\t"
    "v_accvgpr_read_b32 v227, a71\n\t"
    "v_mul_f32 v220, v220, v214\n\t"
    "v_mul_f32 v221, v221, v214\n\t"
    "v_mfma_f32_16x16x32_bf16 a[76:79], a[204:207], v[180:183], a[76:79]\n\t"
    "v_mul_f32 v222, v222, v214\n\t"
    "v_mul_f32 v223, v223, v214\n\t"
    "v_mul_f32 v224, v224, v215\n\t"
    "v_mul_f32 v225, v225, v215\n\t"
    "v_mul_f32 v226, v226, v215\n\t"
    "s_waitcnt lgkmcnt(14)\n\t"
    "v_mfma_f32_16x16x32_bf16 a[80:83], a[208:211], v[160:163], a[80:83]\n\t"
    "ds_read_b64_tr_b16 a[244:245], v22 offset:49408\n\t"
    "ds_read_b64_tr_b16 a[246:247], v22 offset:57600\n\t"
    "v_mul_f32 v227, v227, v215\n\t"
    "v_cvt_pk_bf16_f32 v96, v220, v221\n\t"
    "v_cvt_pk_bf16_f32 v97, v222, v223\n\t"
    "v_cvt_pk_bf16_f32 v100, v224, v225\n\t"
    "v_cvt_pk_bf16_f32 v101, v226, v227\n\t"
    "v_mfma_f32_16x16x32_bf16 a[80:83], a[212:215], v[176:179], a[80:83]\n\t"
    "v_accvgpr_read_b32 v228, a72\n\t"
    "v_accvgpr_read_b32 v229, a73\n\t"
    "v_accvgpr_read_b32 v230, a74\n\t"
    "v_accvgpr_read_b32 v231, a75\n\t"
    "v_accvgpr_read_b32 v232, a76\n\t"
    "v_accvgpr_read_b32 v233, a77\n\t"
    "v_mfma_f32_16x16x32_bf16 a[84:87], a[208:211], v[164:167], a[84:87]\n\t"
    "ds_read_b64_tr_b16 a[248:249], v23 offset:33024\n\t"
    "ds_read_b64_tr_b16 a[250:251], v23 offset:41216\n\t"
    "v_accvgpr_read_b32 v234, a78\n\t"
    "v_accvgpr_read_b32 v235, a79\n\t"
    "v_mul_f32 v228, v228, v214\n\t"
    "v_mul_f32 v229, v229, v214\n\t"
    "v_mul_f32 v230, v230, v214\n\t"
    "v_mfma_f32_16x16x32_bf16 a[84:87], a[212:215], v[180:183], a[84:87]\n\t"
    "v_mul_f32 v231, v231, v214\n\t"
    "v_mul_f32 v232, v232, v215\n\t"
    "v_mul_f32 v233, v233, v215\n\t"
    "v_mul_f32 v234, v234, v215\n\t"
    "v_mul_f32 v235, v235, v215\n\t"
    "v_cvt_pk_bf16_f32 v98, v228, v229\n\t"
    "v_mfma_f32_16x16x32_bf16 a[88:91], a[216:219], v[160:163], a[88:91]\n\t"
    "ds_read_b64_tr_b16 a[252:253], v23 offset:49408\n\t"
    "ds_read_b64_tr_b16 a[254:255], v23 offset:57600\n\t"
    "v_cvt_pk_bf16_f32 v99, v230, v231\n\t"
    "v_cvt_pk_bf16_f32 v102, v232, v233\n\t"
    "v_cvt_pk_bf16_f32 v103, v234, v235\n\t"
    "ds_write_b128 v56, v[96:99] offset:8192\n\t"
    "ds_write_b128 v56, v[100:103] offset:9216\n\t"
    "v_mfma_f32_16x16x32_bf16 a[88:91], a[220:223], v[176:179], a[88:91]\n\t"
    "v_accvgpr_read_b32 v236, a80\n\t"
    "v_accvgpr_read_b32 v237, a81\n\t"
    "v_accvgpr_read_b32 v238, a82\n\t"
    "v_accvgpr_read_b32 v239, a83\n\t"
    "v_accvgpr_read_b32 v240, a84\n\t"
    "v_mfma_f32_16x16x32_bf16 a[92:95], a[216:219], v[164:167], a[92:95]\n\t"
    "v_accvgpr_read_b32 v241, a85\n\t"
    "v_accvgpr_read_b32 v242, a86\n\t"
    "v_accvgpr_read_b32 v243, a87\n\t"
    "v_mul_f32 v236, v236, v214\n\t"
    "v_mul_f32 v237, v237, v214\n\t"
    "v_mfma_f32_16x16x32_bf16 a[92:95], a[220:223], v[180:183], a[92:95]\n\t"
    "v_mul_f32 v238, v238, v214\n\t"
    "v_mul_f32 v239, v239, v214\n\t"
    "v_mul_f32 v240, v240, v215\n\t"
    "v_mul_f32 v241, v241, v215\n\t"
    "v_mul_f32 v242, v242, v215\n\t"
    "s_waitcnt lgkmcnt(12)\n\t"
    "v_mfma_f32_16x16x32_bf16 a[96:99], a[224:227], v[160:163], a[96:99]\n\t"
    "v_mul_f32 v243, v243, v215\n\t"
    "v_cvt_pk_bf16_f32 v104, v236, v237\n\t"
    "v_cvt_pk_bf16_f32 v105, v238, v239\n\t"
    "v_cvt_pk_bf16_f32 v108, v240, v241\n\t"
    "v_cvt_pk_bf16_f32 v109, v242, v243\n\t"
    "v_mfma_f32_16x16x32_bf16 a[96:99], a[228:231], v[176:179], a[96:99]\n\t"
    "v_accvgpr_read_b32 v244, a88\n\t"
    "v_accvgpr_read_b32 v245, a89\n\t"
    "v_accvgpr_read_b32 v246, a90\n\t"
    "v_accvgpr_read_b32 v247, a91\n\t"
    "v_accvgpr_read_b32 v248, a92\n\t"
    "v_accvgpr_read_b32 v249, a93\n\t"
    "v_mfma_f32_16x16x32_bf16 a[100:103], a[224:227], v[164:167], a[100:103]\n\t"
    "v_accvgpr_read_b32 v250, a94\n\t"
    "v_accvgpr_read_b32 v251, a95\n\t"
    "v_mul_f32 v244, v244, v214\n\t"
    "v_mul_f32 v245, v245, v214\n\t"
    "v_mul_f32 v246, v246, v214\n\t"
    "v_mfma_f32_16x16x32_bf16 a[100:103], a[228:231], v[180:183], a[100:103]\n\t"
    "v_mul_f32 v247, v247, v214\n\t"
    "v_mul_f32 v248, v248, v215\n\t"
    "v_mul_f32 v249, v249, v215\n\t"
    "v_mul_f32 v250, v250, v215\n\t"
    "v_mul_f32 v251, v251, v215\n\t"
    "v_cvt_pk_bf16_f32 v106, v244, v245\n\t"
    "v_mfma_f32_16x16x32_bf16 a[104:107], a[232:235], v[160:163], a[104:107]\n\t"
    "v_cvt_pk_bf16_f32 v107, v246, v247\n\t"
    "v_cvt_pk_bf16_f32 v110, v248, v249\n\t"
    "v_cvt_pk_bf16_f32 v111, v250, v251\n\t"
    "ds_write_b128 v56, v[104:107] offset:10240\n\t"
    "ds_write_b128 v56, v[108:111] offset:11264\n\t"
    "v_mfma_f32_16x16x32_bf16 a[104:107], a[236:239], v[176:179], a[104:107]\n\t"
    "v_accvgpr_read_b32 v220, a96\n\t"
    "v_accvgpr_read_b32 v221, a97\n\t"
    "v_accvgpr_read_b32 v222, a98\n\t"
    "v_accvgpr_read_b32 v223, a99\n\t"
    "v_accvgpr_read_b32 v224, a100\n\t"
    "v_mfma_f32_16x16x32_bf16 a[108:111], a[232:235], v[164:167], a[108:111]\n\t"
    "v_accvgpr_read_b32 v225, a101\n\t"
    "v_accvgpr_read_b32 v226, a102\n\t"
    "v_accvgpr_read_b32 v227, a103\n\t"
    "v_mul_f32 v220, v220, v214\n\t"
    "v_mul_f32 v221, v221, v214\n\t"
    "v_mfma_f32_16x16x32_bf16 a[108:111], a[236:239], v[180:183], a[108:111]\n\t"
    "v_mul_f32 v222, v222, v214\n\t"
    "v_mul_f32 v223, v223, v214\n\t"
    "v_mul_f32 v224, v224, v215\n\t"
    "v_mul_f32 v225, v225, v215\n\t"
    "v_mul_f32 v226, v226, v215\n\t"
    "s_waitcnt lgkmcnt(4)\n\t"
    "v_mfma_f32_16x16x32_bf16 a[112:115], a[240:243], v[160:163], a[112:115]\n\t"
    "v_mul_f32 v227, v227, v215\n\t"
    "v_cvt_pk_bf16_f32 v112, v220, v221\n\t"
    "v_cvt_pk_bf16_f32 v113, v222, v223\n\t"
    "v_cvt_pk_bf16_f32 v116, v224, v225\n\t"
    "v_cvt_pk_bf16_f32 v117, v226, v227\n\t"
    "v_mfma_f32_16x16x32_bf16 a[112:115], a[244:247], v[176:179], a[112:115]\n\t"
    "v_accvgpr_read_b32 v228, a104\n\t"
    "v_accvgpr_read_b32 v229, a105\n\t"
    "v_accvgpr_read_b32 v230, a106\n\t"
    "v_accvgpr_read_b32 v231, a107\n\t"
    "v_accvgpr_read_b32 v232, a108\n\t"
    "v_accvgpr_read_b32 v233, a109\n\t"
    "v_mfma_f32_16x16x32_bf16 a[116:119], a[240:243], v[164:167], a[116:119]\n\t"
    "v_accvgpr_read_b32 v234, a110\n\t"
    "v_accvgpr_read_b32 v235, a111\n\t"
    "v_mul_f32 v228, v228, v214\n\t"
    "v_mul_f32 v229, v229, v214\n\t"
    "v_mul_f32 v230, v230, v214\n\t"
    "v_mfma_f32_16x16x32_bf16 a[116:119], a[244:247], v[180:183], a[116:119]\n\t"
    "v_mul_f32 v231, v231, v214\n\t"
    "v_mul_f32 v232, v232, v215\n\t"
    "v_mul_f32 v233, v233, v215\n\t"
    "v_mul_f32 v234, v234, v215\n\t"
    "v_mul_f32 v235, v235, v215\n\t"
    "v_cvt_pk_bf16_f32 v114, v228, v229\n\t"
    "v_mfma_f32_16x16x32_bf16 a[120:123], a[248:251], v[160:163], a[120:123]\n\t"
    "v_cvt_pk_bf16_f32 v115, v230, v231\n\t"
    "v_cvt_pk_bf16_f32 v118, v232, v233\n\t"
    "v_cvt_pk_bf16_f32 v119, v234, v235\n\t"
    "ds_write_b128 v56, v[112:115] offset:12288\n\t"
    "ds_write_b128 v56, v[116:119] offset:13312\n\t"
    "v_mfma_f32_16x16x32_bf16 a[120:123], a[252:255], v[176:179], a[120:123]\n\t"
    "v_accvgpr_read_b32 v236, a112\n\t"
    "v_accvgpr_read_b32 v237, a113\n\t"
    "v_accvgpr_read_b32 v238, a114\n\t"
    "v_accvgpr_read_b32 v239, a115\n\t"
    "v_accvgpr_read_b32 v240, a116\n\t"
    "v_mfma_f32_16x16x32_bf16 a[124:127], a[248:251], v[164:167], a[124:127]\n\t"
    "v_accvgpr_read_b32 v241, a117\n\t"
    "v_accvgpr_read_b32 v242, a118\n\t"
    "v_accvgpr_read_b32 v243, a119\n\t"
    "v_mul_f32 v236, v236, v214\n\t"
    "v_mul_f32 v237, v237, v214\n\t"
    "v_mfma_f32_16x16x32_bf16 a[124:127], a[252:255], v[180:183], a[124:127]\n\t"
    "v_mul_f32 v238, v238, v214\n\t"
    "v_mul_f32 v239, v239, v214\n\t"
    "v_mul_f32 v240, v240, v215\n\t"
    "v_mul_f32 v241, v241, v215\n\t"
    "v_mul_f32 v242, v242, v215\n\t"
    "v_mul_f32 v243, v243, v215\n\t"
    "v_cvt_pk_bf16_f32 v120, v236, v237\n\t"
    "v_cvt_pk_bf16_f32 v121, v238, v239\n\t"
    "v_cvt_pk_bf16_f32 v124, v240, v241\n\t"
    "v_cvt_pk_bf16_f32 v125, v242, v243\n\t"
    "s_nop 3\n\t"
    "v_accvgpr_read_b32 v244, a120\n\t"
    "v_accvgpr_read_b32 v245, a121\n\t"
    "v_accvgpr_read_b32 v246, a122\n\t"
    "v_accvgpr_read_b32 v247, a123\n\t"
    "v_accvgpr_read_b32 v248, a124\n\t"
    "v_accvgpr_read_b32 v249, a125\n\t"
    "v_accvgpr_read_b32 v250, a126\n\t"
    "v_accvgpr_read_b32 v251, a127\n\t"
    "v_mul_f32 v244, v244, v214\n\t"
    "v_mul_f32 v245, v245, v214\n\t"
    "v_mul_f32 v246, v246, v214\n\t"
    "v_mul_f32 v247, v247, v214\n\t"
    "v_mul_f32 v248, v248, v215\n\t"
    "v_mul_f32 v249, v249, v215\n\t"
    "v_mul_f32 v250, v250, v215\n\t"
    "v_mul_f32 v251, v251, v215\n\t"
    "v_cvt_pk_bf16_f32 v122, v244, v245\n\t"
    "v_cvt_pk_bf16_f32 v123, v246, v247\n\t"
    "v_cvt_pk_bf16_f32 v126, v248, v249\n\t"
    "v_cvt_pk_bf16_f32 v127, v250, v251\n\t"
    "ds_write_b128 v56, v[120:123] offset:14336\n\t"
    "ds_write_b128 v56, v[124:127] offset:15360\n\t"
    "s_mul_i32 s61, s60, 7168\n\t"
    "s_add_u32 s61, s61, %[ldsb]\n\t"
    "s_mul_i32 s62, s60, 57344\n\t"
    "v_add_u32 v240, 0x10000, v44\n\t"
    "v_add_u32 v241, 0x10000, v45\n\t"
    "v_add_u32 v242, 0x10000, v46\n\t"
    "v_add_u32 v243, 0x10000, v47\n\t"
    "s_waitcnt lgkmcnt(0)\n\t"
    "s_barrier\n\t"
    "ds_read_b64_tr_b16 a[192:193], v44\n\t"
    "ds_read_b64_tr_b16 a[194:195], v44 offset:2048\n\t"
    "ds_read_b64_tr_b16 a[196:197], v45\n\t"
    "ds_read_b64_tr_b16 a[198:199], v45 offset:2048\n\t"
    "ds_read_b64_tr_b16 a[200:201], v44 offset:4096\n\t"
    "ds_read_b64_tr_b16 a[202:203], v44 offset:6144\n\t"
    "ds_read_b64_tr_b16 a[204:205], v45 offset:4096\n\t"
    "ds_read_b64_tr_b16 a[206:207], v45 offset:6144\n\t"
    "ds_read_b64_tr_b16 a[208:209], v44 offset:8192\n\t"
    "ds_read_b64_tr_b16 a[210:211], v44 offset:10240\n\t"
    "ds_read_b64_tr_b16 a[212:213], v45 offset:8192\n\t"
    "ds_read_b64_tr_b16 a[214:215], v45 offset:10240\n\t"
    "ds_read_b128 v[128:131], v57\n\t"
    "ds_read_b128 v[132:135], v57 offset:1024\n\t"
    "ds_read_b128 v[136:139], v57 offset:2048\n\t"
    "s_waitcnt lgkmcnt(11)\n\t"
    "v_mfma_f32_16x16x32_bf16 v[192:195], a[192:195], v[64:67], 0\n\t"
    "ds_read_b64_tr_b16 a[216:217], v44 offset:12288\n\t"
    "ds_read_b64_tr_b16 a[218:219], v44 offset:14336\n\t"
    "s_add_u32 m0, s63, 0x18000\n\t"
    "s_add_u32 s68, s72, 0x80\n\t"
    "v_mfma_f32_16x16x32_bf16 v[196:199], a[192:195], v[68:71], 0\n\t"
    "ds_read_b128 v[140:143], v57 offset:3072\n\t"
    "ds_read_b128 v[144:147], v57 offset:4096\n\t"
    "ds_read_b128 v[148:151], v57 offset:5120\n\t"
    "v_mfma_f32_16x16x32_bf16 v[200:203], a[196:199], v[64:67], 0\n\t"
    "ds_read_b64_tr_b16 a[220:221], v45 offset:12288\n\t"
    "ds_read_b64_tr_b16 a[222:223], v45 offset:14336\n\t"
    "buffer_load_dwordx4 v48, %[srdwo], s68 offen lds\n\t"
    "v_mfma_f32_16x16x32_bf16 v[204:207], a[196:199], v[68:71], 0\n\t"
    "ds_read_b128 v[152:155], v57 offset:6144\n\t"
    "ds_read_b128 v[156:159], v57 offset:7168\n\t"
    "ds_read_b128 v[160:163], v57 offset:8192\n\t"
    "s_waitcnt lgkmcnt(15)\n\t"
    "v_mfma_f32_16x16x32_bf16 v[192:195], a[200:203], v[72:75], v[192:195]\n\t"
    "ds_read_b64_tr_b16 a[224:225], v46\n\t"
    "ds_read_b64_tr_b16 a[226:227], v46 offset:2048\n\t"
    "buffer_load_dwordx4 v49, %[srdwo], s68 offen offset:1024 lds\n\t"
    "v_mfma_f32_16x16x32_bf16 v[196:199], a[200:203], v[76:79], v[196:199]\n\t"
    "ds_read_b128 v[164:167], v57 offset:9216\n\t"
    "ds_read_b128 v[168:171], v57 offset:10240\n\t"
    "ds_read_b128 v[172:175], v57 offset:11264\n\t"
    "v_mfma_f32_16x16x32_bf16 v[200:203], a[204:207], v[72:75], v[200:203]\n\t"
    "ds_read_b64_tr_b16 a[228:229], v47\n\t"
    "ds_read_b64_tr_b16 a[230:231], v47 offset:2048\n\t"
    "buffer_load_dwordx4 v50, %[srdwo], s68 offen offset:2048 lds\n\t"
    "v_mfma_f32_16x16x32_bf16 v[204:207], a[204:207], v[76:79], v[204:207]\n\t"
    "ds_read_b128 v[176:179], v57 offset:12288\n\t"
    "ds_read_b128 v[180:183], v57 offset:13312\n\t"
    "ds_read_b128 v[184:187], v57 offset:14336\n\t"
    "s_waitcnt lgkmcnt(15)\n\t"
    "v_mfma_f32_16x16x32_bf16 v[192:195], a[208:211], v[80:83], v[192:195]\n\t"
    "ds_read_b64_tr_b16 a[232:233], v46 offset:4096\n\t"
    "ds_read_b64_tr_b16 a[234:235], v46 offset:6144\n\t"
    "buffer_load_dwordx4 v51, %[srdwo], s68 offen offset:3072 lds\n\t"
    "v_mfma_f32_16x16x32_bf16 v[196:199], a[208:211], v[84:87], v[196:199]\n\t"
    "ds_read_b128 v[188:191], v57 offset:15360\n\t"
    "s_add_u32 m0, s63, 0x19000\n\t"
    "s_add_u32 s68, s72, 0x7080\n\t"
    "v_mfma_f32_16x16x32_bf16 v[200:203], a[212:215], v[80:83], v[200:203]\n\t"
    "ds_read_b64_tr_b16 a[236:237], v47 offset:4096\n\t"
    "ds_read_b64_tr_b16 a[238:239], v47 offset:6144\n\t"
    "buffer_load_dwordx4 v48, %[srdwo], s68 offen lds\n\t"
    "v_mfma_f32_16x16x32_bf16 v[204:207], a[212:215], v[84:87], v[204:207]\n\t"
    "s_waitcnt lgkmcnt(15)\n\t"
    "v_mfma_f32_16x16x32_bf16 v[192:195], a[216:219], v[88:91], v[192:195]\n\t"
    "ds_read_b64_tr_b16 a[240:241], v46 offset:8192\n\t"
    "ds_read_b64_tr_b16 a[242:243], v46 offset:10240\n\t"
    "buffer_load_dwordx4 v49, %[srdwo], s68 offen offset:1024 lds\n\t"
    "v_mfma_f32_16x16x32_bf16 v[196:199], a[216:219], v[92:95], v[196:199]\n\t"
    "v_mfma_f32_16x16x32_bf16 v[200:203], a[220:223], v[88:91], v[200:203]\n\t"
    "ds_read_b64_tr_b16 a[244:245], v47 offset:8192\n\t"
    "ds_read_b64_tr_b16 a[246:247], v47 offset:10240\n\t"
    "buffer_load_dwordx4 v50, %[srdwo], s68 offen offset:2048 lds\n\t"
    "v_mfma_f32_16x16x32_bf16 v[204:207], a[220:223], v[92:95], v[204:207]\n\t"
    "s_waitcnt lgkmcnt(12)\n\t"
    "v_mfma_f32_16x16x32_bf16 v[192:195], a[224:227], v[128:131], v[192:195]\n\t"
    "ds_read_b64_tr_b16 a[248:249], v46 offset:12288\n\t"
    "ds_read_b64_tr_b16 a[250:251], v46 offset:14336\n\t"
    "buffer_load_dwordx4 v51, %[srdwo], s68 offen offset:3072 lds\n\t"
    "v_mfma_f32_16x16x32_bf16 v[196:199], a[224:227], v[132:135], v[196:199]\n\t"
    "s_waitcnt lgkmcnt(8)\n\t"
    "s_waitcnt vmcnt(8)\n\t"
    "s_barrier\n\t"
    "v_mfma_f32_16x16x32_bf16 v[200:203], a[228:231], v[128:131], v[200:203]\n\t"
    "ds_read_b64_tr_b16 a[252:253], v47 offset:12288\n\t"
    "ds_read_b64_tr_b16 a[254:255], v47 offset:14336\n\t"
    "v_mfma_f32_16x16x32_bf16 v[204:207], a[228:231], v[132:135], v[204:207]\n\t"
    "s_add_u32 m0, s63, 0x10000\n\t"
    "s_add_u32 s68, s72, 0x1c080\n\t"
    "s_waitcnt lgkmcnt(8)\n\t"
    "v_mfma_f32_16x16x32_bf16 v[192:195], a[232:235], v[136:139], v[192:195]\n\t"
    "ds_read_b64_tr_b16 a[192:193], v44 offset:32768\n\t"
    "ds_read_b64_tr_b16 a[194:195], v44 offset:34816\n\t"
    "v_mfma_f32_16x16x32_bf16 v[196:199], a[232:235], v[140:143], v[196:199]\n\t"
    "buffer_load_dwordx4 v48, %[srdwo], s68 offen lds\n\t"
    "v_mfma_f32_16x16x32_bf16 v[200:203], a[236:239], v[136:139], v[200:203]\n\t"
    "ds_read_b64_tr_b16 a[196:197], v45 offset:32768\n\t"
    "ds_read_b64_tr_b16 a[198:199], v45 offset:34816\n\t"
    "v_mfma_f32_16x16x32_bf16 v[204:207], a[236:239], v[140:143], v[204:207]\n\t"
    "s_waitcnt lgkmcnt(8)\n\t"
    "v_mfma_f32_16x16x32_bf16 v[192:195], a[240:243], v[144:147], v[192:195]\n\t"
    "ds_read_b64_tr_b16 a[200:201], v44 offset:36864\n\t"
    "ds_read_b64_tr_b16 a[202:203], v44 offset:38912\n\t"
    "v_mfma_f32_16x16x32_bf16 v[196:199], a[240:243], v[148:151], v[196:199]\n\t"
    "buffer_load_dwordx4 v49, %[srdwo], s68 offen offset:1024 lds\n\t"
    "v_mfma_f32_16x16x32_bf16 v[200:203], a[244:247], v[144:147], v[200:203]\n\t"
    "ds_read_b64_tr_b16 a[204:205], v45 offset:36864\n\t"
    "ds_read_b64_tr_b16 a[206:207], v45 offset:38912\n\t"
    "v_mfma_f32_16x16x32_bf16 v[204:207], a[244:247], v[148:151], v[204:207]\n\t"
    "s_waitcnt lgkmcnt(8)\n\t"
    "v_mfma_f32_16x16x32_bf16 v[192:195], a[248:251], v[152:155], v[192:195]\n\t"
    "ds_read_b64_tr_b16 a[208:209], v44 offset:40960\n\t"
    "ds_read_b64_tr_b16 a[210:211], v44 offset:43008\n\t"
    "v_mfma_f32_16x16x32_bf16 v[196:199], a[248:251], v[156:159], v[196:199]\n\t"
    "buffer_load_dwordx4 v50, %[srdwo], s68 offen offset:2048 lds\n\t"
    "v_mfma_f32_16x16x32_bf16 v[200:203], a[252:255], v[152:155], v[200:203]\n\t"
    "ds_read_b64_tr_b16 a[212:213], v45 offset:40960\n\t"
    "ds_read_b64_tr_b16 a[214:215], v45 offset:43008\n\t"
    "v_mfma_f32_16x16x32_bf16 v[204:207], a[252:255], v[156:159], v[204:207]\n\t"
    "s_waitcnt lgkmcnt(8)\n\t"
    "v_mfma_f32_16x16x32_bf16 v[192:195], a[192:195], v[96:99], v[192:195]\n\t"
    "ds_read_b64_tr_b16 a[216:217], v44 offset:45056\n\t"
    "ds_read_b64_tr_b16 a[218:219], v44 offset:47104\n\t"
    "v_mfma_f32_16x16x32_bf16 v[196:199], a[192:195], v[100:103], v[196:199]\n\t"
    "v_mfma_f32_16x16x32_bf16 v[200:203], a[196:199], v[96:99], v[200:203]\n\t"
    "ds_read_b64_tr_b16 a[220:221], v45 offset:45056\n\t"
    "ds_read_b64_tr_b16 a[222:223], v45 offset:47104\n\t"
    "v_mfma_f32_16x16x32_bf16 v[204:207], a[196:199], v[100:103], v[204:207]\n\t"
    "buffer_load_dwordx4 v51, %[srdwo], s68 offen offset:3072 lds\n\t"
    "s_add_u32 m0, s63, 0x11000\n\t"
    "s_waitcnt lgkmcnt(8)\n\t"
    "v_mfma_f32_16x16x32_bf16 v[192:195], a[200:203], v[104:107], v[192:195]\n\t"
    "ds_read_b64_tr_b16 a[224:225], v46 offset:32768\n\t"
    "ds_read_b64_tr_b16 a[226:227], v46 offset:34816\n\t"
    "s_add_u32 s68, s72, 0x23080\n\t"
    "v_mfma_f32_16x16x32_bf16 v[196:199], a[200:203], v[108:111], v[196:199]\n\t"
    "buffer_load_dwordx4 v48, %[srdwo], s68 offen lds\n\t"
    "v_mfma_f32_16x16x32_bf16 v[200:203], a[204:207], v[104:107], v[200:203]\n\t"
    "ds_read_b64_tr_b16 a[228:229], v47 offset:32768\n\t"
    "ds_read_b64_tr_b16 a[230:231], v47 offset:34816\n\t"
    "v_mfma_f32_16x16x32_bf16 v[204:207], a[204:207], v[108:111], v[204:207]\n\t"
    "buffer_load_dwordx4 v49, %[srdwo], s68 offen offset:1024 lds\n\t"
    "s_waitcnt lgkmcnt(8)\n\t"
    "v_mfma_f32_16x16x32_bf16 v[192:195], a[208:211], v[112:115], v[192:195]\n\t"
    "ds_read_b64_tr_b16 a[232:233], v46 offset:36864\n\t"
    "ds_read_b64_tr_b16 a[234:235], v46 offset:38912\n\t"
    "v_mfma_f32_16x16x32_bf16 v[196:199], a[208:211], v[116:119], v[196:199]\n\t"
    "v_mfma_f32_16x16x32_bf16 v[200:203], a[212:215], v[112:115], v[200:203]\n\t"
    "ds_read_b64_tr_b16 a[236:237], v47 offset:36864\n\t"
    "ds_read_b64_tr_b16 a[238:239], v47 offset:38912\n\t"
    "v_mfma_f32_16x16x32_bf16 v[204:207], a[212:215], v[116:119], v[204:207]\n\t"
    "buffer_load_dwordx4 v50, %[srdwo], s68 offen offset:2048 lds\n\t"
    "s_waitcnt lgkmcnt(8)\n\t"
    "v_mfma_f32_16x16x32_bf16 v[192:195], a[216:219], v[120:123], v[192:195]\n\t"
    "ds_read_b64_tr_b16 a[240:241], v46 offset:40960\n\t"
    "ds_read_b64_tr_b16 a[242:243], v46 offset:43008\n\t"
    "v_mfma_f32_16x16x32_bf16 v[196:199], a[216:219], v[124:127], v[196:199]\n\t"
    "buffer_load_dwordx4 v51, %[srdwo], s68 offen offset:3072 lds\n\t"
    "v_mfma_f32_16x16x32_bf16 v[200:203], a[220:223], v[120:123], v[200:203]\n\t"
    "ds_read_b64_tr_b16 a[244:245], v47 offset:40960\n\t"
    "ds_read_b64_tr_b16 a[246:247], v47 offset:43008\n\t"
    "v_mfma_f32_16x16x32_bf16 v[204:207], a[220:223], v[124:127], v[204:207]\n\t"
    "s_waitcnt lgkmcnt(8)\n\t"
    "v_mfma_f32_16x16x32_bf16 v[192:195], a[224:227], v[160:163], v[192:195]\n\t"
    "ds_read_b64_tr_b16 a[248:249], v46 offset:45056\n\t"
    "ds_read_b64_tr_b16 a[250:251], v46 offset:47104\n\t"
    "v_mfma_f32_16x16x32_bf16 v[196:199], a[224:227], v[164:167], v[196:199]\n\t"
    "s_waitcnt vmcnt(8)\n\t"
    "s_barrier\n\t"
    "v_mfma_f32_16x16x32_bf16 v[200:203], a[228:231], v[160:163], v[200:203]\n\t"
    "ds_read_b64_tr_b16 a[252:253], v47 offset:45056\n\t"
    "ds_read_b64_tr_b16 a[254:255], v47 offset:47104\n\t"
    "v_mfma_f32_16x16x32_bf16 v[204:207], a[228:231], v[164:167], v[204:207]\n\t"
    "s_add_u32 m0, s63, 0x0\n\t"
    "s_add_u32 s68, s72, 0x100\n\t"
    "s_waitcnt lgkmcnt(8)\n\t"
    "v_mfma_f32_16x16x32_bf16 v[192:195], a[232:235], v[168:171], v[192:195]\n\t"
    "ds_read_b64_tr_b16 a[192:193], v240 offset:32768\n\t"
    "ds_read_b64_tr_b16 a[194:195], v240 offset:34816\n\t"
    "v_mfma_f32_16x16x32_bf16 v[196:199], a[232:235], v[172:175], v[196:199]\n\t"
    "buffer_load_dwordx4 v48, %[srdwo], s68 offen lds\n\t"
    "v_mfma_f32_16x16x32_bf16 v[200:203], a[236:239], v[168:171], v[200:203]\n\t"
    "ds_read_b64_tr_b16 a[196:197], v241 offset:32768\n\t"
    "ds_read_b64_tr_b16 a[198:199], v241 offset:34816\n\t"
    "v_mfma_f32_16x16x32_bf16 v[204:207], a[236:239], v[172:175], v[204:207]\n\t"
    "s_waitcnt lgkmcnt(8)\n\t"
    "v_mfma_f32_16x16x32_bf16 v[192:195], a[240:243], v[176:179], v[192:195]\n\t"
    "ds_read_b64_tr_b16 a[200:201], v240 offset:36864\n\t"
    "ds_read_b64_tr_b16 a[202:203], v240 offset:38912\n\t"
    "v_mfma_f32_16x16x32_bf16 v[196:199], a[240:243], v[180:183], v[196:199]\n\t"
    "buffer_load_dwordx4 v49, %[srdwo], s68 offen offset:1024 lds\n\t"
    "v_mfma_f32_16x16x32_bf16 v[200:203], a[244:247], v[176:179], v[200:203]\n\t"
    "ds_read_b64_tr_b16 a[204:205], v241 offset:36864\n\t"
    "ds_read_b64_tr_b16 a[206:207], v241 offset:38912\n\t"
    "v_mfma_f32_16x16x32_bf16 v[204:207], a[244:247], v[180:183], v[204:207]\n\t"
    "s_waitcnt lgkmcnt(8)\n\t"
    "v_mfma_f32_16x16x32_bf16 v[192:195], a[248:251], v[184:187], v[192:195]\n\t"
    "ds_read_b64_tr_b16 a[208:209], v240 offset:40960\n\t"
    "ds_read_b64_tr_b16 a[210:211], v240 offset:43008\n\t"
    "v_mfma_f32_16x16x32_bf16 v[196:199], a[248:251], v[188:191], v[196:199]\n\t"
    "buffer_load_dwordx4 v50, %[srdwo], s68 offen offset:2048 lds\n\t"
    "v_mfma_f32_16x16x32_bf16 v[200:203], a[252:255], v[184:187], v[200:203]\n\t"
    "ds_read_b64_tr_b16 a[212:213], v241 offset:40960\n\t"
    "ds_read_b64_tr_b16 a[214:215], v241 offset:43008\n\t"
    "v_mfma_f32_16x16x32_bf16 v[204:207], a[252:255], v[188:191], v[204:207]\n\t"
    "s_waitcnt lgkmcnt(8)\n\t"
    "v_mfma_f32_16x16x32_bf16 v[208:211], a[192:195], v[64:67], 0\n\t"
    "ds_read_b64_tr_b16 a[216:217], v240 offset:45056\n\t"
    "ds_read_b64_tr_b16 a[218:219], v240 offset:47104\n\t"
    "v_mfma_f32_16x16x32_bf16 v[212:215], a[192:195], v[68:71], 0\n\t"
    "v_mfma_f32_16x16x32_bf16 v[216:219], a[196:199], v[64:67], 0\n\t"
    "ds_read_b64_tr_b16 a[220:221], v241 offset:45056\n\t"
    "ds_read_b64_tr_b16 a[222:223], v241 offset:47104\n\t"
    "v_mfma_f32_16x16x32_bf16 v[220:223], a[196:199], v[68:71], 0\n\t"
    "buffer_load_dwordx4 v51, %[srdwo], s68 offen offset:3072 lds\n\t"
    "s_add_u32 m0, s63, 0x1000\n\t"
    "s_waitcnt lgkmcnt(8)\n\t"
    "v_mfma_f32_16x16x32_bf16 v[208:211], a[200:203], v[72:75], v[208:211]\n\t"
    "ds_read_b64_tr_b16 a[224:225], v242 offset:32768\n\t"
    "ds_read_b64_tr_b16 a[226:227], v242 offset:34816\n\t"
    "s_add_u32 s68, s72, 0x7100\n\t"
    "v_mfma_f32_16x16x32_bf16 v[212:215], a[200:203], v[76:79], v[212:215]\n\t"
    "buffer_load_dwordx4 v48, %[srdwo], s68 offen lds\n\t"
    "v_cvt_pk_bf16_f32 v224, v192, v193\n\t"
    "v_mfma_f32_16x16x32_bf16 v[216:219], a[204:207], v[72:75], v[216:219]\n\t"
    "ds_read_b64_tr_b16 a[228:229], v243 offset:32768\n\t"
    "ds_read_b64_tr_b16 a[230:231], v243 offset:34816\n\t"
    "v_mfma_f32_16x16x32_bf16 v[220:223], a[204:207], v[76:79], v[220:223]\n\t"
    "buffer_load_dwordx4 v49, %[srdwo], s68 offen offset:1024 lds\n\t"
    "s_waitcnt lgkmcnt(8)\n\t"
    "v_mfma_f32_16x16x32_bf16 v[208:211], a[208:211], v[80:83], v[208:211]\n\t"
    "ds_read_b64_tr_b16 a[232:233], v242 offset:36864\n\t"
    "ds_read_b64_tr_b16 a[234:235], v242 offset:38912\n\t"
    "v_mfma_f32_16x16x32_bf16 v[212:215], a[208:211], v[84:87], v[212:215]\n\t"
    "v_cvt_pk_bf16_f32 v225, v194, v195\n\t"
    "v_cvt_pk_bf16_f32 v226, v200, v201\n\t"
    "v_mfma_f32_16x16x32_bf16 v[216:219], a[212:215], v[80:83], v[216:219]\n\t"
    "ds_read_b64_tr_b16 a[236:237], v243 offset:36864\n\t"
    "ds_read_b64_tr_b16 a[238:239], v243 offset:38912\n\t"
    "v_mfma_f32_16x16x32_bf16 v[220:223], a[212:215], v[84:87], v[220:223]\n\t"
    "buffer_load_dwordx4 v50, %[srdwo], s68 offen offset:2048 lds\n\t"
    "s_waitcnt lgkmcnt(8)\n\t"
    "v_mfma_f32_16x16x32_bf16 v[208:211], a[216:219], v[88:91], v[208:211]\n\t"
    "ds_read_b64_tr_b16 a[240:241], v242 offset:40960\n\t"
    "ds_read_b64_tr_b16 a[242:243], v242 offset:43008\n\t"
    "v_mfma_f32_16x16x32_bf16 v[212:215], a[216:219], v[92:95], v[212:215]\n\t"
    "buffer_load_dwordx4 v51, %[srdwo], s68 offen offset:3072 lds\n\t"
    "v_cvt_pk_bf16_f32 v227, v202, v203\n\t"
    "v_mfma_f32_16x16x32_bf16 v[216:219], a[220:223], v[88:91], v[216:219]\n\t"
    "ds_read_b64_tr_b16 a[244:245], v243 offset:40960\n\t"
    "ds_read_b64_tr_b16 a[246:247], v243 offset:43008\n\t"
    "v_mfma_f32_16x16x32_bf16 v[220:223], a[220:223], v[92:95], v[220:223]\n\t"
    "v_cvt_pk_bf16_f32 v228, v196, v197\n\t"
    "s_waitcnt lgkmcnt(8)\n\t"
    "v_mfma_f32_16x16x32_bf16 v[208:211], a[224:227], v[128:131], v[208:211]\n\t"
    "ds_read_b64_tr_b16 a[248:249], v242 offset:45056\n\t"
    "ds_read_b64_tr_b16 a[250:251], v242 offset:47104\n\t"
    "v_mfma_f32_16x16x32_bf16 v[212:215], a[224:227], v[132:135], v[212:215]\n\t"
    "s_waitcnt vmcnt(8)\n\t"
    "s_barrier\n\t"
    "v_mfma_f32_16x16x32_bf16 v[216:219], a[228:231], v[128:131], v[216:219]\n\t"
    "ds_read_b64_tr_b16 a[252:253], v243 offset:45056\n\t"
    "ds_read_b64_tr_b16 a[254:255], v243 offset:47104\n\t"
    "v_mfma_f32_16x16x32_bf16 v[220:223], a[228:231], v[132:135], v[220:223]\n\t"
    "s_add_u32 m0, s63, 0x8000\n\t"
    "s_add_u32 s68, s72, 0x1c100\n\t"
    "s_waitcnt lgkmcnt(8)\n\t"
    "v_mfma_f32_16x16x32_bf16 v[208:211], a[232:235], v[136:139], v[208:211]\n\t"
    "ds_read_b64_tr_b16 a[192:193], v240\n\t"
    "ds_read_b64_tr_b16 a[194:195], v240 offset:2048\n\t"
    "v_mfma_f32_16x16x32_bf16 v[212:215], a[232:235], v[140:143], v[212:215]\n\t"
    "buffer_load_dwordx4 v48, %[srdwo], s68 offen lds\n\t"
    "v_cvt_pk_bf16_f32 v229, v198, v199\n\t"
    "v_mfma_f32_16x16x32_bf16 v[216:219], a[236:239], v[136:139], v[216:219]\n\t"
    "ds_read_b64_tr_b16 a[196:197], v241\n\t"
    "ds_read_b64_tr_b16 a[198:199], v241 offset:2048\n\t"
    "v_mfma_f32_16x16x32_bf16 v[220:223], a[236:239], v[140:143], v[220:223]\n\t"
    "v_cvt_pk_bf16_f32 v230, v204, v205\n\t"
    "s_waitcnt lgkmcnt(8)\n\t"
    "v_mfma_f32_16x16x32_bf16 v[208:211], a[240:243], v[144:147], v[208:211]\n\t"
    "ds_read_b64_tr_b16 a[200:201], v240 offset:4096\n\t"
    "ds_read_b64_tr_b16 a[202:203], v240 offset:6144\n\t"
    "v_mfma_f32_16x16x32_bf16 v[212:215], a[240:243], v[148:151], v[212:215]\n\t"
    "buffer_load_dwordx4 v49, %[srdwo], s68 offen offset:1024 lds\n\t"
    "v_cvt_pk_bf16_f32 v231, v206, v207\n\t"
    "v_mfma_f32_16x16x32_bf16 v[216:219], a[244:247], v[144:147], v[216:219]\n\t"
    "ds_read_b64_tr_b16 a[204:205], v241 offset:4096\n\t"
    "ds_read_b64_tr_b16 a[206:207], v241 offset:6144\n\t"
    "v_mfma_f32_16x16x32_bf16 v[220:223], a[244:247], v[148:151], v[220:223]\n\t"
    "s_movk_i32 s64, 0x0\n\t"
    "s_waitcnt lgkmcnt(8)\n\t"
    "v_mfma_f32_16x16x32_bf16 v[208:211], a[248:251], v[152:155], v[208:211]\n\t"
    "ds_read_b64_tr_b16 a[208:209], v240 offset:8192\n\t"
    "ds_read_b64_tr_b16 a[210:211], v240 offset:10240\n\t"
    "v_mfma_f32_16x16x32_bf16 v[212:215], a[248:251], v[156:159], v[212:215]\n\t"
    "buffer_load_dwordx4 v50, %[srdwo], s68 offen offset:2048 lds\n\t"
    "buffer_store_dwordx4 v[224:227], v53, %[srdpt], s64 offen sc1\n\t"
    "v_mfma_f32_16x16x32_bf16 v[216:219], a[252:255], v[152:155], v[216:219]\n\t"
    "ds_read_b64_tr_b16 a[212:213], v241 offset:8192\n\t"
    "ds_read_b64_tr_b16 a[214:215], v241 offset:10240\n\t"
    "v_mfma_f32_16x16x32_bf16 v[220:223], a[252:255], v[156:159], v[220:223]\n\t"
    "buffer_store_dwordx4 v[228:231], v53, %[srdpt], s64 offen offset:256 sc1\n\t"
    "s_waitcnt lgkmcnt(8)\n\t"
    "v_mfma_f32_16x16x32_bf16 v[208:211], a[192:195], v[96:99], v[208:211]\n\t"
    "ds_read_b64_tr_b16 a[216:217], v240 offset:12288\n\t"
    "ds_read_b64_tr_b16 a[218:219], v240 offset:14336\n\t"
    "v_mfma_f32_16x16x32_bf16 v[212:215], a[192:195], v[100:103], v[212:215]\n\t"
    "v_mfma_f32_16x16x32_bf16 v[216:219], a[196:199], v[96:99], v[216:219]\n\t"
    "ds_read_b64_tr_b16 a[220:221], v241 offset:12288\n\t"
    "ds_read_b64_tr_b16 a[222:223], v241 offset:14336\n\t"
    "v_mfma_f32_16x16x32_bf16 v[220:223], a[196:199], v[100:103], v[220:223]\n\t"
    "buffer_load_dwordx4 v51, %[srdwo], s68 offen offset:3072 lds\n\t"
    "s_add_u32 m0, s63, 0x9000\n\t"
    "s_waitcnt lgkmcnt(8)\n\t"
    "v_mfma_f32_16x16x32_bf16 v[208:211], a[200:203], v[104:107], v[208:211]\n\t"
    "ds_read_b64_tr_b16 a[224:225], v242\n\t"
    "ds_read_b64_tr_b16 a[226:227], v242 offset:2048\n\t"
    "s_add_u32 s68, s72, 0x23100\n\t"
    "v_mfma_f32_16x16x32_bf16 v[212:215], a[200:203], v[108:111], v[212:215]\n\t"
    "buffer_load_dwordx4 v48, %[srdwo], s68 offen lds\n\t"
    "v_mfma_f32_16x16x32_bf16 v[216:219], a[204:207], v[104:107], v[216:219]\n\t"
    "ds_read_b64_tr_b16 a[228:229], v243\n\t"
    "ds_read_b64_tr_b16 a[230:231], v243 offset:2048\n\t"
    "v_mfma_f32_16x16x32_bf16 v[220:223], a[204:207], v[108:111], v[220:223]\n\t"
    "buffer_load_dwordx4 v49, %[srdwo], s68 offen offset:1024 lds\n\t"
    "s_waitcnt lgkmcnt(8)\n\t"
    "v_mfma_f32_16x16x32_bf16 v[208:211], a[208:211], v[112:115], v[208:211]\n\t"
    "ds_read_b64_tr_b16 a[232:233], v242 offset:4096\n\t"
    "ds_read_b64_tr_b16 a[234:235], v242 offset:6144\n\t"
    "v_mfma_f32_16x16x32_bf16 v[212:215], a[208:211], v[116:119], v[212:215]\n\t"
    "v_mfma_f32_16x16x32_bf16 v[216:219], a[212:215], v[112:115], v[216:219]\n\t"
    "ds_read_b64_tr_b16 a[236:237], v243 offset:4096\n\t"
    "ds_read_b64_tr_b16 a[238:239], v243 offset:6144\n\t"
    "v_mfma_f32_16x16x32_bf16 v[220:223], a[212:215], v[116:119], v[220:223]\n\t"
    "buffer_load_dwordx4 v50, %[srdwo], s68 offen offset:2048 lds\n\t"
    "s_waitcnt lgkmcnt(8)\n\t"
    "v_mfma_f32_16x16x32_bf16 v[208:211], a[216:219], v[120:123], v[208:211]\n\t"
    "ds_read_b64_tr_b16 a[240:241], v242 offset:8192\n\t"
    "ds_read_b64_tr_b16 a[242:243], v242 offset:10240\n\t"
    "v_mfma_f32_16x16x32_bf16 v[212:215], a[216:219], v[124:127], v[212:215]\n\t"
    "buffer_load_dwordx4 v51, %[srdwo], s68 offen offset:3072 lds\n\t"
    "v_mfma_f32_16x16x32_bf16 v[216:219], a[220:223], v[120:123], v[216:219]\n\t"
    "ds_read_b64_tr_b16 a[244:245], v243 offset:8192\n\t"
    "ds_read_b64_tr_b16 a[246:247], v243 offset:10240\n\t"
    "v_mfma_f32_16x16x32_bf16 v[220:223], a[220:223], v[124:127], v[220:223]\n\t"
    "s_waitcnt lgkmcnt(8)\n\t"
    "v_mfma_f32_16x16x32_bf16 v[208:211], a[224:227], v[160:163], v[208:211]\n\t"
    "ds_read_b64_tr_b16 a[248:249], v242 offset:12288\n\t"
    "ds_read_b64_tr_b16 a[250:251], v242 offset:14336\n\t"
    "v_mfma_f32_16x16x32_bf16 v[212:215], a[224:227], v[164:167], v[212:215]\n\t"
    "s_waitcnt vmcnt(10)\n\t"
    "s_barrier\n\t"
    "v_mfma_f32_16x16x32_bf16 v[216:219], a[228:231], v[160:163], v[216:219]\n\t"
    "ds_read_b64_tr_b16 a[252:253], v243 offset:12288\n\t"
    "ds_read_b64_tr_b16 a[254:255], v243 offset:14336\n\t"
    "v_mfma_f32_16x16x32_bf16 v[220:223], a[228:231], v[164:167], v[220:223]\n\t"
    "s_add_u32 m0, s63, 0x18000\n\t"
    "s_add_u32 s68, s72, 0x180\n\t"
    "s_waitcnt lgkmcnt(8)\n\t"
    "v_mfma_f32_16x16x32_bf16 v[208:211], a[232:235], v[168:171], v[208:211]\n\t"
    "ds_read_b64_tr_b16 a[192:193], v44\n\t"
    "ds_read_b64_tr_b16 a[194:195], v44 offset:2048\n\t"
    "v_mfma_f32_16x16x32_bf16 v[212:215], a[232:235], v[172:175], v[212:215]\n\t"
    "buffer_load_dwordx4 v48, %[srdwo], s68 offen lds\n\t"
    "v_mfma_f32_16x16x32_bf16 v[216:219], a[236:239], v[168:171], v[216:219]\n\t"
    "ds_read_b64_tr_b16 a[196:197], v45\n\t"
    "ds_read_b64_tr_b16 a[198:199], v45 offset:2048\n\t"
    "v_mfma_f32_16x16x32_bf16 v[220:223], a[236:239], v[172:175], v[220:223]\n\t"
    "s_waitcnt lgkmcnt(8)\n\t"
    "v_mfma_f32_16x16x32_bf16 v[208:211], a[240:243], v[176:179], v[208:211]\n\t"
    "ds_read_b64_tr_b16 a[200:201], v44 offset:4096\n\t"
    "ds_read_b64_tr_b16 a[202:203], v44 offset:6144\n\t"
    "v_mfma_f32_16x16x32_bf16 v[212:215], a[240:243], v[180:183], v[212:215]\n\t"
    "buffer_load_dwordx4 v49, %[srdwo], s68 offen offset:1024 lds\n\t"
    "v_mfma_f32_16x16x32_bf16 v[216:219], a[244:247], v[176:179], v[216:219]\n\t"
    "ds_read_b64_tr_b16 a[204:205], v45 offset:4096\n\t"
    "ds_read_b64_tr_b16 a[206:207], v45 offset:6144\n\t"
    "v_mfma_f32_16x16x32_bf16 v[220:223], a[244:247], v[180:183], v[220:223]\n\t"
    "s_waitcnt lgkmcnt(8)\n\t"
    "v_mfma_f32_16x16x32_bf16 v[208:211], a[248:251], v[184:187], v[208:211]\n\t"
    "ds_read_b64_tr_b16 a[208:209], v44 offset:8192\n\t"
    "ds_read_b64_tr_b16 a[210:211], v44 offset:10240\n\t"
    "v_mfma_f32_16x16x32_bf16 v[212:215], a[248:251], v[188:191], v[212:215]\n\t"
    "buffer_load_dwordx4 v50, %[srdwo], s68 offen offset:2048 lds\n\t"
    "v_mfma_f32_16x16x32_bf16 v[216:219], a[252:255], v[184:187], v[216:219]\n\t"
    "ds_read_b64_tr_b16 a[212:213], v45 offset:8192\n\t"
    "ds_read_b64_tr_b16 a[214:215], v45 offset:10240\n\t"
    "v_mfma_f32_16x16x32_bf16 v[220:223], a[252:255], v[188:191], v[220:223]\n\t"
    "s_waitcnt lgkmcnt(8)\n\t"
    "v_mfma_f32_16x16x32_bf16 v[192:195], a[192:195], v[64:67], 0\n\t"
    "ds_read_b64_tr_b16 a[216:217], v44 offset:12288\n\t"
    "ds_read_b64_tr_b16 a[218:219], v44 offset:14336\n\t"
    "v_mfma_f32_16x16x32_bf16 v[196:199], a[192:195], v[68:71], 0\n\t"
    "v_mfma_f32_16x16x32_bf16 v[200:203], a[196:199], v[64:67], 0\n\t"
    "ds_read_b64_tr_b16 a[220:221], v45 offset:12288\n\t"
    "ds_read_b64_tr_b16 a[222:223], v45 offset:14336\n\t"
    "v_mfma_f32_16x16x32_bf16 v[204:207], a[196:199], v[68:71], 0\n\t"
    "buffer_load_dwordx4 v51, %[srdwo], s68 offen offset:3072 lds\n\t"
    "s_add_u32 m0, s63, 0x19000\n\t"
    "s_waitcnt lgkmcnt(8)\n\t"
    "v_mfma_f32_16x16x32_bf16 v[192:195], a[200:203], v[72:75], v[192:195]\n\t"
    "ds_read_b64_tr_b16 a[224:225], v46\n\t"
    "ds_read_b64_tr_b16 a[226:227], v46 offset:2048\n\t"
    "s_add_u32 s68, s72, 0x7180\n\t"
    "v_mfma_f32_16x16x32_bf16 v[196:199], a[200:203], v[76:79], v[196:199]\n\t"
    "buffer_load_dwordx4 v48, %[srdwo], s68 offen lds\n\t"
    "v_cvt_pk_bf16_f32 v224, v208, v209\n\t"
    "v_mfma_f32_16x16x32_bf16 v[200:203], a[204:207], v[72:75], v[200:203]\n\t"
    "ds_read_b64_tr_b16 a[228:229], v47\n\t"
    "ds_read_b64_tr_b16 a[230:231], v47 offset:2048\n\t"
    "v_mfma_f32_16x16x32_bf16 v[204:207], a[204:207], v[76:79], v[204:207]\n\t"
    "buffer_load_dwordx4 v49, %[srdwo], s68 offen offset:1024 lds\n\t"
    "s_waitcnt lgkmcnt(8)\n\t"
    "v_mfma_f32_16x16x32_bf16 v[192:195], a[208:211], v[80:83], v[192:195]\n\t"
    "ds_read_b64_tr_b16 a[232:233], v46 offset:4096\n\t"
    "ds_read_b64_tr_b16 a[234:235], v46 offset:6144\n\t"
    "v_mfma_f32_16x16x32_bf16 v[196:199], a[208:211], v[84:87], v[196:199]\n\t"
    "v_cvt_pk_bf16_f32 v225, v210, v211\n\t"
    "v_cvt_pk_bf16_f32 v226, v216, v217\n\t"
    "v_mfma_f32_16x16x32_bf16 v[200:203], a[212:215], v[80:83], v[200:203]\n\t"
    "ds_read_b64_tr_b16 a[236:237], v47 offset:4096\n\t"
    "ds_read_b64_tr_b16 a[238:239], v47 offset:6144\n\t"
    "v_mfma_f32_16x16x32_bf16 v[204:207], a[212:215], v[84:87], v[204:207]\n\t"
    "buffer_load_dwordx4 v50, %[srdwo], s68 offen offset:2048 lds\n\t"
    "s_waitcnt lgkmcnt(8)\n\t"
    "v_mfma_f32_16x16x32_bf16 v[192:195], a[216:219], v[88:91], v[192:195]\n\t"
    "ds_read_b64_tr_b16 a[240:241], v46 offset:8192\n\t"
    "ds_read_b64_tr_b16 a[242:243], v46 offset:10240\n\t"
    "v_mfma_f32_16x16x32_bf16 v[196:199], a[216:219], v[92:95], v[196:199]\n\t"
    "buffer_load_dwordx4 v51, %[srdwo], s68 offen offset:3072 lds\n\t"
    "v_cvt_pk_bf16_f32 v227, v218, v219\n\t"
    "v_mfma_f32_16x16x32_bf16 v[200:203], a[220:223], v[88:91], v[200:203]\n\t"
    "ds_read_b64_tr_b16 a[244:245], v47 offset:8192\n\t"
    "ds_read_b64_tr_b16 a[246:247], v47 offset:10240\n\t"
    "v_mfma_f32_16x16x32_bf16 v[204:207], a[220:223], v[92:95], v[204:207]\n\t"
    "v_cvt_pk_bf16_f32 v228, v212, v213\n\t"
    "s_waitcnt lgkmcnt(8)\n\t"
    "v_mfma_f32_16x16x32_bf16 v[192:195], a[224:227], v[128:131], v[192:195]\n\t"
    "ds_read_b64_tr_b16 a[248:249], v46 offset:12288\n\t"
    "ds_read_b64_tr_b16 a[250:251], v46 offset:14336\n\t"
    "v_mfma_f32_16x16x32_bf16 v[196:199], a[224:227], v[132:135], v[196:199]\n\t"
    "s_waitcnt vmcnt(8)\n\t"
    "s_barrier\n\t"
    "v_mfma_f32_16x16x32_bf16 v[200:203], a[228:231], v[128:131], v[200:203]\n\t"
    "ds_read_b64_tr_b16 a[252:253], v47 offset:12288\n\t"
    "ds_read_b64_tr_b16 a[254:255], v47 offset:14336\n\t"
    "v_mfma_f32_16x16x32_bf16 v[204:207], a[228:231], v[132:135], v[204:207]\n\t"
    "s_add_u32 m0, s63, 0x10000\n\t"
    "s_add_u32 s68, s72, 0x1c180\n\t"
    "s_waitcnt lgkmcnt(8)\n\t"
    "v_mfma_f32_16x16x32_bf16 v[192:195], a[232:235], v[136:139], v[192:195]\n\t"
    "ds_read_b64_tr_b16 a[192:193], v44 offset:32768\n\t"
    "ds_read_b64_tr_b16 a[194:195], v44 offset:34816\n\t"
    "v_mfma_f32_16x16x32_bf16 v[196:199], a[232:235], v[140:143], v[196:199]\n\t"
    "buffer_load_dwordx4 v48, %[srdwo], s68 offen lds\n\t"
    "v_cvt_pk_bf16_f32 v229, v214, v215\n\t"
    "v_mfma_f32_16x16x32_bf16 v[200:203], a[236:239], v[136:139], v[200:203]\n\t"
    "ds_read_b64_tr_b16 a[196:197], v45 offset:32768\n\t"
    "ds_read_b64_tr_b16 a[198:199], v45 offset:34816\n\t"
    "v_mfma_f32_16x16x32_bf16 v[204:207], a[236:239], v[140:143], v[204:207]\n\t"
    "v_cvt_pk_bf16_f32 v230, v220, v221\n\t"
    "s_waitcnt lgkmcnt(8)\n\t"
    "v_mfma_f32_16x16x32_bf16 v[192:195], a[240:243], v[144:147], v[192:195]\n\t"
    "ds_read_b64_tr_b16 a[200:201], v44 offset:36864\n\t"
    "ds_read_b64_tr_b16 a[202:203], v44 offset:38912\n\t"
    "v_mfma_f32_16x16x32_bf16 v[196:199], a[240:243], v[148:151], v[196:199]\n\t"
    "buffer_load_dwordx4 v49, %[srdwo], s68 offen offset:1024 lds\n\t"
    "v_cvt_pk_bf16_f32 v231, v222, v223\n\t"
    "v_mfma_f32_16x16x32_bf16 v[200:203], a[244:247], v[144:147], v[200:203]\n\t"
    "ds_read_b64_tr_b16 a[204:205], v45 offset:36864\n\t"
    "ds_read_b64_tr_b16 a[206:207], v45 offset:38912\n\t"
    "v_mfma_f32_16x16x32_bf16 v[204:207], a[244:247], v[148:151], v[204:207]\n\t"
    "s_movk_i32 s64, 0x1000\n\t"
    "s_waitcnt lgkmcnt(8)\n\t"
    "v_mfma_f32_16x16x32_bf16 v[192:195], a[248:251], v[152:155], v[192:195]\n\t"
    "ds_read_b64_tr_b16 a[208:209], v44 offset:40960\n\t"
    "ds_read_b64_tr_b16 a[210:211], v44 offset:43008\n\t"
    "v_mfma_f32_16x16x32_bf16 v[196:199], a[248:251], v[156:159], v[196:199]\n\t"
    "buffer_load_dwordx4 v50, %[srdwo], s68 offen offset:2048 lds\n\t"
    "buffer_store_dwordx4 v[224:227], v53, %[srdpt], s64 offen sc1\n\t"
    "v_mfma_f32_16x16x32_bf16 v[200:203], a[252:255], v[152:155], v[200:203]\n\t"
    "ds_read_b64_tr_b16 a[212:213], v45 offset:40960\n\t"
    "ds_read_b64_tr_b16 a[214:215], v45 offset:43008\n\t"
    "v_mfma_f32_16x16x32_bf16 v[204:207], a[252:255], v[156:159], v[204:207]\n\t"
    "buffer_store_dwordx4 v[228:231], v53, %[srdpt], s64 offen offset:256 sc1\n\t"
    "s_waitcnt lgkmcnt(8)\n\t"
    "v_mfma_f32_16x16x32_bf16 v[192:195], a[192:195], v[96:99], v[192:195]\n\t"
    "ds_read_b64_tr_b16 a[216:217], v44 offset:45056\n\t"
    "ds_read_b64_tr_b16 a[218:219], v44 offset:47104\n\t"
    "v_mfma_f32_16x16x32_bf16 v[196:199], a[192:195], v[100:103], v[196:199]\n\t"
    "v_mfma_f32_16x16x32_bf16 v[200:203], a[196:199], v[96:99], v[200:203]\n\t"
    "ds_read_b64_tr_b16 a[220:221], v45 offset:45056\n\t"
    "ds_read_b64_tr_b16 a[222:223], v45 offset:47104\n\t"
    "v_mfma_f32_16x16x32_bf16 v[204:207], a[196:199], v[100:103], v[204:207]\n\t"
    "buffer_load_dwordx4 v51, %[srdwo], s68 offen offset:3072 lds\n\t"
    "s_add_u32 m0, s63, 0x11000\n\t"
    "s_waitcnt lgkmcnt(8)\n\t"
    "v_mfma_f32_16x16x32_bf16 v[192:195], a[200:203], v[104:107], v[192:195]\n\t"
    "ds_read_b64_tr_b16 a[224:225], v46 offset:32768\n\t"
    "ds_read_b64_tr_b16 a[226:227], v46 offset:34816\n\t"
    "s_add_u32 s68, s72, 0x23180\n\t"
    "v_mfma_f32_16x16x32_bf16 v[196:199], a[200:203], v[108:111], v[196:199]\n\t"
    "buffer_load_dwordx4 v48, %[srdwo], s68 offen lds\n\t"
    "v_mfma_f32_16x16x32_bf16 v[200:203], a[204:207], v[104:107], v[200:203]\n\t"
    "ds_read_b64_tr_b16 a[228:229], v47 offset:32768\n\t"
    "ds_read_b64_tr_b16 a[230:231], v47 offset:34816\n\t"
    "v_mfma_f32_16x16x32_bf16 v[204:207], a[204:207], v[108:111], v[204:207]\n\t"
    "buffer_load_dwordx4 v49, %[srdwo], s68 offen offset:1024 lds\n\t"
    "s_waitcnt lgkmcnt(8)\n\t"
    "v_mfma_f32_16x16x32_bf16 v[192:195], a[208:211], v[112:115], v[192:195]\n\t"
    "ds_read_b64_tr_b16 a[232:233], v46 offset:36864\n\t"
    "ds_read_b64_tr_b16 a[234:235], v46 offset:38912\n\t"
    "v_mfma_f32_16x16x32_bf16 v[196:199], a[208:211], v[116:119], v[196:199]\n\t"
    "v_mfma_f32_16x16x32_bf16 v[200:203], a[212:215], v[112:115], v[200:203]\n\t"
    "ds_read_b64_tr_b16 a[236:237], v47 offset:36864\n\t"
    "ds_read_b64_tr_b16 a[238:239], v47 offset:38912\n\t"
    "v_mfma_f32_16x16x32_bf16 v[204:207], a[212:215], v[116:119], v[204:207]\n\t"
    "buffer_load_dwordx4 v50, %[srdwo], s68 offen offset:2048 lds\n\t"
    "s_waitcnt lgkmcnt(8)\n\t"
    "v_mfma_f32_16x16x32_bf16 v[192:195], a[216:219], v[120:123], v[192:195]\n\t"
    "ds_read_b64_tr_b16 a[240:241], v46 offset:40960\n\t"
    "ds_read_b64_tr_b16 a[242:243], v46 offset:43008\n\t"
    "v_mfma_f32_16x16x32_bf16 v[196:199], a[216:219], v[124:127], v[196:199]\n\t"
    "buffer_load_dwordx4 v51, %[srdwo], s68 offen offset:3072 lds\n\t"
    "v_mfma_f32_16x16x32_bf16 v[200:203], a[220:223], v[120:123], v[200:203]\n\t"
    "ds_read_b64_tr_b16 a[244:245], v47 offset:40960\n\t"
    "ds_read_b64_tr_b16 a[246:247], v47 offset:43008\n\t"
    "v_mfma_f32_16x16x32_bf16 v[204:207], a[220:223], v[124:127], v[204:207]\n\t"
    "s_waitcnt lgkmcnt(8)\n\t"
    "v_mfma_f32_16x16x32_bf16 v[192:195], a[224:227], v[160:163], v[192:195]\n\t"
    "ds_read_b64_tr_b16 a[248:249], v46 offset:45056\n\t"
    "ds_read_b64_tr_b16 a[250:251], v46 offset:47104\n\t"
    "v_mfma_f32_16x16x32_bf16 v[196:199], a[224:227], v[164:167], v[196:199]\n\t"
    "s_waitcnt vmcnt(10)\n\t"
    "s_barrier\n\t"
    "v_mfma_f32_16x16x32_bf16 v[200:203], a[228:231], v[160:163], v[200:203]\n\t"
    "ds_read_b64_tr_b16 a[252:253], v47 offset:45056\n\t"
    "ds_read_b64_tr_b16 a[254:255], v47 offset:47104\n\t"
    "v_mfma_f32_16x16x32_bf16 v[204:207], a[228:231], v[164:167], v[204:207]\n\t"
    "s_add_u32 m0, s63, 0x0\n\t"
    "s_add_u32 s68, s72, 0x200\n\t"
    "s_waitcnt lgkmcnt(8)\n\t"
    "v_mfma_f32_16x16x32_bf16 v[192:195], a[232:235], v[168:171], v[192:195]\n\t"
    "ds_read_b64_tr_b16 a[192:193], v240 offset:32768\n\t"
    "ds_read_b64_tr_b16 a[194:195], v240 offset:34816\n\t"
    "v_mfma_f32_16x16x32_bf16 v[196:199], a[232:235], v[172:175], v[196:199]\n\t"
    "buffer_load_dwordx4 v48, %[srdwo], s68 offen lds\n\t"
    "v_mfma_f32_16x16x32_bf16 v[200:203], a[236:239], v[168:171], v[200:203]\n\t"
    "ds_read_b64_tr_b16 a[196:197], v241 offset:32768\n\t"
    "ds_read_b64_tr_b16 a[198:199], v241 offset:34816\n\t"
    "v_mfma_f32_16x16x32_bf16 v[204:207], a[236:239], v[172:175], v[204:207]\n\t"
    "s_waitcnt lgkmcnt(8)\n\t"
    "v_mfma_f32_16x16x32_bf16 v[192:195], a[240:243], v[176:179], v[192:195]\n\t"
    "ds_read_b64_tr_b16 a[200:201], v240 offset:36864\n\t"
    "ds_read_b64_tr_b16 a[202:203], v240 offset:38912\n\t"
    "v_mfma_f32_16x16x32_bf16 v[196:199], a[240:243], v[180:183], v[196:199]\n\t"
    "buffer_load_dwordx4 v49, %[srdwo], s68 offen offset:1024 lds\n\t"
    "v_mfma_f32_16x16x32_bf16 v[200:203], a[244:247], v[176:179], v[200:203]\n\t"
    "ds_read_b64_tr_b16 a[204:205], v241 offset:36864\n\t"
    "ds_read_b64_tr_b16 a[206:207], v241 offset:38912\n\t"
    "v_mfma_f32_16x16x32_bf16 v[204:207], a[244:247], v[180:183], v[204:207]\n\t"
    "s_waitcnt lgkmcnt(8)\n\t"
    "v_mfma_f32_16x16x32_bf16 v[192:195], a[248:251], v[184:187], v[192:195]\n\t"
    "ds_read_b64_tr_b16 a[208:209], v240 offset:40960\n\t"
    "ds_read_b64_tr_b16 a[210:211], v240 offset:43008\n\t"
    "v_mfma_f32_16x16x32_bf16 v[196:199], a[248:251], v[188:191], v[196:199]\n\t"
    "buffer_load_dwordx4 v50, %[srdwo], s68 offen offset:2048 lds\n\t"
    "v_mfma_f32_16x16x32_bf16 v[200:203], a[252:255], v[184:187], v[200:203]\n\t"
    "ds_read_b64_tr_b16 a[212:213], v241 offset:40960\n\t"
    "ds_read_b64_tr_b16 a[214:215], v241 offset:43008\n\t"
    "v_mfma_f32_16x16x32_bf16 v[204:207], a[252:255], v[188:191], v[204:207]\n\t"
    "s_waitcnt lgkmcnt(8)\n\t"
    "v_mfma_f32_16x16x32_bf16 v[208:211], a[192:195], v[64:67], 0\n\t"
    "ds_read_b64_tr_b16 a[216:217], v240 offset:45056\n\t"
    "ds_read_b64_tr_b16 a[218:219], v240 offset:47104\n\t"
    "v_mfma_f32_16x16x32_bf16 v[212:215], a[192:195], v[68:71], 0\n\t"
    "v_mfma_f32_16x16x32_bf16 v[216:219], a[196:199], v[64:67], 0\n\t"
    "ds_read_b64_tr_b16 a[220:221], v241 offset:45056\n\t"
    "ds_read_b64_tr_b16 a[222:223], v241 offset:47104\n\t"
    "v_mfma_f32_16x16x32_bf16 v[220:223], a[196:199], v[68:71], 0\n\t"
    "buffer_load_dwordx4 v51, %[srdwo], s68 offen offset:3072 lds\n\t"
    "s_add_u32 m0, s63, 0x1000\n\t"
    "s_waitcnt lgkmcnt(8)\n\t"
    "v_mfma_f32_16x16x32_bf16 v[208:211], a[200:203], v[72:75], v[208:211]\n\t"
    "ds_read_b64_tr_b16 a[224:225], v242 offset:32768\n\t"
    "ds_read_b64_tr_b16 a[226:227], v242 offset:34816\n\t"
    "s_add_u32 s68, s72, 0x7200\n\t"
    "v_mfma_f32_16x16x32_bf16 v[212:215], a[200:203], v[76:79], v[212:215]\n\t"
    "buffer_load_dwordx4 v48, %[srdwo], s68 offen lds\n\t"
    "v_cvt_pk_bf16_f32 v224, v192, v193\n\t"
    "v_mfma_f32_16x16x32_bf16 v[216:219], a[204:207], v[72:75], v[216:219]\n\t"
    "ds_read_b64_tr_b16 a[228:229], v243 offset:32768\n\t"
    "ds_read_b64_tr_b16 a[230:231], v243 offset:34816\n\t"
    "v_mfma_f32_16x16x32_bf16 v[220:223], a[204:207], v[76:79], v[220:223]\n\t"
    "buffer_load_dwordx4 v49, %[srdwo], s68 offen offset:1024 lds\n\t"
    "s_waitcnt lgkmcnt(8)\n\t"
    "v_mfma_f32_16x16x32_bf16 v[208:211], a[208:211], v[80:83], v[208:211]\n\t"
    "ds_read_b64_tr_b16 a[232:233], v242 offset:36864\n\t"
    "ds_read_b64_tr_b16 a[234:235], v242 offset:38912\n\t"
    "v_mfma_f32_16x16x32_bf16 v[212:215], a[208:211], v[84:87], v[212:215]\n\t"
    "v_cvt_pk_bf16_f32 v225, v194, v195\n\t"
    "v_cvt_pk_bf16_f32 v226, v200, v201\n\t"
    "v_mfma_f32_16x16x32_bf16 v[216:219], a[212:215], v[80:83], v[216:219]\n\t"
    "ds_read_b64_tr_b16 a[236:237], v243 offset:36864\n\t"
    "ds_read_b64_tr_b16 a[238:239], v243 offset:38912\n\t"
    "v_mfma_f32_16x16x32_bf16 v[220:223], a[212:215], v[84:87], v[220:223]\n\t"
    "buffer_load_dwordx4 v50, %[srdwo], s68 offen offset:2048 lds\n\t"
    "s_waitcnt lgkmcnt(8)\n\t"
    "v_mfma_f32_16x16x32_bf16 v[208:211], a[216:219], v[88:91], v[208:211]\n\t"
    "ds_read_b64_tr_b16 a[240:241], v242 offset:40960\n\t"
    "ds_read_b64_tr_b16 a[242:243], v242 offset:43008\n\t"
    "v_mfma_f32_16x16x32_bf16 v[212:215], a[216:219], v[92:95], v[212:215]\n\t"
    "buffer_load_dwordx4 v51, %[srdwo], s68 offen offset:3072 lds\n\t"
    "v_cvt_pk_bf16_f32 v227, v202, v203\n\t"
    "v_mfma_f32_16x16x32_bf16 v[216:219], a[220:223], v[88:91], v[216:219]\n\t"
    "ds_read_b64_tr_b16 a[244:245], v243 offset:40960\n\t"
    "ds_read_b64_tr_b16 a[246:247], v243 offset:43008\n\t"
    "v_mfma_f32_16x16x32_bf16 v[220:223], a[220:223], v[92:95], v[220:223]\n\t"
    "v_cvt_pk_bf16_f32 v228, v196, v197\n\t"
    "s_waitcnt lgkmcnt(8)\n\t"
    "v_mfma_f32_16x16x32_bf16 v[208:211], a[224:227], v[128:131], v[208:211]\n\t"
    "ds_read_b64_tr_b16 a[248:249], v242 offset:45056\n\t"
    "ds_read_b64_tr_b16 a[250:251], v242 offset:47104\n\t"
    "v_mfma_f32_16x16x32_bf16 v[212:215], a[224:227], v[132:135], v[212:215]\n\t"
    "s_waitcnt vmcnt(8)\n\t"
    "s_barrier\n\t"
    "v_mfma_f32_16x16x32_bf16 v[216:219], a[228:231], v[128:131], v[216:219]\n\t"
    "ds_read_b64_tr_b16 a[252:253], v243 offset:45056\n\t"
    "ds_read_b64_tr_b16 a[254:255], v243 offset:47104\n\t"
    "v_mfma_f32_16x16x32_bf16 v[220:223], a[228:231], v[132:135], v[220:223]\n\t"
    "s_add_u32 m0, s63, 0x8000\n\t"
    "s_add_u32 s68, s72, 0x1c200\n\t"
    "s_waitcnt lgkmcnt(8)\n\t"
    "v_mfma_f32_16x16x32_bf16 v[208:211], a[232:235], v[136:139], v[208:211]\n\t"
    "ds_read_b64_tr_b16 a[192:193], v240\n\t"
    "ds_read_b64_tr_b16 a[194:195], v240 offset:2048\n\t"
    "v_mfma_f32_16x16x32_bf16 v[212:215], a[232:235], v[140:143], v[212:215]\n\t"
    "buffer_load_dwordx4 v48, %[srdwo], s68 offen lds\n\t"
    "v_cvt_pk_bf16_f32 v229, v198, v199\n\t"
    "v_mfma_f32_16x16x32_bf16 v[216:219], a[236:239], v[136:139], v[216:219]\n\t"
    "ds_read_b64_tr_b16 a[196:197], v241\n\t"
    "ds_read_b64_tr_b16 a[198:199], v241 offset:2048\n\t"
    "v_mfma_f32_16x16x32_bf16 v[220:223], a[236:239], v[140:143], v[220:223]\n\t"
    "v_cvt_pk_bf16_f32 v230, v204, v205\n\t"
    "s_waitcnt lgkmcnt(8)\n\t"
    "v_mfma_f32_16x16x32_bf16 v[208:211], a[240:243], v[144:147], v[208:211]\n\t"
    "ds_read_b64_tr_b16 a[200:201], v240 offset:4096\n\t"
    "ds_read_b64_tr_b16 a[202:203], v240 offset:6144\n\t"
    "v_mfma_f32_16x16x32_bf16 v[212:215], a[240:243], v[148:151], v[212:215]\n\t"
    "buffer_load_dwordx4 v49, %[srdwo], s68 offen offset:1024 lds\n\t"
    "v_cvt_pk_bf16_f32 v231, v206, v207\n\t"
    "v_mfma_f32_16x16x32_bf16 v[216:219], a[244:247], v[144:147], v[216:219]\n\t"
    "ds_read_b64_tr_b16 a[204:205], v241 offset:4096\n\t"
    "ds_read_b64_tr_b16 a[206:207], v241 offset:6144\n\t"
    "v_mfma_f32_16x16x32_bf16 v[220:223], a[244:247], v[148:151], v[220:223]\n\t"
    "s_movk_i32 s64, 0x2000\n\t"
    "s_waitcnt lgkmcnt(8)\n\t"
    "v_mfma_f32_16x16x32_bf16 v[208:211], a[248:251], v[152:155], v[208:211]\n\t"
    "ds_read_b64_tr_b16 a[208:209], v240 offset:8192\n\t"
    "ds_read_b64_tr_b16 a[210:211], v240 offset:10240\n\t"
    "v_mfma_f32_16x16x32_bf16 v[212:215], a[248:251], v[156:159], v[212:215]\n\t"
    "buffer_load_dwordx4 v50, %[srdwo], s68 offen offset:2048 lds\n\t"
    "buffer_store_dwordx4 v[224:227], v53, %[srdpt], s64 offen sc1\n\t"
    "v_mfma_f32_16x16x32_bf16 v[216:219], a[252:255], v[152:155], v[216:219]\n\t"
    "ds_read_b64_tr_b16 a[212:213], v241 offset:8192\n\t"
    "ds_read_b64_tr_b16 a[214:215], v241 offset:10240\n\t"
    "v_mfma_f32_16x16x32_bf16 v[220:223], a[252:255], v[156:159], v[220:223]\n\t"
    "buffer_store_dwordx4 v[228:231], v53, %[srdpt], s64 offen offset:256 sc1\n\t"
    "s_waitcnt lgkmcnt(8)\n\t"
    "v_mfma_f32_16x16x32_bf16 v[208:211], a[192:195], v[96:99], v[208:211]\n\t"
    "ds_read_b64_tr_b16 a[216:217], v240 offset:12288\n\t"
    "ds_read_b64_tr_b16 a[218:219], v240 offset:14336\n\t"
    "v_mfma_f32_16x16x32_bf16 v[212:215], a[192:195], v[100:103], v[212:215]\n\t"
    "v_mfma_f32_16x16x32_bf16 v[216:219], a[196:199], v[96:99], v[216:219]\n\t"
    "ds_read_b64_tr_b16 a[220:221], v241 offset:12288\n\t"
    "ds_read_b64_tr_b16 a[222:223], v241 offset:14336\n\t"
    "v_mfma_f32_16x16x32_bf16 v[220:223], a[196:199], v[100:103], v[220:223]\n\t"
    "buffer_load_dwordx4 v51, %[srdwo], s68 offen offset:3072 lds\n\t"
    "s_add_u32 m0, s63, 0x9000\n\t"
    "s_waitcnt lgkmcnt(8)\n\t"
    "v_mfma_f32_16x16x32_bf16 v[208:211], a[200:203], v[104:107], v[208:211]\n\t"
    "ds_read_b64_tr_b16 a[224:225], v242\n\t"
    "ds_read_b64_tr_b16 a[226:227], v242 offset:2048\n\t"
    "s_add_u32 s68, s72, 0x23200\n\t"
    "v_mfma_f32_16x16x32_bf16 v[212:215], a[200:203], v[108:111], v[212:215]\n\t"
    "buffer_load_dwordx4 v48, %[srdwo], s68 offen lds\n\t"
    "v_mfma_f32_16x16x32_bf16 v[216:219], a[204:207], v[104:107], v[216:219]\n\t"
    "ds_read_b64_tr_b16 a[228:229], v243\n\t"
    "ds_read_b64_tr_b16 a[230:231], v243 offset:2048\n\t"
    "v_mfma_f32_16x16x32_bf16 v[220:223], a[204:207], v[108:111], v[220:223]\n\t"
    "buffer_load_dwordx4 v49, %[srdwo], s68 offen offset:1024 lds\n\t"
    "s_waitcnt lgkmcnt(8)\n\t"
    "v_mfma_f32_16x16x32_bf16 v[208:211], a[208:211], v[112:115], v[208:211]\n\t"
    "ds_read_b64_tr_b16 a[232:233], v242 offset:4096\n\t"
    "ds_read_b64_tr_b16 a[234:235], v242 offset:6144\n\t"
    "v_mfma_f32_16x16x32_bf16 v[212:215], a[208:211], v[116:119], v[212:215]\n\t"
    "v_mfma_f32_16x16x32_bf16 v[216:219], a[212:215], v[112:115], v[216:219]\n\t"
    "ds_read_b64_tr_b16 a[236:237], v243 offset:4096\n\t"
    "ds_read_b64_tr_b16 a[238:239], v243 offset:6144\n\t"
    "v_mfma_f32_16x16x32_bf16 v[220:223], a[212:215], v[116:119], v[220:223]\n\t"
    "buffer_load_dwordx4 v50, %[srdwo], s68 offen offset:2048 lds\n\t"
    "s_waitcnt lgkmcnt(8)\n\t"
    "v_mfma_f32_16x16x32_bf16 v[208:211], a[216:219], v[120:123], v[208:211]\n\t"
    "ds_read_b64_tr_b16 a[240:241], v242 offset:8192\n\t"
    "ds_read_b64_tr_b16 a[242:243], v242 offset:10240\n\t"
    "v_mfma_f32_16x16x32_bf16 v[212:215], a[216:219], v[124:127], v[212:215]\n\t"
    "buffer_load_dwordx4 v51, %[srdwo], s68 offen offset:3072 lds\n\t"
    "v_mfma_f32_16x16x32_bf16 v[216:219], a[220:223], v[120:123], v[216:219]\n\t"
    "ds_read_b64_tr_b16 a[244:245], v243 offset:8192\n\t"
    "ds_read_b64_tr_b16 a[246:247], v243 offset:10240\n\t"
    "v_mfma_f32_16x16x32_bf16 v[220:223], a[220:223], v[124:127], v[220:223]\n\t"
    "s_waitcnt lgkmcnt(8)\n\t"
    "v_mfma_f32_16x16x32_bf16 v[208:211], a[224:227], v[160:163], v[208:211]\n\t"
    "ds_read_b64_tr_b16 a[248:249], v242 offset:12288\n\t"
    "ds_read_b64_tr_b16 a[250:251], v242 offset:14336\n\t"
    "v_mfma_f32_16x16x32_bf16 v[212:215], a[224:227], v[164:167], v[212:215]\n\t"
    "s_waitcnt vmcnt(10)\n\t"
    "s_barrier\n\t"
    "v_mfma_f32_16x16x32_bf16 v[216:219], a[228:231], v[160:163], v[216:219]\n\t"
    "ds_read_b64_tr_b16 a[252:253], v243 offset:12288\n\t"
    "ds_read_b64_tr_b16 a[254:255], v243 offset:14336\n\t"
    "v_mfma_f32_16x16x32_bf16 v[220:223], a[228:231], v[164:167], v[220:223]\n\t"
    "s_add_u32 m0, s63, 0x18000\n\t"
    "s_add_u32 s68, s72, 0x280\n\t"
    "s_waitcnt lgkmcnt(8)\n\t"
    "v_mfma_f32_16x16x32_bf16 v[208:211], a[232:235], v[168:171], v[208:211]\n\t"
    "ds_read_b64_tr_b16 a[192:193], v44\n\t"
    "ds_read_b64_tr_b16 a[194:195], v44 offset:2048\n\t"
    "v_mfma_f32_16x16x32_bf16 v[212:215], a[232:235], v[172:175], v[212:215]\n\t"
    "buffer_load_dwordx4 v48, %[srdwo], s68 offen lds\n\t"
    "v_mfma_f32_16x16x32_bf16 v[216:219], a[236:239], v[168:171], v[216:219]\n\t"
    "ds_read_b64_tr_b16 a[196:197], v45\n\t"
    "ds_read_b64_tr_b16 a[198:199], v45 offset:2048\n\t"
    "v_mfma_f32_16x16x32_bf16 v[220:223], a[236:239], v[172:175], v[220:223]\n\t"
    "s_waitcnt lgkmcnt(8)\n\t"
    "v_mfma_f32_16x16x32_bf16 v[208:211], a[240:243], v[176:179], v[208:211]\n\t"
    "ds_read_b64_tr_b16 a[200:201], v44 offset:4096\n\t"
    "ds_read_b64_tr_b16 a[202:203], v44 offset:6144\n\t"
    "v_mfma_f32_16x16x32_bf16 v[212:215], a[240:243], v[180:183], v[212:215]\n\t"
    "buffer_load_dwordx4 v49, %[srdwo], s68 offen offset:1024 lds\n\t"
    "v_mfma_f32_16x16x32_bf16 v[216:219], a[244:247], v[176:179], v[216:219]\n\t"
    "ds_read_b64_tr_b16 a[204:205], v45 offset:4096\n\t"
    "ds_read_b64_tr_b16 a[206:207], v45 offset:6144\n\t"
    "v_mfma_f32_16x16x32_bf16 v[220:223], a[244:247], v[180:183], v[220:223]\n\t"
    "s_waitcnt lgkmcnt(8)\n\t"
    "v_mfma_f32_16x16x32_bf16 v[208:211], a[248:251], v[184:187], v[208:211]\n\t"
    "ds_read_b64_tr_b16 a[208:209], v44 offset:8192\n\t"
    "ds_read_b64_tr_b16 a[210:211], v44 offset:10240\n\t"
    "v_mfma_f32_16x16x32_bf16 v[212:215], a[248:251], v[188:191], v[212:215]\n\t"
    "buffer_load_dwordx4 v50, %[srdwo], s68 offen offset:2048 lds\n\t"
    "v_mfma_f32_16x16x32_bf16 v[216:219], a[252:255], v[184:187], v[216:219]\n\t"
    "ds_read_b64_tr_b16 a[212:213], v45 offset:8192\n\t"
    "ds_read_b64_tr_b16 a[214:215], v45 offset:10240\n\t"
    "v_mfma_f32_16x16x32_bf16 v[220:223], a[252:255], v[188:191], v[220:223]\n\t"
    "s_waitcnt lgkmcnt(8)\n\t"
    "v_mfma_f32_16x16x32_bf16 v[192:195], a[192:195], v[64:67], 0\n\t"
    "ds_read_b64_tr_b16 a[216:217], v44 offset:12288\n\t"
    "ds_read_b64_tr_b16 a[218:219], v44 offset:14336\n\t"
    "v_mfma_f32_16x16x32_bf16 v[196:199], a[192:195], v[68:71], 0\n\t"
    "v_mfma_f32_16x16x32_bf16 v[200:203], a[196:199], v[64:67], 0\n\t"
    "ds_read_b64_tr_b16 a[220:221], v45 offset:12288\n\t"
    "ds_read_b64_tr_b16 a[222:223], v45 offset:14336\n\t"
    "v_mfma_f32_16x16x32_bf16 v[204:207], a[196:199], v[68:71], 0\n\t"
    "buffer_load_dwordx4 v51, %[srdwo], s68 offen offset:3072 lds\n\t"
    "s_add_u32 m0, s63, 0x19000\n\t"
    "s_waitcnt lgkmcnt(8)\n\t"
    "v_mfma_f32_16x16x32_bf16 v[192:195], a[200:203], v[72:75], v[192:195]\n\t"
    "ds_read_b64_tr_b16 a[224:225], v46\n\t"
    "ds_read_b64_tr_b16 a[226:227], v46 offset:2048\n\t"
    "s_add_u32 s68, s72, 0x7280\n\t"
    "v_mfma_f32_16x16x32_bf16 v[196:199], a[200:203], v[76:79], v[196:199]\n\t"
    "buffer_load_dwordx4 v48, %[srdwo], s68 offen lds\n\t"
    "v_cvt_pk_bf16_f32 v224, v208, v209\n\t"
    "v_mfma_f32_16x16x32_bf16 v[200:203], a[204:207], v[72:75], v[200:203]\n\t"
    "ds_read_b64_tr_b16 a[228:229], v47\n\t"
    "ds_read_b64_tr_b16 a[230:231], v47 offset:2048\n\t"
    "v_mfma_f32_16x16x32_bf16 v[204:207], a[204:207], v[76:79], v[204:207]\n\t"
    "buffer_load_dwordx4 v49, %[srdwo], s68 offen offset:1024 lds\n\t"
    "s_waitcnt lgkmcnt(8)\n\t"
    "v_mfma_f32_16x16x32_bf16 v[192:195], a[208:211], v[80:83], v[192:195]\n\t"
    "ds_read_b64_tr_b16 a[232:233], v46 offset:4096\n\t"
    "ds_read_b64_tr_b16 a[234:235], v46 offset:6144\n\t"
    "v_mfma_f32_16x16x32_bf16 v[196:199], a[208:211], v[84:87], v[196:199]\n\t"
    "v_cvt_pk_bf16_f32 v225, v210, v211\n\t"
    "v_cvt_pk_bf16_f32 v226, v216, v217\n\t"
    "v_mfma_f32_16x16x32_bf16 v[200:203], a[212:215], v[80:83], v[200:203]\n\t"
    "ds_read_b64_tr_b16 a[236:237], v47 offset:4096\n\t"
    "ds_read_b64_tr_b16 a[238:239], v47 offset:6144\n\t"
    "v_mfma_f32_16x16x32_bf16 v[204:207], a[212:215], v[84:87], v[204:207]\n\t"
    "buffer_load_dwordx4 v50, %[srdwo], s68 offen offset:2048 lds\n\t"
    "s_waitcnt lgkmcnt(8)\n\t"
    "v_mfma_f32_16x16x32_bf16 v[192:195], a[216:219], v[88:91], v[192:195]\n\t"
    "ds_read_b64_tr_b16 a[240:241], v46 offset:8192\n\t"
    "ds_read_b64_tr_b16 a[242:243], v46 offset:10240\n\t"
    "v_mfma_f32_16x16x32_bf16 v[196:199], a[216:219], v[92:95], v[196:199]\n\t"
    "buffer_load_dwordx4 v51, %[srdwo], s68 offen offset:3072 lds\n\t"
    "v_cvt_pk_bf16_f32 v227, v218, v219\n\t"
    "v_mfma_f32_16x16x32_bf16 v[200:203], a[220:223], v[88:91], v[200:203]\n\t"
    "ds_read_b64_tr_b16 a[244:245], v47 offset:8192\n\t"
    "ds_read_b64_tr_b16 a[246:247], v47 offset:10240\n\t"
    "v_mfma_f32_16x16x32_bf16 v[204:207], a[220:223], v[92:95], v[204:207]\n\t"
    "v_cvt_pk_bf16_f32 v228, v212, v213\n\t"
    "s_waitcnt lgkmcnt(8)\n\t"
    "v_mfma_f32_16x16x32_bf16 v[192:195], a[224:227], v[128:131], v[192:195]\n\t"
    "ds_read_b64_tr_b16 a[248:249], v46 offset:12288\n\t"
    "ds_read_b64_tr_b16 a[250:251], v46 offset:14336\n\t"
    "v_mfma_f32_16x16x32_bf16 v[196:199], a[224:227], v[132:135], v[196:199]\n\t"
    "s_waitcnt vmcnt(8)\n\t"
    "s_barrier\n\t"
    "v_mfma_f32_16x16x32_bf16 v[200:203], a[228:231], v[128:131], v[200:203]\n\t"
    "ds_read_b64_tr_b16 a[252:253], v47 offset:12288\n\t"
    "ds_read_b64_tr_b16 a[254:255], v47 offset:14336\n\t"
    "v_mfma_f32_16x16x32_bf16 v[204:207], a[228:231], v[132:135], v[204:207]\n\t"
    "s_add_u32 m0, s63, 0x10000\n\t"
    "s_add_u32 s68, s72, 0x1c280\n\t"
    "s_waitcnt lgkmcnt(8)\n\t"
    "v_mfma_f32_16x16x32_bf16 v[192:195], a[232:235], v[136:139], v[192:195]\n\t"
    "ds_read_b64_tr_b16 a[192:193], v44 offset:32768\n\t"
    "ds_read_b64_tr_b16 a[194:195], v44 offset:34816\n\t"
    "v_mfma_f32_16x16x32_bf16 v[196:199], a[232:235], v[140:143], v[196:199]\n\t"
    "buffer_load_dwordx4 v48, %[srdwo], s68 offen lds\n\t"
    "v_cvt_pk_bf16_f32 v229, v214, v215\n\t"
    "v_mfma_f32_16x16x32_bf16 v[200:203], a[236:239], v[136:139], v[200:203]\n\t"
    "ds_read_b64_tr_b16 a[196:197], v45 offset:32768\n\t"
    "ds_read_b64_tr_b16 a[198:199], v45 offset:34816\n\t"
    "v_mfma_f32_16x16x32_bf16 v[204:207], a[236:239], v[140:143], v[204:207]\n\t"
    "v_cvt_pk_bf16_f32 v230, v220, v221\n\t"
    "s_waitcnt lgkmcnt(8)\n\t"
    "v_mfma_f32_16x16x32_bf16 v[192:195], a[240:243], v[144:147], v[192:195]\n\t"
    "ds_read_b64_tr_b16 a[200:201], v44 offset:36864\n\t"
    "ds_read_b64_tr_b16 a[202:203], v44 offset:38912\n\t"
    "v_mfma_f32_16x16x32_bf16 v[196:199], a[240:243], v[148:151], v[196:199]\n\t"
    "buffer_load_dwordx4 v49, %[srdwo], s68 offen offset:1024 lds\n\t"
    "v_cvt_pk_bf16_f32 v231, v222, v223\n\t"
    "v_mfma_f32_16x16x32_bf16 v[200:203], a[244:247], v[144:147], v[200:203]\n\t"
    "ds_read_b64_tr_b16 a[204:205], v45 offset:36864\n\t"
    "ds_read_b64_tr_b16 a[206:207], v45 offset:38912\n\t"
    "v_mfma_f32_16x16x32_bf16 v[204:207], a[244:247], v[148:151], v[204:207]\n\t"
    "s_movk_i32 s64, 0x3000\n\t"
    "s_waitcnt lgkmcnt(8)\n\t"
    "v_mfma_f32_16x16x32_bf16 v[192:195], a[248:251], v[152:155], v[192:195]\n\t"
    "ds_read_b64_tr_b16 a[208:209], v44 offset:40960\n\t"
    "ds_read_b64_tr_b16 a[210:211], v44 offset:43008\n\t"
    "v_mfma_f32_16x16x32_bf16 v[196:199], a[248:251], v[156:159], v[196:199]\n\t"
    "buffer_load_dwordx4 v50, %[srdwo], s68 offen offset:2048 lds\n\t"
    "buffer_store_dwordx4 v[224:227], v53, %[srdpt], s64 offen sc1\n\t"
    "v_mfma_f32_16x16x32_bf16 v[200:203], a[252:255], v[152:155], v[200:203]\n\t"
    "ds_read_b64_tr_b16 a[212:213], v45 offset:40960\n\t"
    "ds_read_b64_tr_b16 a[214:215], v45 offset:43008\n\t"
    "v_mfma_f32_16x16x32_bf16 v[204:207], a[252:255], v[156:159], v[204:207]\n\t"
    "buffer_store_dwordx4 v[228:231], v53, %[srdpt], s64 offen offset:256 sc1\n\t"
    "s_waitcnt lgkmcnt(8)\n\t"
    "v_mfma_f32_16x16x32_bf16 v[192:195], a[192:195], v[96:99], v[192:195]\n\t"
    "ds_read_b64_tr_b16 a[216:217], v44 offset:45056\n\t"
    "ds_read_b64_tr_b16 a[218:219], v44 offset:47104\n\t"
    "v_mfma_f32_16x16x32_bf16 v[196:199], a[192:195], v[100:103], v[196:199]\n\t"
    "v_mfma_f32_16x16x32_bf16 v[200:203], a[196:199], v[96:99], v[200:203]\n\t"
    "ds_read_b64_tr_b16 a[220:221], v45 offset:45056\n\t"
    "ds_read_b64_tr_b16 a[222:223], v45 offset:47104\n\t"
    "v_mfma_f32_16x16x32_bf16 v[204:207], a[196:199], v[100:103], v[204:207]\n\t"
    "buffer_load_dwordx4 v51, %[srdwo], s68 offen offset:3072 lds\n\t"
    "s_add_u32 m0, s63, 0x11000\n\t"
    "s_waitcnt lgkmcnt(8)\n\t"
    "v_mfma_f32_16x16x32_bf16 v[192:195], a[200:203], v[104:107], v[192:195]\n\t"
    "ds_read_b64_tr_b16 a[224:225], v46 offset:32768\n\t"
    "ds_read_b64_tr_b16 a[226:227], v46 offset:34816\n\t"
    "s_add_u32 s68, s72, 0x23280\n\t"
    "v_mfma_f32_16x16x32_bf16 v[196:199], a[200:203], v[108:111], v[196:199]\n\t"
    "buffer_load_dwordx4 v48, %[srdwo], s68 offen lds\n\t"
    "v_mfma_f32_16x16x32_bf16 v[200:203], a[204:207], v[104:107], v[200:203]\n\t"
    "ds_read_b64_tr_b16 a[228:229], v47 offset:32768\n\t"
    "ds_read_b64_tr_b16 a[230:231], v47 offset:34816\n\t"
    "v_mfma_f32_16x16x32_bf16 v[204:207], a[204:207], v[108:111], v[204:207]\n\t"
    "buffer_load_dwordx4 v49, %[srdwo], s68 offen offset:1024 lds\n\t"
    "s_waitcnt lgkmcnt(8)\n\t"
    "v_mfma_f32_16x16x32_bf16 v[192:195], a[208:211], v[112:115], v[192:195]\n\t"
    "ds_read_b64_tr_b16 a[232:233], v46 offset:36864\n\t"
    "ds_read_b64_tr_b16 a[234:235], v46 offset:38912\n\t"
    "v_mfma_f32_16x16x32_bf16 v[196:199], a[208:211], v[116:119], v[196:199]\n\t"
    "v_mfma_f32_16x16x32_bf16 v[200:203], a[212:215], v[112:115], v[200:203]\n\t"
    "ds_read_b64_tr_b16 a[236:237], v47 offset:36864\n\t"
    "ds_read_b64_tr_b16 a[238:239], v47 offset:38912\n\t"
    "v_mfma_f32_16x16x32_bf16 v[204:207], a[212:215], v[116:119], v[204:207]\n\t"
    "buffer_load_dwordx4 v50, %[srdwo], s68 offen offset:2048 lds\n\t"
    "s_waitcnt lgkmcnt(8)\n\t"
    "v_mfma_f32_16x16x32_bf16 v[192:195], a[216:219], v[120:123], v[192:195]\n\t"
    "ds_read_b64_tr_b16 a[240:241], v46 offset:40960\n\t"
    "ds_read_b64_tr_b16 a[242:243], v46 offset:43008\n\t"
    "v_mfma_f32_16x16x32_bf16 v[196:199], a[216:219], v[124:127], v[196:199]\n\t"
    "buffer_load_dwordx4 v51, %[srdwo], s68 offen offset:3072 lds\n\t"
    "v_mfma_f32_16x16x32_bf16 v[200:203], a[220:223], v[120:123], v[200:203]\n\t"
    "ds_read_b64_tr_b16 a[244:245], v47 offset:40960\n\t"
    "ds_read_b64_tr_b16 a[246:247], v47 offset:43008\n\t"
    "v_mfma_f32_16x16x32_bf16 v[204:207], a[220:223], v[124:127], v[204:207]\n\t"
    "s_waitcnt lgkmcnt(8)\n\t"
    "v_mfma_f32_16x16x32_bf16 v[192:195], a[224:227], v[160:163], v[192:195]\n\t"
    "ds_read_b64_tr_b16 a[248:249], v46 offset:45056\n\t"
    "ds_read_b64_tr_b16 a[250:251], v46 offset:47104\n\t"
    "v_mfma_f32_16x16x32_bf16 v[196:199], a[224:227], v[164:167], v[196:199]\n\t"
    "s_waitcnt vmcnt(10)\n\t"
    "s_barrier\n\t"
    "v_mfma_f32_16x16x32_bf16 v[200:203], a[228:231], v[160:163], v[200:203]\n\t"
    "ds_read_b64_tr_b16 a[252:253], v47 offset:45056\n\t"
    "ds_read_b64_tr_b16 a[254:255], v47 offset:47104\n\t"
    "v_mfma_f32_16x16x32_bf16 v[204:207], a[228:231], v[164:167], v[204:207]\n\t"
    "s_add_u32 m0, s63, 0x0\n\t"
    "s_add_u32 s68, s72, 0x300\n\t"
    "s_waitcnt lgkmcnt(8)\n\t"
    "v_mfma_f32_16x16x32_bf16 v[192:195], a[232:235], v[168:171], v[192:195]\n\t"
    "ds_read_b64_tr_b16 a[192:193], v240 offset:32768\n\t"
    "ds_read_b64_tr_b16 a[194:195], v240 offset:34816\n\t"
    "v_mfma_f32_16x16x32_bf16 v[196:199], a[232:235], v[172:175], v[196:199]\n\t"
    "buffer_load_dwordx4 v48, %[srdwo], s68 offen lds\n\t"
    "v_mfma_f32_16x16x32_bf16 v[200:203], a[236:239], v[168:171], v[200:203]\n\t"
    "ds_read_b64_tr_b16 a[196:197], v241 offset:32768\n\t"
    "ds_read_b64_tr_b16 a[198:199], v241 offset:34816\n\t"
    "v_mfma_f32_16x16x32_bf16 v[204:207], a[236:239], v[172:175], v[204:207]\n\t"
    "s_waitcnt lgkmcnt(8)\n\t"
    "v_mfma_f32_16x16x32_bf16 v[192:195], a[240:243], v[176:179], v[192:195]\n\t"
    "ds_read_b64_tr_b16 a[200:201], v240 offset:36864\n\t"
    "ds_read_b64_tr_b16 a[202:203], v240 offset:38912\n\t"
    "v_mfma_f32_16x16x32_bf16 v[196:199], a[240:243], v[180:183], v[196:199]\n\t"
    "buffer_load_dwordx4 v49, %[srdwo], s68 offen offset:1024 lds\n\t"
    "v_mfma_f32_16x16x32_bf16 v[200:203], a[244:247], v[176:179], v[200:203]\n\t"
    "ds_read_b64_tr_b16 a[204:205], v241 offset:36864\n\t"
    "ds_read_b64_tr_b16 a[206:207], v241 offset:38912\n\t"
    "v_mfma_f32_16x16x32_bf16 v[204:207], a[244:247], v[180:183], v[204:207]\n\t"
    "s_waitcnt lgkmcnt(8)\n\t"
    "v_mfma_f32_16x16x32_bf16 v[192:195], a[248:251], v[184:187], v[192:195]\n\t"
    "ds_read_b64_tr_b16 a[208:209], v240 offset:40960\n\t"
    "ds_read_b64_tr_b16 a[210:211], v240 offset:43008\n\t"
    "v_mfma_f32_16x16x32_bf16 v[196:199], a[248:251], v[188:191], v[196:199]\n\t"
    "buffer_load_dwordx4 v50, %[srdwo], s68 offen offset:2048 lds\n\t"
    "v_mfma_f32_16x16x32_bf16 v[200:203], a[252:255], v[184:187], v[200:203]\n\t"
    "ds_read_b64_tr_b16 a[212:213], v241 offset:40960\n\t"
    "ds_read_b64_tr_b16 a[214:215], v241 offset:43008\n\t"
    "v_mfma_f32_16x16x32_bf16 v[204:207], a[252:255], v[188:191], v[204:207]\n\t"
    "s_waitcnt lgkmcnt(8)\n\t"
    "v_mfma_f32_16x16x32_bf16 v[208:211], a[192:195], v[64:67], 0\n\t"
    "ds_read_b64_tr_b16 a[216:217], v240 offset:45056\n\t"
    "ds_read_b64_tr_b16 a[218:219], v240 offset:47104\n\t"
    "v_mfma_f32_16x16x32_bf16 v[212:215], a[192:195], v[68:71], 0\n\t"
    "v_mfma_f32_16x16x32_bf16 v[216:219], a[196:199], v[64:67], 0\n\t"
    "ds_read_b64_tr_b16 a[220:221], v241 offset:45056\n\t"
    "ds_read_b64_tr_b16 a[222:223], v241 offset:47104\n\t"
    "v_mfma_f32_16x16x32_bf16 v[220:223], a[196:199], v[68:71], 0\n\t"
    "buffer_load_dwordx4 v51, %[srdwo], s68 offen offset:3072 lds\n\t"
    "s_add_u32 m0, s63, 0x1000\n\t"
    "s_waitcnt lgkmcnt(8)\n\t"
    "v_mfma_f32_16x16x32_bf16 v[208:211], a[200:203], v[72:75], v[208:211]\n\t"
    "ds_read_b64_tr_b16 a[224:225], v242 offset:32768\n\t"
    "ds_read_b64_tr_b16 a[226:227], v242 offset:34816\n\t"
    "s_add_u32 s68, s72, 0x7300\n\t"
    "v_mfma_f32_16x16x32_bf16 v[212:215], a[200:203], v[76:79], v[212:215]\n\t"
    "buffer_load_dwordx4 v48, %[srdwo], s68 offen lds\n\t"
    "v_cvt_pk_bf16_f32 v224, v192, v193\n\t"
    "v_mfma_f32_16x16x32_bf16 v[216:219], a[204:207], v[72:75], v[216:219]\n\t"
    "ds_read_b64_tr_b16 a[228:229], v243 offset:32768\n\t"
    "ds_read_b64_tr_b16 a[230:231], v243 offset:34816\n\t"
    "v_mfma_f32_16x16x32_bf16 v[220:223], a[204:207], v[76:79], v[220:223]\n\t"
    "buffer_load_dwordx4 v49, %[srdwo], s68 offen offset:1024 lds\n\t"
    "s_waitcnt lgkmcnt(8)\n\t"
    "v_mfma_f32_16x16x32_bf16 v[208:211], a[208:211], v[80:83], v[208:211]\n\t"
    "ds_read_b64_tr_b16 a[232:233], v242 offset:36864\n\t"
    "ds_read_b64_tr_b16 a[234:235], v242 offset:38912\n\t"
    "v_mfma_f32_16x16x32_bf16 v[212:215], a[208:211], v[84:87], v[212:215]\n\t"
    "v_cvt_pk_bf16_f32 v225, v194, v195\n\t"
    "v_cvt_pk_bf16_f32 v226, v200, v201\n\t"
    "v_mfma_f32_16x16x32_bf16 v[216:219], a[212:215], v[80:83], v[216:219]\n\t"
    "ds_read_b64_tr_b16 a[236:237], v243 offset:36864\n\t"
    "ds_read_b64_tr_b16 a[238:239], v243 offset:38912\n\t"
    "v_mfma_f32_16x16x32_bf16 v[220:223], a[212:215], v[84:87], v[220:223]\n\t"
    "buffer_load_dwordx4 v50, %[srdwo], s68 offen offset:2048 lds\n\t"
    "s_waitcnt lgkmcnt(8)\n\t"
    "v_mfma_f32_16x16x32_bf16 v[208:211], a[216:219], v[88:91], v[208:211]\n\t"
    "ds_read_b64_tr_b16 a[240:241], v242 offset:40960\n\t"
    "ds_read_b64_tr_b16 a[242:243], v242 offset:43008\n\t"
    "v_mfma_f32_16x16x32_bf16 v[212:215], a[216:219], v[92:95], v[212:215]\n\t"
    "buffer_load_dwordx4 v51, %[srdwo], s68 offen offset:3072 lds\n\t"
    "v_cvt_pk_bf16_f32 v227, v202, v203\n\t"
    "v_mfma_f32_16x16x32_bf16 v[216:219], a[220:223], v[88:91], v[216:219]\n\t"
    "ds_read_b64_tr_b16 a[244:245], v243 offset:40960\n\t"
    "ds_read_b64_tr_b16 a[246:247], v243 offset:43008\n\t"
    "v_mfma_f32_16x16x32_bf16 v[220:223], a[220:223], v[92:95], v[220:223]\n\t"
    "v_cvt_pk_bf16_f32 v228, v196, v197\n\t"
    "s_waitcnt lgkmcnt(8)\n\t"
    "v_mfma_f32_16x16x32_bf16 v[208:211], a[224:227], v[128:131], v[208:211]\n\t"
    "ds_read_b64_tr_b16 a[248:249], v242 offset:45056\n\t"
    "ds_read_b64_tr_b16 a[250:251], v242 offset:47104\n\t"
    "v_mfma_f32_16x16x32_bf16 v[212:215], a[224:227], v[132:135], v[212:215]\n\t"
    "s_waitcnt vmcnt(8)\n\t"
    "s_barrier\n\t"
    "v_mfma_f32_16x16x32_bf16 v[216:219], a[228:231], v[128:131], v[216:219]\n\t"
    "ds_read_b64_tr_b16 a[252:253], v243 offset:45056\n\t"
    "ds_read_b64_tr_b16 a[254:255], v243 offset:47104\n\t"
    "v_mfma_f32_16x16x32_bf16 v[220:223], a[228:231], v[132:135], v[220:223]\n\t"
    "s_add_u32 m0, s63, 0x8000\n\t"
    "s_add_u32 s68, s72, 0x1c300\n\t"
    "s_waitcnt lgkmcnt(8)\n\t"
    "v_mfma_f32_16x16x32_bf16 v[208:211], a[232:235], v[136:139], v[208:211]\n\t"
    "ds_read_b64_tr_b16 a[192:193], v240\n\t"
    "ds_read_b64_tr_b16 a[194:195], v240 offset:2048\n\t"
    "v_mfma_f32_16x16x32_bf16 v[212:215], a[232:235], v[140:143], v[212:215]\n\t"
    "buffer_load_dwordx4 v48, %[srdwo], s68 offen lds\n\t"
    "v_cvt_pk_bf16_f32 v229, v198, v199\n\t"
    "v_mfma_f32_16x16x32_bf16 v[216:219], a[236:239], v[136:139], v[216:219]\n\t"
    "ds_read_b64_tr_b16 a[196:197], v241\n\t"
    "ds_read_b64_tr_b16 a[198:199], v241 offset:2048\n\t"
    "v_mfma_f32_16x16x32_bf16 v[220:223], a[236:239], v[140:143], v[220:223]\n\t"
    "v_cvt_pk_bf16_f32 v230, v204, v205\n\t"
    "s_waitcnt lgkmcnt(8)\n\t"
    "v_mfma_f32_16x16x32_bf16 v[208:211], a[240:243], v[144:147], v[208:211]\n\t"
    "ds_read_b64_tr_b16 a[200:201], v240 offset:4096\n\t"
    "ds_read_b64_tr_b16 a[202:203], v240 offset:6144\n\t"
    "v_mfma_f32_16x16x32_bf16 v[212:215], a[240:243], v[148:151], v[212:215]\n\t"
    "buffer_load_dwordx4 v49, %[srdwo], s68 offen offset:1024 lds\n\t"
    "v_cvt_pk_bf16_f32 v231, v206, v207\n\t"
    "v_mfma_f32_16x16x32_bf16 v[216:219], a[244:247], v[144:147], v[216:219]\n\t"
    "ds_read_b64_tr_b16 a[204:205], v241 offset:4096\n\t"
    "ds_read_b64_tr_b16 a[206:207], v241 offset:6144\n\t"
    "v_mfma_f32_16x16x32_bf16 v[220:223], a[244:247], v[148:151], v[220:223]\n\t"
    "s_movk_i32 s64, 0x4000\n\t"
    "s_waitcnt lgkmcnt(8)\n\t"
    "v_mfma_f32_16x16x32_bf16 v[208:211], a[248:251], v[152:155], v[208:211]\n\t"
    "ds_read_b64_tr_b16 a[208:209], v240 offset:8192\n\t"
    "ds_read_b64_tr_b16 a[210:211], v240 offset:10240\n\t"
    "v_mfma_f32_16x16x32_bf16 v[212:215], a[248:251], v[156:159], v[212:215]\n\t"
    "buffer_load_dwordx4 v50, %[srdwo], s68 offen offset:2048 lds\n\t"
    "buffer_store_dwordx4 v[224:227], v53, %[srdpt], s64 offen sc1\n\t"
    "v_mfma_f32_16x16x32_bf16 v[216:219], a[252:255], v[152:155], v[216:219]\n\t"
    "ds_read_b64_tr_b16 a[212:213], v241 offset:8192\n\t"
    "ds_read_b64_tr_b16 a[214:215], v241 offset:10240\n\t"
    "v_mfma_f32_16x16x32_bf16 v[220:223], a[252:255], v[156:159], v[220:223]\n\t"
    "buffer_store_dwordx4 v[228:231], v53, %[srdpt], s64 offen offset:256 sc1\n\t"
    "s_waitcnt lgkmcnt(8)\n\t"
    "v_mfma_f32_16x16x32_bf16 v[208:211], a[192:195], v[96:99], v[208:211]\n\t"
    "ds_read_b64_tr_b16 a[216:217], v240 offset:12288\n\t"
    "ds_read_b64_tr_b16 a[218:219], v240 offset:14336\n\t"
    "v_mfma_f32_16x16x32_bf16 v[212:215], a[192:195], v[100:103], v[212:215]\n\t"
    "v_mfma_f32_16x16x32_bf16 v[216:219], a[196:199], v[96:99], v[216:219]\n\t"
    "ds_read_b64_tr_b16 a[220:221], v241 offset:12288\n\t"
    "ds_read_b64_tr_b16 a[222:223], v241 offset:14336\n\t"
    "v_mfma_f32_16x16x32_bf16 v[220:223], a[196:199], v[100:103], v[220:223]\n\t"
    "buffer_load_dwordx4 v51, %[srdwo], s68 offen offset:3072 lds\n\t"
    "s_add_u32 m0, s63, 0x9000\n\t"
    "s_waitcnt lgkmcnt(8)\n\t"
    "v_mfma_f32_16x16x32_bf16 v[208:211], a[200:203], v[104:107], v[208:211]\n\t"
    "ds_read_b64_tr_b16 a[224:225], v242\n\t"
    "ds_read_b64_tr_b16 a[226:227], v242 offset:2048\n\t"
    "s_add_u32 s68, s72, 0x23300\n\t"
    "v_mfma_f32_16x16x32_bf16 v[212:215], a[200:203], v[108:111], v[212:215]\n\t"
    "buffer_load_dwordx4 v48, %[srdwo], s68 offen lds\n\t"
    "v_mfma_f32_16x16x32_bf16 v[216:219], a[204:207], v[104:107], v[216:219]\n\t"
    "ds_read_b64_tr_b16 a[228:229], v243\n\t"
    "ds_read_b64_tr_b16 a[230:231], v243 offset:2048\n\t"
    "v_mfma_f32_16x16x32_bf16 v[220:223], a[204:207], v[108:111], v[220:223]\n\t"
    "buffer_load_dwordx4 v49, %[srdwo], s68 offen offset:1024 lds\n\t"
    "s_waitcnt lgkmcnt(8)\n\t"
    "v_mfma_f32_16x16x32_bf16 v[208:211], a[208:211], v[112:115], v[208:211]\n\t"
    "ds_read_b64_tr_b16 a[232:233], v242 offset:4096\n\t"
    "ds_read_b64_tr_b16 a[234:235], v242 offset:6144\n\t"
    "v_mfma_f32_16x16x32_bf16 v[212:215], a[208:211], v[116:119], v[212:215]\n\t"
    "v_mfma_f32_16x16x32_bf16 v[216:219], a[212:215], v[112:115], v[216:219]\n\t"
    "ds_read_b64_tr_b16 a[236:237], v243 offset:4096\n\t"
    "ds_read_b64_tr_b16 a[238:239], v243 offset:6144\n\t"
    "v_mfma_f32_16x16x32_bf16 v[220:223], a[212:215], v[116:119], v[220:223]\n\t"
    "buffer_load_dwordx4 v50, %[srdwo], s68 offen offset:2048 lds\n\t"
    "s_waitcnt lgkmcnt(8)\n\t"
    "v_mfma_f32_16x16x32_bf16 v[208:211], a[216:219], v[120:123], v[208:211]\n\t"
    "ds_read_b64_tr_b16 a[240:241], v242 offset:8192\n\t"
    "ds_read_b64_tr_b16 a[242:243], v242 offset:10240\n\t"
    "v_mfma_f32_16x16x32_bf16 v[212:215], a[216:219], v[124:127], v[212:215]\n\t"
    "buffer_load_dwordx4 v51, %[srdwo], s68 offen offset:3072 lds\n\t"
    "v_mfma_f32_16x16x32_bf16 v[216:219], a[220:223], v[120:123], v[216:219]\n\t"
    "ds_read_b64_tr_b16 a[244:245], v243 offset:8192\n\t"
    "ds_read_b64_tr_b16 a[246:247], v243 offset:10240\n\t"
    "v_mfma_f32_16x16x32_bf16 v[220:223], a[220:223], v[124:127], v[220:223]\n\t"
    "s_waitcnt lgkmcnt(8)\n\t"
    "v_mfma_f32_16x16x32_bf16 v[208:211], a[224:227], v[160:163], v[208:211]\n\t"
    "ds_read_b64_tr_b16 a[248:249], v242 offset:12288\n\t"
    "ds_read_b64_tr_b16 a[250:251], v242 offset:14336\n\t"
    "v_mfma_f32_16x16x32_bf16 v[212:215], a[224:227], v[164:167], v[212:215]\n\t"
    "s_waitcnt vmcnt(10)\n\t"
    "s_barrier\n\t"
    "v_mfma_f32_16x16x32_bf16 v[216:219], a[228:231], v[160:163], v[216:219]\n\t"
    "ds_read_b64_tr_b16 a[252:253], v243 offset:12288\n\t"
    "ds_read_b64_tr_b16 a[254:255], v243 offset:14336\n\t"
    "v_mfma_f32_16x16x32_bf16 v[220:223], a[228:231], v[164:167], v[220:223]\n\t"
    "s_add_u32 m0, s61, 0x20000\n\t"
    "s_add_u32 s68, s62, 0x0\n\t"
    "s_waitcnt lgkmcnt(8)\n\t"
    "v_mfma_f32_16x16x32_bf16 v[208:211], a[232:235], v[168:171], v[208:211]\n\t"
    "ds_read_b64_tr_b16 a[192:193], v44\n\t"
    "ds_read_b64_tr_b16 a[194:195], v44 offset:2048\n\t"
    "v_mfma_f32_16x16x32_bf16 v[212:215], a[232:235], v[172:175], v[212:215]\n\t"
    "buffer_load_dwordx4 v2, %[srdw1], s68 offen lds\n\t"
    "v_mfma_f32_16x16x32_bf16 v[216:219], a[236:239], v[168:171], v[216:219]\n\t"
    "ds_read_b64_tr_b16 a[196:197], v45\n\t"
    "ds_read_b64_tr_b16 a[198:199], v45 offset:2048\n\t"
    "v_mfma_f32_16x16x32_bf16 v[220:223], a[236:239], v[172:175], v[220:223]\n\t"
    "s_waitcnt lgkmcnt(8)\n\t"
    "v_mfma_f32_16x16x32_bf16 v[208:211], a[240:243], v[176:179], v[208:211]\n\t"
    "ds_read_b64_tr_b16 a[200:201], v44 offset:4096\n\t"
    "ds_read_b64_tr_b16 a[202:203], v44 offset:6144\n\t"
    "v_mfma_f32_16x16x32_bf16 v[212:215], a[240:243], v[180:183], v[212:215]\n\t"
    "buffer_load_dwordx4 v3, %[srdw1], s68 offen offset:1024 lds\n\t"
    "v_mfma_f32_16x16x32_bf16 v[216:219], a[244:247], v[176:179], v[216:219]\n\t"
    "ds_read_b64_tr_b16 a[204:205], v45 offset:4096\n\t"
    "ds_read_b64_tr_b16 a[206:207], v45 offset:6144\n\t"
    "v_mfma_f32_16x16x32_bf16 v[220:223], a[244:247], v[180:183], v[220:223]\n\t"
    "s_waitcnt lgkmcnt(8)\n\t"
    "v_mfma_f32_16x16x32_bf16 v[208:211], a[248:251], v[184:187], v[208:211]\n\t"
    "ds_read_b64_tr_b16 a[208:209], v44 offset:8192\n\t"
    "ds_read_b64_tr_b16 a[210:211], v44 offset:10240\n\t"
    "v_mfma_f32_16x16x32_bf16 v[212:215], a[248:251], v[188:191], v[212:215]\n\t"
    "buffer_load_dwordx4 v4, %[srdw1], s68 offen offset:2048 lds\n\t"
    "v_mfma_f32_16x16x32_bf16 v[216:219], a[252:255], v[184:187], v[216:219]\n\t"
    "ds_read_b64_tr_b16 a[212:213], v45 offset:8192\n\t"
    "ds_read_b64_tr_b16 a[214:215], v45 offset:10240\n\t"
    "v_mfma_f32_16x16x32_bf16 v[220:223], a[252:255], v[188:191], v[220:223]\n\t"
    "s_waitcnt lgkmcnt(8)\n\t"
    "v_mfma_f32_16x16x32_bf16 v[192:195], a[192:195], v[64:67], 0\n\t"
    "ds_read_b64_tr_b16 a[216:217], v44 offset:12288\n\t"
    "ds_read_b64_tr_b16 a[218:219], v44 offset:14336\n\t"
    "v_mfma_f32_16x16x32_bf16 v[196:199], a[192:195], v[68:71], 0\n\t"
    "v_mfma_f32_16x16x32_bf16 v[200:203], a[196:199], v[64:67], 0\n\t"
    "ds_read_b64_tr_b16 a[220:221], v45 offset:12288\n\t"
    "ds_read_b64_tr_b16 a[222:223], v45 offset:14336\n\t"
    "v_mfma_f32_16x16x32_bf16 v[204:207], a[196:199], v[68:71], 0\n\t"
    "buffer_load_dwordx4 v5, %[srdw1], s68 offen offset:3072 lds\n\t"
    "s_add_u32 m0, s61, 0x21000\n\t"
    "s_waitcnt lgkmcnt(8)\n\t"
    "v_mfma_f32_16x16x32_bf16 v[192:195], a[200:203], v[72:75], v[192:195]\n\t"
    "ds_read_b64_tr_b16 a[224:225], v46\n\t"
    "ds_read_b64_tr_b16 a[226:227], v46 offset:2048\n\t"
    "v_mfma_f32_16x16x32_bf16 v[196:199], a[200:203], v[76:79], v[196:199]\n\t"
    "buffer_load_dwordx4 v61, %[srdw1], s68 offen lds\n\t"
    "v_cvt_pk_bf16_f32 v224, v208, v209\n\t"
    "v_mfma_f32_16x16x32_bf16 v[200:203], a[204:207], v[72:75], v[200:203]\n\t"
    "ds_read_b64_tr_b16 a[228:229], v47\n\t"
    "ds_read_b64_tr_b16 a[230:231], v47 offset:2048\n\t"
    "v_mfma_f32_16x16x32_bf16 v[204:207], a[204:207], v[76:79], v[204:207]\n\t"
    "v_cvt_pk_bf16_f32 v225, v210, v211\n\t"
    "s_waitcnt lgkmcnt(8)\n\t"
    "v_mfma_f32_16x16x32_bf16 v[192:195], a[208:211], v[80:83], v[192:195]\n\t"
    "ds_read_b64_tr_b16 a[232:233], v46 offset:4096\n\t"
    "ds_read_b64_tr_b16 a[234:235], v46 offset:6144\n\t"
    "v_mfma_f32_16x16x32_bf16 v[196:199], a[208:211], v[84:87], v[196:199]\n\t"
    "buffer_load_dwordx4 v62, %[srdw1], s68 offen offset:1024 lds\n\t"
    "v_cvt_pk_bf16_f32 v226, v216, v217\n\t"
    "v_mfma_f32_16x16x32_bf16 v[200:203], a[212:215], v[80:83], v[200:203]\n\t"
    "ds_read_b64_tr_b16 a[236:237], v47 offset:4096\n\t"
    "ds_read_b64_tr_b16 a[238:239], v47 offset:6144\n\t"
    "v_mfma_f32_16x16x32_bf16 v[204:207], a[212:215], v[84:87], v[204:207]\n\t"
    "v_cvt_pk_bf16_f32 v227, v218, v219\n\t"
    "s_waitcnt lgkmcnt(8)\n\t"
    "v_mfma_f32_16x16x32_bf16 v[192:195], a[216:219], v[88:91], v[192:195]\n\t"
    "ds_read_b64_tr_b16 a[240:241], v46 offset:8192\n\t"
    "ds_read_b64_tr_b16 a[242:243], v46 offset:10240\n\t"
    "v_mfma_f32_16x16x32_bf16 v[196:199], a[216:219], v[92:95], v[196:199]\n\t"
    "buffer_load_dwordx4 v63, %[srdw1], s68 offen offset:2048 lds\n\t"
    "v_cvt_pk_bf16_f32 v228, v212, v213\n\t"
    "v_mfma_f32_16x16x32_bf16 v[200:203], a[220:223], v[88:91], v[200:203]\n\t"
    "ds_read_b64_tr_b16 a[244:245], v47 offset:8192\n\t"
    "ds_read_b64_tr_b16 a[246:247], v47 offset:10240\n\t"
    "v_mfma_f32_16x16x32_bf16 v[204:207], a[220:223], v[92:95], v[204:207]\n\t"
    "v_cvt_pk_bf16_f32 v229, v214, v215\n\t"
    "s_waitcnt lgkmcnt(8)\n\t"
    "v_mfma_f32_16x16x32_bf16 v[192:195], a[224:227], v[128:131], v[192:195]\n\t"
    "ds_read_b64_tr_b16 a[248:249], v46 offset:12288\n\t"
    "ds_read_b64_tr_b16 a[250:251], v46 offset:14336\n\t"
    "v_mfma_f32_16x16x32_bf16 v[196:199], a[224:227], v[132:135], v[196:199]\n\t"
    "s_waitcnt vmcnt(7)\n\t"
    "s_barrier\n\t"
    "v_mfma_f32_16x16x32_bf16 v[200:203], a[228:231], v[128:131], v[200:203]\n\t"
    "ds_read_b64_tr_b16 a[252:253], v47 offset:12288\n\t"
    "ds_read_b64_tr_b16 a[254:255], v47 offset:14336\n\t"
    "v_mfma_f32_16x16x32_bf16 v[204:207], a[228:231], v[132:135], v[204:207]\n\t"
    "s_add_u32 m0, s61, 0x18000\n\t"
    "s_add_u32 s68, s62, 0x38000\n\t"
    "s_waitcnt lgkmcnt(8)\n\t"
    "v_mfma_f32_16x16x32_bf16 v[192:195], a[232:235], v[136:139], v[192:195]\n\t"
    "ds_read_b64_tr_b16 a[192:193], v44 offset:32768\n\t"
    "ds_read_b64_tr_b16 a[194:195], v44 offset:34816\n\t"
    "v_mfma_f32_16x16x32_bf16 v[196:199], a[232:235], v[140:143], v[196:199]\n\t"
    "buffer_load_dwordx4 v2, %[srdw1], s68 offen lds\n\t"
    "v_cvt_pk_bf16_f32 v230, v220, v221\n\t"
    "v_mfma_f32_16x16x32_bf16 v[200:203], a[236:239], v[136:139], v[200:203]\n\t"
    "ds_read_b64_tr_b16 a[196:197], v45 offset:32768\n\t"
    "ds_read_b64_tr_b16 a[198:199], v45 offset:34816\n\t"
    "v_mfma_f32_16x16x32_bf16 v[204:207], a[236:239], v[140:143], v[204:207]\n\t"
    "v_cvt_pk_bf16_f32 v231, v222, v223\n\t"
    "s_waitcnt lgkmcnt(8)\n\t"
    "v_mfma_f32_16x16x32_bf16 v[192:195], a[240:243], v[144:147], v[192:195]\n\t"
    "ds_read_b64_tr_b16 a[200:201], v44 offset:36864\n\t"
    "ds_read_b64_tr_b16 a[202:203], v44 offset:38912\n\t"
    "v_mfma_f32_16x16x32_bf16 v[196:199], a[240:243], v[148:151], v[196:199]\n\t"
    "buffer_load_dwordx4 v3, %[srdw1], s68 offen offset:1024 lds\n\t"
    "s_movk_i32 s64, 0x5000\n\t"
    "v_mfma_f32_16x16x32_bf16 v[200:203], a[244:247], v[144:147], v[200:203]\n\t"
    "ds_read_b64_tr_b16 a[204:205], v45 offset:36864\n\t"
    "ds_read_b64_tr_b16 a[206:207], v45 offset:38912\n\t"
    "v_mfma_f32_16x16x32_bf16 v[204:207], a[244:247], v[148:151], v[204:207]\n\t"
    "buffer_store_dwordx4 v[224:227], v53, %[srdpt], s64 offen sc1\n\t"
    "s_waitcnt lgkmcnt(8)\n\t"
    "v_mfma_f32_16x16x32_bf16 v[192:195], a[248:251], v[152:155], v[192:195]\n\t"
    "ds_read_b64_tr_b16 a[208:209], v44 offset:40960\n\t"
    "ds_read_b64_tr_b16 a[210:211], v44 offset:43008\n\t"
    "v_mfma_f32_16x16x32_bf16 v[196:199], a[248:251], v[156:159], v[196:199]\n\t"
    "buffer_load_dwordx4 v4, %[srdw1], s68 offen offset:2048 lds\n\t"
    "buffer_store_dwordx4 v[228:231], v53, %[srdpt], s64 offen offset:256 sc1\n\t"
    "v_mfma_f32_16x16x32_bf16 v[200:203], a[252:255], v[152:155], v[200:203]\n\t"
    "ds_read_b64_tr_b16 a[212:213], v45 offset:40960\n\t"
    "ds_read_b64_tr_b16 a[214:215], v45 offset:43008\n\t"
    "v_mfma_f32_16x16x32_bf16 v[204:207], a[252:255], v[156:159], v[204:207]\n\t"
    "s_waitcnt lgkmcnt(8)\n\t"
    "v_mfma_f32_16x16x32_bf16 v[192:195], a[192:195], v[96:99], v[192:195]\n\t"
    "ds_read_b64_tr_b16 a[216:217], v44 offset:45056\n\t"
    "ds_read_b64_tr_b16 a[218:219], v44 offset:47104\n\t"
    "v_mfma_f32_16x16x32_bf16 v[196:199], a[192:195], v[100:103], v[196:199]\n\t"
    "buffer_load_dwordx4 v5, %[srdw1], s68 offen offset:3072 lds\n\t"
    "s_add_u32 m0, s61, 0x19000\n\t"
    "v_mfma_f32_16x16x32_bf16 v[200:203], a[196:199], v[96:99], v[200:203]\n\t"
    "ds_read_b64_tr_b16 a[220:221], v45 offset:45056\n\t"
    "ds_read_b64_tr_b16 a[222:223], v45 offset:47104\n\t"
    "v_mfma_f32_16x16x32_bf16 v[204:207], a[196:199], v[100:103], v[204:207]\n\t"
    "buffer_load_dwordx4 v61, %[srdw1], s68 offen lds\n\t"
    "s_waitcnt lgkmcnt(8)\n\t"
    "v_mfma_f32_16x16x32_bf16 v[192:195], a[200:203], v[104:107], v[192:195]\n\t"
    "ds_read_b64_tr_b16 a[224:225], v46 offset:32768\n\t"
    "ds_read_b64_tr_b16 a[226:227], v46 offset:34816\n\t"
    "v_mfma_f32_16x16x32_bf16 v[196:199], a[200:203], v[108:111], v[196:199]\n\t"
    "buffer_load_dwordx4 v62, %[srdw1], s68 offen offset:1024 lds\n\t"
    "v_mfma_f32_16x16x32_bf16 v[200:203], a[204:207], v[104:107], v[200:203]\n\t"
    "ds_read_b64_tr_b16 a[228:229], v47 offset:32768\n\t"
    "ds_read_b64_tr_b16 a[230:231], v47 offset:34816\n\t"
    "v_mfma_f32_16x16x32_bf16 v[204:207], a[204:207], v[108:111], v[204:207]\n\t"
    "buffer_load_dwordx4 v63, %[srdw1], s68 offen offset:2048 lds\n\t"
    "s_waitcnt lgkmcnt(8)\n\t"
    "v_mfma_f32_16x16x32_bf16 v[192:195], a[208:211], v[112:115], v[192:195]\n\t"
    "ds_read_b64_tr_b16 a[232:233], v46 offset:36864\n\t"
    "ds_read_b64_tr_b16 a[234:235], v46 offset:38912\n\t"
    "v_mfma_f32_16x16x32_bf16 v[196:199], a[208:211], v[116:119], v[196:199]\n\t"
    "v_mfma_f32_16x16x32_bf16 v[200:203], a[212:215], v[112:115], v[200:203]\n\t"
    "ds_read_b64_tr_b16 a[236:237], v47 offset:36864\n\t"
    "ds_read_b64_tr_b16 a[238:239], v47 offset:38912\n\t"
    "v_mfma_f32_16x16x32_bf16 v[204:207], a[212:215], v[116:119], v[204:207]\n\t"
    "s_waitcnt lgkmcnt(8)\n\t"
    "v_mfma_f32_16x16x32_bf16 v[192:195], a[216:219], v[120:123], v[192:195]\n\t"
    "ds_read_b64_tr_b16 a[240:241], v46 offset:40960\n\t"
    "ds_read_b64_tr_b16 a[242:243], v46 offset:43008\n\t"
    "v_mfma_f32_16x16x32_bf16 v[196:199], a[216:219], v[124:127], v[196:199]\n\t"
    "v_mfma_f32_16x16x32_bf16 v[200:203], a[220:223], v[120:123], v[200:203]\n\t"
    "ds_read_b64_tr_b16 a[244:245], v47 offset:40960\n\t"
    "ds_read_b64_tr_b16 a[246:247], v47 offset:43008\n\t"
    "v_mfma_f32_16x16x32_bf16 v[204:207], a[220:223], v[124:127], v[204:207]\n\t"
    "s_waitcnt lgkmcnt(8)\n\t"
    "v_mfma_f32_16x16x32_bf16 v[192:195], a[224:227], v[160:163], v[192:195]\n\t"
    "ds_read_b64_tr_b16 a[248:249], v46 offset:45056\n\t"
    "ds_read_b64_tr_b16 a[250:251], v46 offset:47104\n\t"
    "v_mfma_f32_16x16x32_bf16 v[196:199], a[224:227], v[164:167], v[196:199]\n\t"
    "v_mfma_f32_16x16x32_bf16 v[200:203], a[228:231], v[160:163], v[200:203]\n\t"
    "ds_read_b64_tr_b16 a[252:253], v47 offset:45056\n\t"
    "ds_read_b64_tr_b16 a[254:255], v47 offset:47104\n\t"
    "v_mfma_f32_16x16x32_bf16 v[204:207], a[228:231], v[164:167], v[204:207]\n\t"
    "s_waitcnt lgkmcnt(8)\n\t"
    "v_mfma_f32_16x16x32_bf16 v[192:195], a[232:235], v[168:171], v[192:195]\n\t"
    "v_mfma_f32_16x16x32_bf16 v[196:199], a[232:235], v[172:175], v[196:199]\n\t"
    "v_mfma_f32_16x16x32_bf16 v[200:203], a[236:239], v[168:171], v[200:203]\n\t"
    "v_mfma_f32_16x16x32_bf16 v[204:207], a[236:239], v[172:175], v[204:207]\n\t"
    "s_waitcnt lgkmcnt(4)\n\t"
    "v_mfma_f32_16x16x32_bf16 v[192:195], a[240:243], v[176:179], v[192:195]\n\t"
    "v_mfma_f32_16x16x32_bf16 v[196:199], a[240:243], v[180:183], v[196:199]\n\t"
    "v_mfma_f32_16x16x32_bf16 v[200:203], a[244:247], v[176:179], v[200:203]\n\t"
    "v_mfma_f32_16x16x32_bf16 v[204:207], a[244:247], v[180:183], v[204:207]\n\t"
    "s_waitcnt lgkmcnt(0)\n\t"
    "v_mfma_f32_16x16x32_bf16 v[192:195], a[248:251], v[184:187], v[192:195]\n\t"
    "v_mfma_f32_16x16x32_bf16 v[196:199], a[248:251], v[188:191], v[196:199]\n\t"
    "v_mfma_f32_16x16x32_bf16 v[200:203], a[252:255], v[184:187], v[200:203]\n\t"
    "v_mfma_f32_16x16x32_bf16 v[204:207], a[252:255], v[188:191], v[204:207]\n\t"
    "s_nop 7\n\t"
    "v_cvt_pk_bf16_f32 v224, v192, v193\n\t"
    "v_cvt_pk_bf16_f32 v225, v194, v195\n\t"
    "v_cvt_pk_bf16_f32 v226, v200, v201\n\t"
    "v_cvt_pk_bf16_f32 v227, v202, v203\n\t"
    "v_cvt_pk_bf16_f32 v228, v196, v197\n\t"
    "v_cvt_pk_bf16_f32 v229, v198, v199\n\t"
    "v_cvt_pk_bf16_f32 v230, v204, v205\n\t"
    "v_cvt_pk_bf16_f32 v231, v206, v207\n\t"
    "s_movk_i32 s64, 0x6000\n\t"
    "buffer_store_dwordx4 v[224:227], v53, %[srdpt], s64 offen sc1\n\t"
    "buffer_store_dwordx4 v[228:231], v53, %[srdpt], s64 offen offset:256 sc1\n\t"
    "s_nop 1\n\t"
    "s_branch LEND_%=\n\t"
    "Lsa2_%=:\n\t"
    "s_call_b64 s[70:71], LRA1_%=\n\t"
    "s_branch Lba1_%=\n\t"
    "Lsb4_%=:\n\t"
    "s_call_b64 s[70:71], LRB_%=\n\t"
    "s_branch Lbb3_%=\n\t"
    "Lsa6_%=:\n\t"
    "s_call_b64 s[70:71], LRA0_%=\n\t"
    "s_branch Lba5_%=\n\t"
    "Lsb8_%=:\n\t"
    "s_call_b64 s[70:71], LRB_%=\n\t"
    "s_branch Lbb7_%=\n\t"
    "Lsa10_%=:\n\t"
    "s_call_b64 s[70:71], LRA1_%=\n\t"
    "s_branch Lba9_%=\n\t"
    "Lsb12_%=:\n\t"
    "s_call_b64 s[70:71], LRB_%=\n\t"
    "s_branch Lbb11_%=\n\t"
    "Lsa14_%=:\n\t"
    "s_call_b64 s[70:71], LRA0_%=\n\t"
    "s_branch Lba13_%=\n\t"
    "Lsb16_%=:\n\t"
    "s_call_b64 s[70:71], LRB_%=\n\t"
    "s_branch Lbb15_%=\n\t"
    "Lsa18_%=:\n\t"
    "s_call_b64 s[70:71], LRA1_%=\n\t"
    "s_branch Lba17_%=\n\t"
    "Lsb20_%=:\n\t"
    "s_call_b64 s[70:71], LRB_%=\n\t"
    "s_branch Lbb19_%=\n\t"
    "LRA0_%=:\n\t"
    "s_nop 3\n\t"
    "v_max3_f32 v216, v128, v129, v130\n\t"
    "v_max3_f32 v216, v216, v131, v136\n\t"
    "v_max3_f32 v216, v216, v137, v138\n\t"
    "v_max3_f32 v216, v216, v139, v144\n\t"
    "v_max3_f32 v216, v216, v145, v146\n\t"
    "v_max3_f32 v216, v216, v147, v152\n\t"
    "v_max3_f32 v216, v216, v153, v154\n\t"
    "v_max_f32 v216, v216, v155\n\t"
    "v_max3_f32 v217, v132, v133, v134\n\t"
    "v_max3_f32 v217, v217, v135, v140\n\t"
    "v_max3_f32 v217, v217, v141, v142\n\t"
    "v_max3_f32 v217, v217, v143, v148\n\t"
    "v_max3_f32 v217, v217, v149, v150\n\t"
    "v_max3_f32 v217, v217, v151, v156\n\t"
    "v_max3_f32 v217, v217, v157, v158\n\t"
    "v_max_f32 v217, v217, v159\n\t"
    "v_mov_b32 v218, v216\n\t"
    "v_mov_b32 v219, v217\n\t"
    "s_nop 1\n\t"
    "v_permlane32_swap_b32 v216, v218\n\t"
    "v_permlane32_swap_b32 v217, v219\n\t"
    "s_nop 1\n\t"
    "v_max_f32 v216, v216, v218\n\t"
    "v_max_f32 v217, v217, v219\n\t"
    "v_mov_b32 v218, v216\n\t"
    "v_mov_b32 v219, v217\n\t"
    "s_nop 1\n\t"
    "v_permlane16_swap_b32 v216, v218\n\t"
    "v_permlane16_swap_b32 v217, v219\n\t"
    "s_nop 1\n\t"
    "v_max_f32 v216, v216, v218\n\t"
    "v_max_f32 v217, v217, v219\n\t"
    "v_max_f32 v216, 0, v216\n\t"
    "v_add_f32 v212, v212, v216\n\t"
    "v_exp_f32 v214, -v216\n\t"
    "v_max_f32 v217, 0, v217\n\t"
    "v_add_f32 v213, v213, v217\n\t"
    "v_exp_f32 v215, -v217\n\t"
    "v_sub_f32 v128, v128, v216\n\t"
    "v_sub_f32 v129, v129, v216\n\t"
    "v_sub_f32 v130, v130, v216\n\t"
    "v_sub_f32 v131, v131, v216\n\t"
    "v_sub_f32 v132, v132, v217\n\t"
    "v_sub_f32 v133, v133, v217\n\t"
    "v_sub_f32 v134, v134, v217\n\t"
    "v_sub_f32 v135, v135, v217\n\t"
    "v_sub_f32 v136, v136, v216\n\t"
    "v_sub_f32 v137, v137, v216\n\t"
    "v_sub_f32 v138, v138, v216\n\t"
    "v_sub_f32 v139, v139, v216\n\t"
    "v_sub_f32 v140, v140, v217\n\t"
    "v_sub_f32 v141, v141, v217\n\t"
    "v_sub_f32 v142, v142, v217\n\t"
    "v_sub_f32 v143, v143, v217\n\t"
    "v_sub_f32 v144, v144, v216\n\t"
    "v_sub_f32 v145, v145, v216\n\t"
    "v_sub_f32 v146, v146, v216\n\t"
    "v_sub_f32 v147, v147, v216\n\t"
    "v_sub_f32 v148, v148, v217\n\t"
    "v_sub_f32 v149, v149, v217\n\t"
    "v_sub_f32 v150, v150, v217\n\t"
    "v_sub_f32 v151, v151, v217\n\t"
    "v_sub_f32 v152, v152, v216\n\t"
    "v_sub_f32 v153, v153, v216\n\t"
    "v_sub_f32 v154, v154, v216\n\t"
    "v_sub_f32 v155, v155, v216\n\t"
    "v_sub_f32 v156, v156, v217\n\t"
    "v_sub_f32 v157, v157, v217\n\t"
    "v_sub_f32 v158, v158, v217\n\t"
    "v_sub_f32 v159, v159, v217\n\t"
    "v_sub_f32 v192, 0, v212\n\t"
    "v_sub_f32 v193, 0, v212\n\t"
    "v_sub_f32 v194, 0, v212\n\t"
    "v_sub_f32 v195, 0, v212\n\t"
    "v_sub_f32 v196, 0, v213\n\t"
    "v_sub_f32 v197, 0, v213\n\t"
    "v_sub_f32 v198, 0, v213\n\t"
    "v_sub_f32 v199, 0, v213\n\t"
    "s_mov_b32 s65, 1\n\t"
    "s_nop 3\n\t"
    "s_setpc_b64 s[70:71]\n\t"
    "LRA1_%=:\n\t"
    "s_nop 3\n\t"
    "v_max3_f32 v216, v160, v161, v162\n\t"
    "v_max3_f32 v216, v216, v163, v168\n\t"
    "v_max3_f32 v216, v216, v169, v170\n\t"
    "v_max3_f32 v216, v216, v171, v176\n\t"
    "v_max3_f32 v216, v216, v177, v178\n\t"
    "v_max3_f32 v216, v216, v179, v184\n\t"
    "v_max3_f32 v216, v216, v185, v186\n\t"
    "v_max_f32 v216, v216, v187\n\t"
    "v_max3_f32 v217, v164, v165, v166\n\t"
    "v_max3_f32 v217, v217, v167, v172\n\t"
    "v_max3_f32 v217, v217, v173, v174\n\t"
    "v_max3_f32 v217, v217, v175, v180\n\t"
    "v_max3_f32 v217, v217, v181, v182\n\t"
    "v_max3_f32 v217, v217, v183, v188\n\t"
    "v_max3_f32 v217, v217, v189, v190\n\t"
    "v_max_f32 v217, v217, v191\n\t"
    "v_mov_b32 v218, v216\n\t"
    "v_mov_b32 v219, v217\n\t"
    "s_nop 1\n\t"
    "v_permlane32_swap_b32 v216, v218\n\t"
    "v_permlane32_swap_b32 v217, v219\n\t"
    "s_nop 1\n\t"
    "v_max_f32 v216, v216, v218\n\t"
    "v_max_f32 v217, v217, v219\n\t"
    "v_mov_b32 v218, v216\n\t"
    "v_mov_b32 v219, v217\n\t"
    "s_nop 1\n\t"
    "v_permlane16_swap_b32 v216, v218\n\t"
    "v_permlane16_swap_b32 v217, v219\n\t"
    "s_nop 1\n\t"
    "v_max_f32 v216, v216, v218\n\t"
    "v_max_f32 v217, v217, v219\n\t"
    "v_max_f32 v216, 0, v216\n\t"
    "v_add_f32 v212, v212, v216\n\t"
    "v_exp_f32 v214, -v216\n\t"
    "v_max_f32 v217, 0, v217\n\t"
    "v_add_f32 v213, v213, v217\n\t"
    "v_exp_f32 v215, -v217\n\t"
    "v_sub_f32 v160, v160, v216\n\t"
    "v_sub_f32 v161, v161, v216\n\t"
    "v_sub_f32 v162, v162, v216\n\t"
    "v_sub_f32 v163, v163, v216\n\t"
    "v_sub_f32 v164, v164, v217\n\t"
    "v_sub_f32 v165, v165, v217\n\t"
    "v_sub_f32 v166, v166, v217\n\t"
    "v_sub_f32 v167, v167, v217\n\t"
    "v_sub_f32 v168, v168, v216\n\t"
    "v_sub_f32 v169, v169, v216\n\t"
    "v_sub_f32 v170, v170, v216\n\t"
    "v_sub_f32 v171, v171, v216\n\t"
    "v_sub_f32 v172, v172, v217\n\t"
    "v_sub_f32 v173, v173, v217\n\t"
    "v_sub_f32 v174, v174, v217\n\t"
    "v_sub_f32 v175, v175, v217\n\t"
    "v_sub_f32 v176, v176, v216\n\t"
    "v_sub_f32 v177, v177, v216\n\t"
    "v_sub_f32 v178, v178, v216\n\t"
    "v_sub_f32 v179, v179, v216\n\t"
    "v_sub_f32 v180, v180, v217\n\t"
    "v_sub_f32 v181, v181, v217\n\t"
    "v_sub_f32 v182, v182, v217\n\t"
    "v_sub_f32 v183, v183, v217\n\t"
    "v_sub_f32 v184, v184, v216\n\t"
    "v_sub_f32 v185, v185, v216\n\t"
    "v_sub_f32 v186, v186, v216\n\t"
    "v_sub_f32 v187, v187, v216\n\t"
    "v_sub_f32 v188, v188, v217\n\t"
    "v_sub_f32 v189, v189, v217\n\t"
    "v_sub_f32 v190, v190, v217\n\t"
    "v_sub_f32 v191, v191, v217\n\t"
    "v_sub_f32 v192, 0, v212\n\t"
    "v_sub_f32 v193, 0, v212\n\t"
    "v_sub_f32 v194, 0, v212\n\t"
    "v_sub_f32 v195, 0, v212\n\t"
    "v_sub_f32 v196, 0, v213\n\t"
    "v_sub_f32 v197, 0, v213\n\t"
    "v_sub_f32 v198, 0, v213\n\t"
    "v_sub_f32 v199, 0, v213\n\t"
    "s_mov_b32 s65, 1\n\t"
    "s_nop 3\n\t"
    "s_setpc_b64 s[70:71]\n\t"
    "LRB_%=:\n\t"
    "s_nop 15\n\t"
    "v_accvgpr_read_b32 v252, a0\n\t"
    "v_mul_f32 v252, v252, v214\n\t"
    "v_accvgpr_write_b32 a0, v252\n\t"
    "v_accvgpr_read_b32 v253, a1\n\t"
    "v_mul_f32 v253, v253, v214\n\t"
    "v_accvgpr_write_b32 a1, v253\n\t"
    "v_accvgpr_read_b32 v252, a2\n\t"
    "v_mul_f32 v252, v252, v214\n\t"
    "v_accvgpr_write_b32 a2, v252\n\t"
    "v_accvgpr_read_b32 v253, a3\n\t"
    "v_mul_f32 v253, v253, v214\n\t"
    "v_accvgpr_write_b32 a3, v253\n\t"
    "v_accvgpr_read_b32 v252, a4\n\t"
    "v_mul_f32 v252, v252, v215\n\t"
    "v_accvgpr_write_b32 a4, v252\n\t"
    "v_accvgpr_read_b32 v253, a5\n\t"
    "v_mul_f32 v253, v253, v215\n\t"
    "v_accvgpr_write_b32 a5, v253\n\t"
    "v_accvgpr_read_b32 v252, a6\n\t"
    "v_mul_f32 v252, v252, v215\n\t"
    "v_accvgpr_write_b32 a6, v252\n\t"
    "v_accvgpr_read_b32 v253, a7\n\t"
    "v_mul_f32 v253, v253, v215\n\t"
    "v_accvgpr_write_b32 a7, v253\n\t"
    "v_accvgpr_read_b32 v252, a8\n\t"
    "v_mul_f32 v252, v252, v214\n\t"
    "v_accvgpr_write_b32 a8, v252\n\t"
    "v_accvgpr_read_b32 v253, a9\n\t"
    "v_mul_f32 v253, v253, v214\n\t"
    "v_accvgpr_write_b32 a9, v253\n\t"
    "v_accvgpr_read_b32 v252, a10\n\t"
    "v_mul_f32 v252, v252, v214\n\t"
    "v_accvgpr_write_b32 a10, v252\n\t"
    "v_accvgpr_read_b32 v253, a11\n\t"
    "v_mul_f32 v253, v253, v214\n\t"
    "v_accvgpr_write_b32 a11, v253\n\t"
    "v_accvgpr_read_b32 v252, a12\n\t"
    "v_mul_f32 v252, v252, v215\n\t"
    "v_accvgpr_write_b32 a12, v252\n\t"
    "v_accvgpr_read_b32 v253, a13\n\t"
    "v_mul_f32 v253, v253, v215\n\t"
    "v_accvgpr_write_b32 a13, v253\n\t"
    "v_accvgpr_read_b32 v252, a14\n\t"
    "v_mul_f32 v252, v252, v215\n\t"
    "v_accvgpr_write_b32 a14, v252\n\t"
    "v_accvgpr_read_b32 v253, a15\n\t"
    "v_mul_f32 v253, v253, v215\n\t"
    "v_accvgpr_write_b32 a15, v253\n\t"
    "v_accvgpr_read_b32 v252, a16\n\t"
    "v_mul_f32 v252, v252, v214\n\t"
    "v_accvgpr_write_b32 a16, v252\n\t"
    "v_accvgpr_read_b32 v253, a17\n\t"
    "v_mul_f32 v253, v253, v214\n\t"
    "v_accvgpr_write_b32 a17, v253\n\t"
    "v_accvgpr_read_b32 v252, a18\n\t"
    "v_mul_f32 v252, v252, v214\n\t"
    "v_accvgpr_write_b32 a18, v252\n\t"
    "v_accvgpr_read_b32 v253, a19\n\t"
    "v_mul_f32 v253, v253, v214\n\t"
    "v_accvgpr_write_b32 a19, v253\n\t"
    "v_accvgpr_read_b32 v252, a20\n\t"
    "v_mul_f32 v252, v252, v215\n\t"
    "v_accvgpr_write_b32 a20, v252\n\t"
    "v_accvgpr_read_b32 v253, a21\n\t"
    "v_mul_f32 v253, v253, v215\n\t"
    "v_accvgpr_write_b32 a21, v253\n\t"
    "v_accvgpr_read_b32 v252, a22\n\t"
    "v_mul_f32 v252, v252, v215\n\t"
    "v_accvgpr_write_b32 a22, v252\n\t"
    "v_accvgpr_read_b32 v253, a23\n\t"
    "v_mul_f32 v253, v253, v215\n\t"
    "v_accvgpr_write_b32 a23, v253\n\t"
    "v_accvgpr_read_b32 v252, a24\n\t"
    "v_mul_f32 v252, v252, v214\n\t"
    "v_accvgpr_write_b32 a24, v252\n\t"
    "v_accvgpr_read_b32 v253, a25\n\t"
    "v_mul_f32 v253, v253, v214\n\t"
    "v_accvgpr_write_b32 a25, v253\n\t"
    "v_accvgpr_read_b32 v252, a26\n\t"
    "v_mul_f32 v252, v252, v214\n\t"
    "v_accvgpr_write_b32 a26, v252\n\t"
    "v_accvgpr_read_b32 v253, a27\n\t"
    "v_mul_f32 v253, v253, v214\n\t"
    "v_accvgpr_write_b32 a27, v253\n\t"
    "v_accvgpr_read_b32 v252, a28\n\t"
    "v_mul_f32 v252, v252, v215\n\t"
    "v_accvgpr_write_b32 a28, v252\n\t"
    "v_accvgpr_read_b32 v253, a29\n\t"
    "v_mul_f32 v253, v253, v215\n\t"
    "v_accvgpr_write_b32 a29, v253\n\t"
    "v_accvgpr_read_b32 v252, a30\n\t"
    "v_mul_f32 v252, v252, v215\n\t"
    "v_accvgpr_write_b32 a30, v252\n\t"
    "v_accvgpr_read_b32 v253, a31\n\t"
    "v_mul_f32 v253, v253, v215\n\t"
    "v_accvgpr_write_b32 a31, v253\n\t"
    "v_accvgpr_read_b32 v252, a32\n\t"
    "v_mul_f32 v252, v252, v214\n\t"
    "v_accvgpr_write_b32 a32, v252\n\t"
    "v_accvgpr_read_b32 v253, a33\n\t"
    "v_mul_f32 v253, v253, v214\n\t"
    "v_accvgpr_write_b32 a33, v253\n\t"
    "v_accvgpr_read_b32 v252, a34\n\t"
    "v_mul_f32 v252, v252, v214\n\t"
    "v_accvgpr_write_b32 a34, v252\n\t"
    "v_accvgpr_read_b32 v253, a35\n\t"
    "v_mul_f32 v253, v253, v214\n\t"
    "v_accvgpr_write_b32 a35, v253\n\t"
    "v_accvgpr_read_b32 v252, a36\n\t"
    "v_mul_f32 v252, v252, v215\n\t"
    "v_accvgpr_write_b32 a36, v252\n\t"
    "v_accvgpr_read_b32 v253, a37\n\t"
    "v_mul_f32 v253, v253, v215\n\t"
    "v_accvgpr_write_b32 a37, v253\n\t"
    "v_accvgpr_read_b32 v252, a38\n\t"
    "v_mul_f32 v252, v252, v215\n\t"
    "v_accvgpr_write_b32 a38, v252\n\t"
    "v_accvgpr_read_b32 v253, a39\n\t"
    "v_mul_f32 v253, v253, v215\n\t"
    "v_accvgpr_write_b32 a39, v253\n\t"
    "v_accvgpr_read_b32 v252, a40\n\t"
    "v_mul_f32 v252, v252, v214\n\t"
    "v_accvgpr_write_b32 a40, v252\n\t"
    "v_accvgpr_read_b32 v253, a41\n\t"
    "v_mul_f32 v253, v253, v214\n\t"
    "v_accvgpr_write_b32 a41, v253\n\t"
    "v_accvgpr_read_b32 v252, a42\n\t"
    "v_mul_f32 v252, v252, v214\n\t"
    "v_accvgpr_write_b32 a42, v252\n\t"
    "v_accvgpr_read_b32 v253, a43\n\t"
    "v_mul_f32 v253, v253, v214\n\t"
    "v_accvgpr_write_b32 a43, v253\n\t"
    "v_accvgpr_read_b32 v252, a44\n\t"
    "v_mul_f32 v252, v252, v215\n\t"
    "v_accvgpr_write_b32 a44, v252\n\t"
    "v_accvgpr_read_b32 v253, a45\n\t"
    "v_mul_f32 v253, v253, v215\n\t"
    "v_accvgpr_write_b32 a45, v253\n\t"
    "v_accvgpr_read_b32 v252, a46\n\t"
    "v_mul_f32 v252, v252, v215\n\t"
    "v_accvgpr_write_b32 a46, v252\n\t"
    "v_accvgpr_read_b32 v253, a47\n\t"
    "v_mul_f32 v253, v253, v215\n\t"
    "v_accvgpr_write_b32 a47, v253\n\t"
    "v_accvgpr_read_b32 v252, a48\n\t"
    "v_mul_f32 v252, v252, v214\n\t"
    "v_accvgpr_write_b32 a48, v252\n\t"
    "v_accvgpr_read_b32 v253, a49\n\t"
    "v_mul_f32 v253, v253, v214\n\t"
    "v_accvgpr_write_b32 a49, v253\n\t"
    "v_accvgpr_read_b32 v252, a50\n\t"
    "v_mul_f32 v252, v252, v214\n\t"
    "v_accvgpr_write_b32 a50, v252\n\t"
    "v_accvgpr_read_b32 v253, a51\n\t"
    "v_mul_f32 v253, v253, v214\n\t"
    "v_accvgpr_write_b32 a51, v253\n\t"
    "v_accvgpr_read_b32 v252, a52\n\t"
    "v_mul_f32 v252, v252, v215\n\t"
    "v_accvgpr_write_b32 a52, v252\n\t"
    "v_accvgpr_read_b32 v253, a53\n\t"
    "v_mul_f32 v253, v253, v215\n\t"
    "v_accvgpr_write_b32 a53, v253\n\t"
    "v_accvgpr_read_b32 v252, a54\n\t"
    "v_mul_f32 v252, v252, v215\n\t"
    "v_accvgpr_write_b32 a54, v252\n\t"
    "v_accvgpr_read_b32 v253, a55\n\t"
    "v_mul_f32 v253, v253, v215\n\t"
    "v_accvgpr_write_b32 a55, v253\n\t"
    "v_accvgpr_read_b32 v252, a56\n\t"
    "v_mul_f32 v252, v252, v214\n\t"
    "v_accvgpr_write_b32 a56, v252\n\t"
    "v_accvgpr_read_b32 v253, a57\n\t"
    "v_mul_f32 v253, v253, v214\n\t"
    "v_accvgpr_write_b32 a57, v253\n\t"
    "v_accvgpr_read_b32 v252, a58\n\t"
    "v_mul_f32 v252, v252, v214\n\t"
    "v_accvgpr_write_b32 a58, v252\n\t"
    "v_accvgpr_read_b32 v253, a59\n\t"
    "v_mul_f32 v253, v253, v214\n\t"
    "v_accvgpr_write_b32 a59, v253\n\t"
    "v_accvgpr_read_b32 v252, a60\n\t"
    "v_mul_f32 v252, v252, v215\n\t"
    "v_accvgpr_write_b32 a60, v252\n\t"
    "v_accvgpr_read_b32 v253, a61\n\t"
    "v_mul_f32 v253, v253, v215\n\t"
    "v_accvgpr_write_b32 a61, v253\n\t"
    "v_accvgpr_read_b32 v252, a62\n\t"
    "v_mul_f32 v252, v252, v215\n\t"
    "v_accvgpr_write_b32 a62, v252\n\t"
    "v_accvgpr_read_b32 v253, a63\n\t"
    "v_mul_f32 v253, v253, v215\n\t"
    "v_accvgpr_write_b32 a63, v253\n\t"
    "v_accvgpr_read_b32 v252, a64\n\t"
    "v_mul_f32 v252, v252, v214\n\t"
    "v_accvgpr_write_b32 a64, v252\n\t"
    "v_accvgpr_read_b32 v253, a65\n\t"
    "v_mul_f32 v253, v253, v214\n\t"
    "v_accvgpr_write_b32 a65, v253\n\t"
    "v_accvgpr_read_b32 v252, a66\n\t"
    "v_mul_f32 v252, v252, v214\n\t"
    "v_accvgpr_write_b32 a66, v252\n\t"
    "v_accvgpr_read_b32 v253, a67\n\t"
    "v_mul_f32 v253, v253, v214\n\t"
    "v_accvgpr_write_b32 a67, v253\n\t"
    "v_accvgpr_read_b32 v252, a68\n\t"
    "v_mul_f32 v252, v252, v215\n\t"
    "v_accvgpr_write_b32 a68, v252\n\t"
    "v_accvgpr_read_b32 v253, a69\n\t"
    "v_mul_f32 v253, v253, v215\n\t"
    "v_accvgpr_write_b32 a69, v253\n\t"
    "v_accvgpr_read_b32 v252, a70\n\t"
    "v_mul_f32 v252, v252, v215\n\t"
    "v_accvgpr_write_b32 a70, v252\n\t"
    "v_accvgpr_read_b32 v253, a71\n\t"
    "v_mul_f32 v253, v253, v215\n\t"
    "v_accvgpr_write_b32 a71, v253\n\t"
    "v_accvgpr_read_b32 v252, a72\n\t"
    "v_mul_f32 v252, v252, v214\n\t"
    "v_accvgpr_write_b32 a72, v252\n\t"
    "v_accvgpr_read_b32 v253, a73\n\t"
    "v_mul_f32 v253, v253, v214\n\t"
    "v_accvgpr_write_b32 a73, v253\n\t"
    "v_accvgpr_read_b32 v252, a74\n\t"
    "v_mul_f32 v252, v252, v214\n\t"
    "v_accvgpr_write_b32 a74, v252\n\t"
    "v_accvgpr_read_b32 v253, a75\n\t"
    "v_mul_f32 v253, v253, v214\n\t"
    "v_accvgpr_write_b32 a75, v253\n\t"
    "v_accvgpr_read_b32 v252, a76\n\t"
    "v_mul_f32 v252, v252, v215\n\t"
    "v_accvgpr_write_b32 a76, v252\n\t"
    "v_accvgpr_read_b32 v253, a77\n\t"
    "v_mul_f32 v253, v253, v215\n\t"
    "v_accvgpr_write_b32 a77, v253\n\t"
    "v_accvgpr_read_b32 v252, a78\n\t"
    "v_mul_f32 v252, v252, v215\n\t"
    "v_accvgpr_write_b32 a78, v252\n\t"
    "v_accvgpr_read_b32 v253, a79\n\t"
    "v_mul_f32 v253, v253, v215\n\t"
    "v_accvgpr_write_b32 a79, v253\n\t"
    "v_accvgpr_read_b32 v252, a80\n\t"
    "v_mul_f32 v252, v252, v214\n\t"
    "v_accvgpr_write_b32 a80, v252\n\t"
    "v_accvgpr_read_b32 v253, a81\n\t"
    "v_mul_f32 v253, v253, v214\n\t"
    "v_accvgpr_write_b32 a81, v253\n\t"
    "v_accvgpr_read_b32 v252, a82\n\t"
    "v_mul_f32 v252, v252, v214\n\t"
    "v_accvgpr_write_b32 a82, v252\n\t"
    "v_accvgpr_read_b32 v253, a83\n\t"
    "v_mul_f32 v253, v253, v214\n\t"
    "v_accvgpr_write_b32 a83, v253\n\t"
    "v_accvgpr_read_b32 v252, a84\n\t"
    "v_mul_f32 v252, v252, v215\n\t"
    "v_accvgpr_write_b32 a84, v252\n\t"
    "v_accvgpr_read_b32 v253, a85\n\t"
    "v_mul_f32 v253, v253, v215\n\t"
    "v_accvgpr_write_b32 a85, v253\n\t"
    "v_accvgpr_read_b32 v252, a86\n\t"
    "v_mul_f32 v252, v252, v215\n\t"
    "v_accvgpr_write_b32 a86, v252\n\t"
    "v_accvgpr_read_b32 v253, a87\n\t"
    "v_mul_f32 v253, v253, v215\n\t"
    "v_accvgpr_write_b32 a87, v253\n\t"
    "v_accvgpr_read_b32 v252, a88\n\t"
    "v_mul_f32 v252, v252, v214\n\t"
    "v_accvgpr_write_b32 a88, v252\n\t"
    "v_accvgpr_read_b32 v253, a89\n\t"
    "v_mul_f32 v253, v253, v214\n\t"
    "v_accvgpr_write_b32 a89, v253\n\t"
    "v_accvgpr_read_b32 v252, a90\n\t"
    "v_mul_f32 v252, v252, v214\n\t"
    "v_accvgpr_write_b32 a90, v252\n\t"
    "v_accvgpr_read_b32 v253, a91\n\t"
    "v_mul_f32 v253, v253, v214\n\t"
    "v_accvgpr_write_b32 a91, v253\n\t"
    "v_accvgpr_read_b32 v252, a92\n\t"
    "v_mul_f32 v252, v252, v215\n\t"
    "v_accvgpr_write_b32 a92, v252\n\t"
    "v_accvgpr_read_b32 v253, a93\n\t"
    "v_mul_f32 v253, v253, v215\n\t"
    "v_accvgpr_write_b32 a93, v253\n\t"
    "v_accvgpr_read_b32 v252, a94\n\t"
    "v_mul_f32 v252, v252, v215\n\t"
    "v_accvgpr_write_b32 a94, v252\n\t"
    "v_accvgpr_read_b32 v253, a95\n\t"
    "v_mul_f32 v253, v253, v215\n\t"
    "v_accvgpr_write_b32 a95, v253\n\t"
    "v_accvgpr_read_b32 v252, a96\n\t"
    "v_mul_f32 v252, v252, v214\n\t"
    "v_accvgpr_write_b32 a96, v252\n\t"
    "v_accvgpr_read_b32 v253, a97\n\t"
    "v_mul_f32 v253, v253, v214\n\t"
    "v_accvgpr_write_b32 a97, v253\n\t"
    "v_accvgpr_read_b32 v252, a98\n\t"
    "v_mul_f32 v252, v252, v214\n\t"
    "v_accvgpr_write_b32 a98, v252\n\t"
    "v_accvgpr_read_b32 v253, a99\n\t"
    "v_mul_f32 v253, v253, v214\n\t"
    "v_accvgpr_write_b32 a99, v253\n\t"
    "v_accvgpr_read_b32 v252, a100\n\t"
    "v_mul_f32 v252, v252, v215\n\t"
    "v_accvgpr_write_b32 a100, v252\n\t"
    "v_accvgpr_read_b32 v253, a101\n\t"
    "v_mul_f32 v253, v253, v215\n\t"
    "v_accvgpr_write_b32 a101, v253\n\t"
    "v_accvgpr_read_b32 v252, a102\n\t"
    "v_mul_f32 v252, v252, v215\n\t"
    "v_accvgpr_write_b32 a102, v252\n\t"
    "v_accvgpr_read_b32 v253, a103\n\t"
    "v_mul_f32 v253, v253, v215\n\t"
    "v_accvgpr_write_b32 a103, v253\n\t"
    "v_accvgpr_read_b32 v252, a104\n\t"
    "v_mul_f32 v252, v252, v214\n\t"
    "v_accvgpr_write_b32 a104, v252\n\t"
    "v_accvgpr_read_b32 v253, a105\n\t"
    "v_mul_f32 v253, v253, v214\n\t"
    "v_accvgpr_write_b32 a105, v253\n\t"
    "v_accvgpr_read_b32 v252, a106\n\t"
    "v_mul_f32 v252, v252, v214\n\t"
    "v_accvgpr_write_b32 a106, v252\n\t"
    "v_accvgpr_read_b32 v253, a107\n\t"
    "v_mul_f32 v253, v253, v214\n\t"
    "v_accvgpr_write_b32 a107, v253\n\t"
    "v_accvgpr_read_b32 v252, a108\n\t"
    "v_mul_f32 v252, v252, v215\n\t"
    "v_accvgpr_write_b32 a108, v252\n\t"
    "v_accvgpr_read_b32 v253, a109\n\t"
    "v_mul_f32 v253, v253, v215\n\t"
    "v_accvgpr_write_b32 a109, v253\n\t"
    "v_accvgpr_read_b32 v252, a110\n\t"
    "v_mul_f32 v252, v252, v215\n\t"
    "v_accvgpr_write_b32 a110, v252\n\t"
    "v_accvgpr_read_b32 v253, a111\n\t"
    "v_mul_f32 v253, v253, v215\n\t"
    "v_accvgpr_write_b32 a111, v253\n\t"
    "v_accvgpr_read_b32 v252, a112\n\t"
    "v_mul_f32 v252, v252, v214\n\t"
    "v_accvgpr_write_b32 a112, v252\n\t"
    "v_accvgpr_read_b32 v253, a113\n\t"
    "v_mul_f32 v253, v253, v214\n\t"
    "v_accvgpr_write_b32 a113, v253\n\t"
    "v_accvgpr_read_b32 v252, a114\n\t"
    "v_mul_f32 v252, v252, v214\n\t"
    "v_accvgpr_write_b32 a114, v252\n\t"
    "v_accvgpr_read_b32 v253, a115\n\t"
    "v_mul_f32 v253, v253, v214\n\t"
    "v_accvgpr_write_b32 a115, v253\n\t"
    "v_accvgpr_read_b32 v252, a116\n\t"
    "v_mul_f32 v252, v252, v215\n\t"
    "v_accvgpr_write_b32 a116, v252\n\t"
    "v_accvgpr_read_b32 v253, a117\n\t"
    "v_mul_f32 v253, v253, v215\n\t"
    "v_accvgpr_write_b32 a117, v253\n\t"
    "v_accvgpr_read_b32 v252, a118\n\t"
    "v_mul_f32 v252, v252, v215\n\t"
    "v_accvgpr_write_b32 a118, v252\n\t"
    "v_accvgpr_read_b32 v253, a119\n\t"
    "v_mul_f32 v253, v253, v215\n\t"
    "v_accvgpr_write_b32 a119, v253\n\t"
    "v_accvgpr_read_b32 v252, a120\n\t"
    "v_mul_f32 v252, v252, v214\n\t"
    "v_accvgpr_write_b32 a120, v252\n\t"
    "v_accvgpr_read_b32 v253, a121\n\t"
    "v_mul_f32 v253, v253, v214\n\t"
    "v_accvgpr_write_b32 a121, v253\n\t"
    "v_accvgpr_read_b32 v252, a122\n\t"
    "v_mul_f32 v252, v252, v214\n\t"
    "v_accvgpr_write_b32 a122, v252\n\t"
    "v_accvgpr_read_b32 v253, a123\n\t"
    "v_mul_f32 v253, v253, v214\n\t"
    "v_accvgpr_write_b32 a123, v253\n\t"
    "v_accvgpr_read_b32 v252, a124\n\t"
    "v_mul_f32 v252, v252, v215\n\t"
    "v_accvgpr_write_b32 a124, v252\n\t"
    "v_accvgpr_read_b32 v253, a125\n\t"
    "v_mul_f32 v253, v253, v215\n\t"
    "v_accvgpr_write_b32 a125, v253\n\t"
    "v_accvgpr_read_b32 v252, a126\n\t"
    "v_mul_f32 v252, v252, v215\n\t"
    "v_accvgpr_write_b32 a126, v252\n\t"
    "v_accvgpr_read_b32 v253, a127\n\t"
    "v_mul_f32 v253, v253, v215\n\t"
    "v_accvgpr_write_b32 a127, v253\n\t"
    "v_mul_f32 v200, v200, v214\n\t"
    "v_mul_f32 v201, v201, v214\n\t"
    "v_mul_f32 v202, v202, v214\n\t"
    "v_mul_f32 v203, v203, v214\n\t"
    "v_mul_f32 v204, v204, v215\n\t"
    "v_mul_f32 v205, v205, v215\n\t"
    "v_mul_f32 v206, v206, v215\n\t"
    "v_mul_f32 v207, v207, v215\n\t"
    "s_mov_b32 s65, 0\n\t"
    "s_nop 3\n\t"
    "s_setpc_b64 s[70:71]\n\t"
    "LEND_%=:\n\t"
    :
    : [srdk] "s"(srdk), [srdv] "s"(srdv), [srdwq] "s"(srdwq), [srdwo] "s"(srdwo), [srdxn] "s"(srdxn), [srdmp] "s"(srdmp), [srdpt] "s"(srdpt),
      [srdt] "s"(srdt), [tstr] "s"(tstr), [wid] "s"(w), [ldsb] "s"(lb), [xoff] "s"(xoff), [wobase] "s"(wobase),
      "{v[8:23]}"(aA), "{v[24:39]}"(aB), "{v[40:55]}"(aC), "{v[56:63]}"(aD), "{v[2:5]}"(aE), [srdw1] "s"(srdw1)
    : "v6", "v7", "v64", "v65", "v66", "v67", "v68", "v69", "v70", "v71", "v72", "v73", "v74", "v75", "v76", "v77", "v78", "v79", "v80", "v81", "v82", "v83", "v84", "v85", "v86", "v87", "v88", "v89", "v90", "v91", "v92", "v93", "v94", "v95", "v96", "v97", "v98", "v99", "v100", "v101", "v102", "v103", "v104", "v105", "v106", "v107", "v108", "v109", "v110", "v111", "v112", "v113", "v114", "v115", "v116", "v117", "v118", "v119", "v120", "v121", "v122", "v123", "v124", "v125", "v126", "v127", "v128", "v129", "v130", "v131", "v132", "v133", "v134", "v135", "v136", "v137", "v138", "v139", "v140", "v141", "v142", "v143", "v144", "v145", "v146", "v147", "v148", "v149", "v150", "v151", "v152", "v153", "v154", "v155", "v156", "v157", "v158", "v159", "v160", "v161", "v162", "v163", "v164", "v165", "v166", "v167", "v168", "v169", "v170", "v171", "v172", "v173", "v174", "v175", "v176", "v177", "v178", "v179", "v180", "v181", "v182", "v183", "v184", "v185", "v186", "v187", "v188", "v189", "v190", "v191", "v192", "v193", "v194", "v195", "v196", "v197", "v198", "v199", "v200", "v201", "v202", "v203", "v204", "v205", "v206", "v207", "v208", "v209", "v210", "v211", "v212", "v213", "v214", "v215", "v216", "v217", "v218", "v219", "v220", "v221", "v222", "v223", "v224", "v225", "v226", "v227", "v228", "v229", "v230", "v231", "v232", "v233", "v234", "v235", "v236", "v237", "v238", "v239", "v240", "v241", "v242", "v243", "v244", "v245", "v246", "v247", "v248", "v249", "v250", "v251", "v252", "v253", "v254", "v255", "a0", "a1", "a2", "a3", "a4", "a5", "a6", "a7", "a8", "a9", "a10", "a11", "a12", "a13", "a14", "a15", "a16", "a17", "a18", "a19", "a20", "a21", "a22", "a23", "a24", "a25", "a26", "a27", "a28", "a29", "a30", "a31", "a32", "a33", "a34", "a35", "a36", "a37", "a38", "a39", "a40", "a41", "a42", "a43", "a44", "a45", "a46", "a47", "a48", "a49", "a50", "a51", "a52", "a53", "a54", "a55", "a56", "a57", "a58", "a59", "a60", "a61", "a62", "a63", "a64", "a65", "a66", "a67", "a68", "a69", "a70", "a71", "a72", "a73", "a74", "a75", "a76", "a77", "a78", "a79", "a80", "a81", "a82", "a83", "a84", "a85", "a86", "a87", "a88", "a89", "a90", "a91", "a92", "a93", "a94", "a95", "a96", "a97", "a98", "a99", "a100", "a101", "a102", "a103", "a104", "a105", "a106", "a107", "a108", "a109", "a110", "a111", "a112", "a113", "a114", "a115", "a116", "a117", "a118", "a119", "a120", "a121", "a122", "a123", "a124", "a125", "a126", "a127", "a128", "a129", "a130", "a131", "a132", "a133", "a134", "a135", "a136", "a137", "a138", "a139", "a140", "a141", "a142", "a143", "a144", "a145", "a146", "a147", "a148", "a149", "a150", "a151", "a152", "a153", "a154", "a155", "a156", "a157", "a158", "a159", "a160", "a161", "a162", "a163", "a164", "a165", "a166", "a167", "a168", "a169", "a170", "a171", "a172", "a173", "a174", "a175", "a176", "a177", "a178", "a179", "a180", "a181", "a182", "a183", "a184", "a185", "a186", "a187", "a188", "a189", "a190", "a191", "a192", "a193", "a194", "a195", "a196", "a197", "a198", "a199", "a200", "a201", "a202", "a203", "a204", "a205", "a206", "a207", "a208", "a209", "a210", "a211", "a212", "a213", "a214", "a215", "a216", "a217", "a218", "a219", "a220", "a221", "a222", "a223", "a224", "a225", "a226", "a227", "a228", "a229", "a230", "a231", "a232", "a233", "a234", "a235", "a236", "a237", "a238", "a239", "a240", "a241", "a242", "a243", "a244", "a245", "a246", "a247", "a248", "a249", "a250", "a251", "a252", "a253", "a254", "a255", "s60", "s61", "s62", "s63", "s64", "s65", "s66", "s67", "s68", "s69", "s70", "s71", "s72", "s73", "s74", "s75", "s98", "s99", "vcc", "scc", "memory"
  );
  unsigned m;
  asm volatile("v_mbcnt_lo_u32_b32 %0, -1, 0\n\tv_mbcnt_hi_u32_b32 %0, -1, %0" : "=v"(m));
  gu32* fl = (gu32*)(ws + WS_FLAG + rb * 384u);
  const unsigned q8 = m & 7u, hh = (m >> 3) & 1u, s0 = w * 4u + (m >> 4), row = rb * 64u + g * 8u + q8, oob = w < 3u ? 0u : 0x40000000u;
  const unsigned fo0 = (row >> 5) * 28672u + s0 * 1024u + ((row & 31u) + 32u * hh) * 16u, fo1 = fo0 + 16384u + oob;
  const __amdgpu_buffer_rsrc_t rm = mkrsrc(ws + WS_MOD + 2u * DM * 4u, 4u * DM * 4u);
  const unsigned c0 = 16u * s0 + 4u * hh, own = 1u << g, xo0 = (row * DM + c0) * 2u, xo1 = xo0 + 512u + oob;
  unsigned pf; u32x8 xr, br; f32x16 mg, ms, mc, gv, fs;
  u32x16 bA, bB, bC;
  asm volatile(
    "global_load_dword v248, %[fo], %[fl] sc1\n\t"
    "buffer_load_dwordx2 v[240:241], %[x0], %[rx], 0 offen\n\tbuffer_load_dwordx2 v[242:243], %[x0], %[rx], 0 offen offset:16\n\t"
    "buffer_load_dwordx2 v[244:245], %[x1], %[rx], 0 offen\n\tbuffer_load_dwordx2 v[246:247], %[x1], %[rx], 0 offen offset:16\n\t"
    "buffer_load_dwordx4 v[192:195], %[m0], %[rm], 0 offen\n\tbuffer_load_dwordx4 v[196:199], %[m0], %[rm], 0 offen offset:32\n\t"
    "buffer_load_dwordx4 v[200:203], %[m1], %[rm], 0 offen\n\tbuffer_load_dwordx4 v[204:207], %[m1], %[rm], 0 offen offset:32\n\t"
    : "=&{v248}"(pf), "=&{v[240:247]}"(xr), "=&{v[192:207]}"(mg)
    : [fo] "v"(q8 * 4u), [fl] "s"(fl), [x0] "v"(xo0), [x1] "v"(xo1), [m0] "v"(c0 * 4u), [m1] "v"(c0 * 4u + 1024u + oob), [rx] "s"(mkrsrc(x, (unsigned)FBYTES)), [rm] "s"(rm)
    : "memory");
  __builtin_amdgcn_sched_barrier(0);
  const unsigned n2 = m & 15u, k2 = m >> 4, lo2 = (k2 >> 1) * 1024u + (k2 & 1u) * 512u + n2 * 16u, m8 = m >> 3;
#pragma unroll
  for (unsigned j = 0; j < 7; ++j) {
    bA[j] = w1fill(j, m);
  }
  bA[7] = m * 16u;
  const unsigned rr = 4u * k2 + (n2 >> 2);
#pragma unroll
  for (unsigned j = 0; j < 4; ++j) {
    bA[8 + j] = (8u * j + m8) * 896u + (((m & 7u) ^ (((m8 >> 1) & 1u) + 4u * (m8 >> 2))) << 4) - j * 1024u;
    bB[j] = lb + (((j >> 1) ^ hs) * 112u + rr) * 128u + (((4u * hs + 2u * ((n2 >> 1) & 1u) + (j & 1u)) ^ (((rr >> 1) & 1u) + 4u * ((rr >> 2) & 1u))) << 4) + (n2 & 1u) * 8u;
  }
#pragma unroll
  for (unsigned e = 0; e < 2; ++e)
    bA[12 + e] = lb + (16u * (k2 >> 1) + 4u * (k2 & 1u) + (n2 >> 2)) * 448u + hs * 224u + (n2 & 3u) * 8u + (k2 & 1u) * (((hs + e) & 1u) ? -32u : 32u);
  bA[14] = 0; bA[15] = lo2;
  bB[4] = 0;
  bB[5] = lb + 131072u + w * 6144u + m * 16u;
  bB[6] = lb + 155648u + w * 1024u + m * 8u;
  bB[7] = lb + 131072u + (w ^ 2u) * 6144u + m * 16u;
  bB[8] = lb + 155648u + (w ^ 2u) * 1024u + m * 8u;
#pragma unroll
  for (unsigned j = 9; j < 16; ++j) bB[j] = 0;
  asm volatile("" : "+v"(bA), "+v"(bB));
  __builtin_amdgcn_sched_barrier(0);
  asm volatile(
    "s_mov_b32 s60, 0\n\t"
    "s_mov_b32 s64, 0\n\t"
    "s_mov_b32 s61, 0x100000\n\t"
    "v_mov_b32 v250, 1\n\t"
    "s_waitcnt vmcnt(8)\n\t"
    "s_barrier\n\t"
    "s_cmp_lg_u32 %[wid], 0\n\t"
    "s_cbranch_scc1 SH_%=\n\t"
    "s_mov_b64 exec, 1\n\t"
    "global_store_dword %[fo], v250, %[flo] sc1\n\t"
    "s_mov_b64 exec, -1\n\t"
    "SH_%=:\n\t"
    "v_cmp_ne_u32 vcc, 0, v248\n\t"
    "s_or_b32 s62, vcc_lo, %[own]\n\t"
    "s_and_b32 s62, s62, 0xff\n\t"
    "s_cmp_eq_u32 s62, 0xff\n\t"
    "s_cbranch_scc0 SB_%=\n\t"
    "SQ_%=:\n\t"
    "s_mov_b32 s63, 0x0\n\t"
    "buffer_load_dwordx4 v[64:67], %[o0], %[srd], s63 offen sc1\n\t"
    "buffer_load_dwordx4 v[96:99], %[o1], %[srd], s63 offen sc1\n\t"
    "s_mov_b32 s63, 0x1c0000\n\t"
    "buffer_load_dwordx4 v[68:71], %[o0], %[srd], s63 offen sc1\n\t"
    "buffer_load_dwordx4 v[100:103], %[o1], %[srd], s63 offen sc1\n\t"
    "s_mov_b32 s63, 0x380000\n\t"
    "buffer_load_dwordx4 v[72:75], %[o0], %[srd], s63 offen sc1\n\t"
    "buffer_load_dwordx4 v[104:107], %[o1], %[srd], s63 offen sc1\n\t"
    "s_mov_b32 s63, 0x540000\n\t"
    "buffer_load_dwordx4 v[76:79], %[o0], %[srd], s63 offen sc1\n\t"
    "buffer_load_dwordx4 v[108:111], %[o1], %[srd], s63 offen sc1\n\t"
    "s_mov_b32 s63, 0x700000\n\t"
    "buffer_load_dwordx4 v[80:83], %[o0], %[srd], s63 offen sc1\n\t"
    "buffer_load_dwordx4 v[112:115], %[o1], %[srd], s63 offen sc1\n\t"
    "s_mov_b32 s63, 0x8c0000\n\t"
    "buffer_load_dwordx4 v[84:87], %[o0], %[srd], s63 offen sc1\n\t"
    "buffer_load_dwordx4 v[116:119], %[o1], %[srd], s63 offen sc1\n\t"
    "s_mov_b32 s63, 0xa80000\n\t"
    "buffer_load_dwordx4 v[88:91], %[o0], %[srd], s63 offen sc1\n\t"
    "buffer_load_dwordx4 v[120:123], %[o1], %[srd], s63 offen sc1\n\t"
    "s_mov_b32 s63, 0xc40000\n\t"
    "buffer_load_dwordx4 v[92:95], %[o0], %[srd], s63 offen sc1\n\t"
    "buffer_load_dwordx4 v[124:127], %[o1], %[srd], s63 offen sc1\n\t"
    "buffer_load_dwordx4 v[224:227], %[m0], %[rm], 0 offen offset:3584\n\t"
    "buffer_load_dwordx4 v[228:231], %[m0], %[rm], 0 offen offset:3616\n\t"
    "buffer_load_dwordx4 v[232:235], %[m1], %[rm], 0 offen offset:3584\n\t"
    "buffer_load_dwordx4 v[236:239], %[m1], %[rm], 0 offen offset:3616\n\t"
    "buffer_load_dwordx4 v[208:211], %[m0], %[rm], 0 offen offset:1792\n\t"
    "buffer_load_dwordx4 v[212:215], %[m0], %[rm], 0 offen offset:1824\n\t"
    "buffer_load_dwordx4 v[216:219], %[m1], %[rm], 0 offen offset:1792\n\t"
    "buffer_load_dwordx4 v[220:223], %[m1], %[rm], 0 offen offset:1824\n\t"
    "buffer_load_dwordx4 a[184:187], %[m0], %[rm], %[sg] offen offset:0\n\t"
    "buffer_load_dwordx4 a[188:191], %[m0], %[rm], %[sg] offen offset:32\n\t"
    "buffer_load_dwordx4 a[192:195], %[m1], %[rm], %[sg] offen offset:0\n\t"
    "buffer_load_dwordx4 a[196:199], %[m1], %[rm], %[sg] offen offset:32\n\t"
    "buffer_load_dwordx2 a[176:177], %[c0], %[rb], 0 offen offset:0\n\t"
    "buffer_load_dwordx2 a[178:179], %[c0], %[rb], 0 offen offset:16\n\t"
    "buffer_load_dwordx2 a[180:181], %[c1], %[rb], 0 offen offset:0\n\t"
    "buffer_load_dwordx2 a[182:183], %[c1], %[rb], 0 offen offset:16\n\t"
    "global_load_dwordx2 v[40:41], %[vb], %[pb] offset:0\n\t"
    "global_load_dwordx2 v[42:43], %[vb], %[pb] offset:32\n\t"
    "global_load_dwordx2 v[44:45], %[vb], %[pb] offset:64\n\t"
    "global_load_dwordx2 v[46:47], %[vb], %[pb] offset:96\n\t"
    "global_load_dwordx2 v[48:49], %[vb], %[pb] offset:128\n\t"
    "global_load_dwordx2 v[50:51], %[vb], %[pb] offset:160\n\t"
    "global_load_dwordx2 v[52:53], %[vb], %[pb] offset:192\n\t"
    "s_waitcnt vmcnt(35)\n\t"
    "v_lshlrev_b32 v128, 16, v64\n\t"
    "v_and_b32 v129, 0xffff0000, v64\n\t"
    "v_lshlrev_b32 v250, 16, v68\n\t"
    "v_and_b32 v251, 0xffff0000, v68\n\t"
    "v_pk_add_f32 v[128:129], v[128:129], v[250:251]\n\t"
    "v_lshlrev_b32 v130, 16, v65\n\t"
    "v_and_b32 v131, 0xffff0000, v65\n\t"
    "v_lshlrev_b32 v250, 16, v69\n\t"
    "v_and_b32 v251, 0xffff0000, v69\n\t"
    "v_pk_add_f32 v[130:131], v[130:131], v[250:251]\n\t"
    "v_lshlrev_b32 v132, 16, v66\n\t"
    "v_and_b32 v133, 0xffff0000, v66\n\t"
    "v_lshlrev_b32 v250, 16, v70\n\t"
    "v_and_b32 v251, 0xffff0000, v70\n\t"
    "v_pk_add_f32 v[132:133], v[132:133], v[250:251]\n\t"
    "v_lshlrev_b32 v134, 16, v67\n\t"
    "v_and_b32 v135, 0xffff0000, v67\n\t"
    "v_lshlrev_b32 v250, 16, v71\n\t"
    "v_and_b32 v251, 0xffff0000, v71\n\t"
    "v_pk_add_f32 v[134:135], v[134:135], v[250:251]\n\t"
    "v_lshlrev_b32 v136, 16, v96\n\t"
    "v_and_b32 v137, 0xffff0000, v96\n\t"
    "v_lshlrev_b32 v250, 16, v100\n\t"
    "v_and_b32 v251, 0xffff0000, v100\n\t"
    "v_pk_add_f32 v[136:137], v[136:137], v[250:251]\n\t"
    "v_lshlrev_b32 v138, 16, v97\n\t"
    "v_and_b32 v139, 0xffff0000, v97\n\t"
    "v_lshlrev_b32 v250, 16, v101\n\t"
    "v_and_b32 v251, 0xffff0000, v101\n\t"
    "v_pk_add_f32 v[138:139], v[138:139], v[250:251]\n\t"
    "v_lshlrev_b32 v140, 16, v98\n\t"
    "v_and_b32 v141, 0xffff0000, v98\n\t"
    "v_lshlrev_b32 v250, 16, v102\n\t"
    "v_and_b32 v251, 0xffff0000, v102\n\t"
    "v_pk_add_f32 v[140:141], v[140:141], v[250:251]\n\t"
    "v_lshlrev_b32 v142, 16, v99\n\t"
    "v_and_b32 v143, 0xffff0000, v99\n\t"
    "v_lshlrev_b32 v250, 16, v103\n\t"
    "v_and_b32 v251, 0xffff0000, v103\n\t"
    "v_pk_add_f32 v[142:143], v[142:143], v[250:251]\n\t"
    "s_waitcnt vmcnt(31)\n\t"
    "v_lshlrev_b32 v144, 16, v72\n\t"
    "v_and_b32 v145, 0xffff0000, v72\n\t"
    "v_lshlrev_b32 v250, 16, v76\n\t"
    "v_and_b32 v251, 0xffff0000, v76\n\t"
    "v_pk_add_f32 v[144:145], v[144:145], v[250:251]\n\t"
    "v_lshlrev_b32 v146, 16, v73\n\t"
    "v_and_b32 v147, 0xffff0000, v73\n\t"
    "v_lshlrev_b32 v250, 16, v77\n\t"
    "v_and_b32 v251, 0xffff0000, v77\n\t"
    "v_pk_add_f32 v[146:147], v[146:147], v[250:251]\n\t"
    "v_lshlrev_b32 v148, 16, v74\n\t"
    "v_and_b32 v149, 0xffff0000, v74\n\t"
    "v_lshlrev_b32 v250, 16, v78\n\t"
    "v_and_b32 v251, 0xffff0000, v78\n\t"
    "v_pk_add_f32 v[148:149], v[148:149], v[250:251]\n\t"
    "v_lshlrev_b32 v150, 16, v75\n\t"
    "v_and_b32 v151, 0xffff0000, v75\n\t"
    "v_lshlrev_b32 v250, 16, v79\n\t"
    "v_and_b32 v251, 0xffff0000, v79\n\t"
    "v_pk_add_f32 v[150:151], v[150:151], v[250:251]\n\t"
    "v_lshlrev_b32 v152, 16, v104\n\t"
    "v_and_b32 v153, 0xffff0000, v104\n\t"
    "v_lshlrev_b32 v250, 16, v108\n\t"
    "v_and_b32 v251, 0xffff0000, v108\n\t"
    "v_pk_add_f32 v[152:153], v[152:153], v[250:251]\n\t"
    "v_lshlrev_b32 v154, 16, v105\n\t"
    "v_and_b32 v155, 0xffff0000, v105\n\t"
    "v_lshlrev_b32 v250, 16, v109\n\t"
    "v_and_b32 v251, 0xffff0000, v109\n\t"
    "v_pk_add_f32 v[154:155], v[154:155], v[250:251]\n\t"
    "v_lshlrev_b32 v156, 16, v106\n\t"
    "v_and_b32 v157, 0xffff0000, v106\n\t"
    "v_lshlrev_b32 v250, 16, v110\n\t"
    "v_and_b32 v251, 0xffff0000, v110\n\t"
    "v_pk_add_f32 v[156:157], v[156:157], v[250:251]\n\t"
    "v_lshlrev_b32 v158, 16, v107\n\t"
    "v_and_b32 v159, 0xffff0000, v107\n\t"
    "v_lshlrev_b32 v250, 16, v111\n\t"
    "v_and_b32 v251, 0xffff0000, v111\n\t"
    "v_pk_add_f32 v[158:159], v[158:159], v[250:251]\n\t"
    "s_waitcnt vmcnt(27)\n\t"
    "v_lshlrev_b32 v160, 16, v80\n\t"
    "v_and_b32 v161, 0xffff0000, v80\n\t"
    "v_lshlrev_b32 v250, 16, v84\n\t"
    "v_and_b32 v251, 0xffff0000, v84\n\t"
    "v_pk_add_f32 v[160:161], v[160:161], v[250:251]\n\t"
    "v_lshlrev_b32 v162, 16, v81\n\t"
    "v_and_b32 v163, 0xffff0000, v81\n\t"
    "v_lshlrev_b32 v250, 16, v85\n\t"
    "v_and_b32 v251, 0xffff0000, v85\n\t"
    "v_pk_add_f32 v[162:163], v[162:163], v[250:251]\n\t"
    "v_lshlrev_b32 v164, 16, v82\n\t"
    "v_and_b32 v165, 0xffff0000, v82\n\t"
    "v_lshlrev_b32 v250, 16, v86\n\t"
    "v_and_b32 v251, 0xffff0000, v86\n\t"
    "v_pk_add_f32 v[164:165], v[164:165], v[250:251]\n\t"
    "v_lshlrev_b32 v166, 16, v83\n\t"
    "v_and_b32 v167, 0xffff0000, v83\n\t"
    "v_lshlrev_b32 v250, 16, v87\n\t"
    "v_and_b32 v251, 0xffff0000, v87\n\t"
    "v_pk_add_f32 v[166:167], v[166:167], v[250:251]\n\t"
    "v_lshlrev_b32 v168, 16, v112\n\t"
    "v_and_b32 v169, 0xffff0000, v112\n\t"
    "v_lshlrev_b32 v250, 16, v116\n\t"
    "v_and_b32 v251, 0xffff0000, v116\n\t"
    "v_pk_add_f32 v[168:169], v[168:169], v[250:251]\n\t"
    "v_lshlrev_b32 v170, 16, v113\n\t"
    "v_and_b32 v171, 0xffff0000, v113\n\t"
    "v_lshlrev_b32 v250, 16, v117\n\t"
    "v_and_b32 v251, 0xffff0000, v117\n\t"
    "v_pk_add_f32 v[170:171], v[170:171], v[250:251]\n\t"
    "v_lshlrev_b32 v172, 16, v114\n\t"
    "v_and_b32 v173, 0xffff0000, v114\n\t"
    "v_lshlrev_b32 v250, 16, v118\n\t"
    "v_and_b32 v251, 0xffff0000, v118\n\t"
    "v_pk_add_f32 v[172:173], v[172:173], v[250:251]\n\t"
    "v_lshlrev_b32 v174, 16, v115\n\t"
    "v_and_b32 v175, 0xffff0000, v115\n\t"
    "v_lshlrev_b32 v250, 16, v119\n\t"
    "v_and_b32 v251, 0xffff0000, v119\n\t"
    "v_pk_add_f32 v[174:175], v[174:175], v[250:251]\n\t"
    "s_waitcnt vmcnt(23)\n\t"
    "v_lshlrev_b32 v176, 16, v88\n\t"
    "v_and_b32 v177, 0xffff0000, v88\n\t"
    "v_lshlrev_b32 v250, 16, v92\n\t"
    "v_and_b32 v251, 0xffff0000, v92\n\t"
    "v_pk_add_f32 v[176:177], v[176:177], v[250:251]\n\t"
    "v_lshlrev_b32 v178, 16, v89\n\t"
    "v_and_b32 v179, 0xffff0000, v89\n\t"
    "v_lshlrev_b32 v250, 16, v93\n\t"
    "v_and_b32 v251, 0xffff0000, v93\n\t"
    "v_pk_add_f32 v[178:179], v[178:179], v[250:251]\n\t"
    "v_lshlrev_b32 v180, 16, v90\n\t"
    "v_and_b32 v181, 0xffff0000, v90\n\t"
    "v_lshlrev_b32 v250, 16, v94\n\t"
    "v_and_b32 v251, 0xffff0000, v94\n\t"
    "v_pk_add_f32 v[180:181], v[180:181], v[250:251]\n\t"
    "v_lshlrev_b32 v182, 16, v91\n\t"
    "v_and_b32 v183, 0xffff0000, v91\n\t"
    "v_lshlrev_b32 v250, 16, v95\n\t"
    "v_and_b32 v251, 0xffff0000, v95\n\t"
    "v_pk_add_f32 v[182:183], v[182:183], v[250:251]\n\t"
    "v_lshlrev_b32 v184, 16, v120\n\t"
    "v_and_b32 v185, 0xffff0000, v120\n\t"
    "v_lshlrev_b32 v250, 16, v124\n\t"
    "v_and_b32 v251, 0xffff0000, v124\n\t"
    "v_pk_add_f32 v[184:185], v[184:185], v[250:251]\n\t"
    "v_lshlrev_b32 v186, 16, v121\n\t"
    "v_and_b32 v187, 0xffff0000, v121\n\t"
    "v_lshlrev_b32 v250, 16, v125\n\t"
    "v_and_b32 v251, 0xffff0000, v125\n\t"
    "v_pk_add_f32 v[186:187], v[186:187], v[250:251]\n\t"
    "v_lshlrev_b32 v188, 16, v122\n\t"
    "v_and_b32 v189, 0xffff0000, v122\n\t"
    "v_lshlrev_b32 v250, 16, v126\n\t"
    "v_and_b32 v251, 0xffff0000, v126\n\t"
    "v_pk_add_f32 v[188:189], v[188:189], v[250:251]\n\t"
    "v_lshlrev_b32 v190, 16, v123\n\t"
    "v_and_b32 v191, 0xffff0000, v123\n\t"
    "v_lshlrev_b32 v250, 16, v127\n\t"
    "v_and_b32 v251, 0xffff0000, v127\n\t"
    "v_pk_add_f32 v[190:191], v[190:191], v[250:251]\n\t"
    "s_waitcnt vmcnt(15)\n\t"
    "s_branch SF_%=\n\t"
    "SB_%=:\n\t"
    "s_bitcmp1_b32 s62, 0\n\t"
    "s_cbranch_scc0 SG0_%=\n\t"
    "s_mov_b32 s63, 0x0\n\t"
    "buffer_load_dwordx4 v[64:67], %[o0], %[srd], s63 offen sc1\n\t"
    "buffer_load_dwordx4 v[96:99], %[o1], %[srd], s63 offen sc1\n\t"
    "SG0_%=:\n\t"
    "s_bitcmp1_b32 s62, 1\n\t"
    "s_cbranch_scc0 SG1_%=\n\t"
    "s_mov_b32 s63, 0x1c0000\n\t"
    "buffer_load_dwordx4 v[68:71], %[o0], %[srd], s63 offen sc1\n\t"
    "buffer_load_dwordx4 v[100:103], %[o1], %[srd], s63 offen sc1\n\t"
    "SG1_%=:\n\t"
    "s_bitcmp1_b32 s62, 2\n\t"
    "s_cbranch_scc0 SG2_%=\n\t"
    "s_mov_b32 s63, 0x380000\n\t"
    "buffer_load_dwordx4 v[72:75], %[o0], %[srd], s63 offen sc1\n\t"
    "buffer_load_dwordx4 v[104:107], %[o1], %[srd], s63 offen sc1\n\t"
    "SG2_%=:\n\t"
    "s_bitcmp1_b32 s62, 3\n\t"
    "s_cbranch_scc0 SG3_%=\n\t"
    "s_mov_b32 s63, 0x540000\n\t"
    "buffer_load_dwordx4 v[76:79], %[o0], %[srd], s63 offen sc1\n\t"
    "buffer_load_dwordx4 v[108:111], %[o1], %[srd], s63 offen sc1\n\t"
    "SG3_%=:\n\t"
    "s_bitcmp1_b32 s62, 4\n\t"
    "s_cbranch_scc0 SG4_%=\n\t"
    "s_mov_b32 s63, 0x700000\n\t"
    "buffer_load_dwordx4 v[80:83], %[o0], %[srd], s63 offen sc1\n\t"
    "buffer_load_dwordx4 v[112:115], %[o1], %[srd], s63 offen sc1\n\t"
    "SG4_%=:\n\t"
    "s_bitcmp1_b32 s62, 5\n\t"
    "s_cbranch_scc0 SG5_%=\n\t"
    "s_mov_b32 s63, 0x8c0000\n\t"
    "buffer_load_dwordx4 v[84:87], %[o0], %[srd], s63 offen sc1\n\t"
    "buffer_load_dwordx4 v[116:119], %[o1], %[srd], s63 offen sc1\n\t"
    "SG5_%=:\n\t"
    "s_bitcmp1_b32 s62, 6\n\t"
    "s_cbranch_scc0 SG6_%=\n\t"
    "s_mov_b32 s63, 0xa80000\n\t"
    "buffer_load_dwordx4 v[88:91], %[o0], %[srd], s63 offen sc1\n\t"
    "buffer_load_dwordx4 v[120:123], %[o1], %[srd], s63 offen sc1\n\t"
    "SG6_%=:\n\t"
    "s_bitcmp1_b32 s62, 7\n\t"
    "s_cbranch_scc0 SG7_%=\n\t"
    "s_mov_b32 s63, 0xc40000\n\t"
    "buffer_load_dwordx4 v[92:95], %[o0], %[srd], s63 offen sc1\n\t"
    "buffer_load_dwordx4 v[124:127], %[o1], %[srd], s63 offen sc1\n\t"
    "SG7_%=:\n\t"
    "s_cmp_lg_u32 s60, 0\n\t"
    "s_cbranch_scc1 SX_%=\n\t"
    "buffer_load_dwordx4 v[224:227], %[m0], %[rm], 0 offen offset:3584\n\t"
    "buffer_load_dwordx4 v[228:231], %[m0], %[rm], 0 offen offset:3616\n\t"
    "buffer_load_dwordx4 v[232:235], %[m1], %[rm], 0 offen offset:3584\n\t"
    "buffer_load_dwordx4 v[236:239], %[m1], %[rm], 0 offen offset:3616\n\t"
    "buffer_load_dwordx4 v[208:211], %[m0], %[rm], 0 offen offset:1792\n\t"
    "buffer_load_dwordx4 v[212:215], %[m0], %[rm], 0 offen offset:1824\n\t"
    "buffer_load_dwordx4 v[216:219], %[m1], %[rm], 0 offen offset:1792\n\t"
    "buffer_load_dwordx4 v[220:223], %[m1], %[rm], 0 offen offset:1824\n\t"
    "buffer_load_dwordx4 a[184:187], %[m0], %[rm], %[sg] offen offset:0\n\t"
    "buffer_load_dwordx4 a[188:191], %[m0], %[rm], %[sg] offen offset:32\n\t"
    "buffer_load_dwordx4 a[192:195], %[m1], %[rm], %[sg] offen offset:0\n\t"
    "buffer_load_dwordx4 a[196:199], %[m1], %[rm], %[sg] offen offset:32\n\t"
    "buffer_load_dwordx2 a[176:177], %[c0], %[rb], 0 offen offset:0\n\t"
    "buffer_load_dwordx2 a[178:179], %[c0], %[rb], 0 offen offset:16\n\t"
    "buffer_load_dwordx2 a[180:181], %[c1], %[rb], 0 offen offset:0\n\t"
    "buffer_load_dwordx2 a[182:183], %[c1], %[rb], 0 offen offset:16\n\t"
    "global_load_dwordx2 v[40:41], %[vb], %[pb] offset:0\n\t"
    "global_load_dwordx2 v[42:43], %[vb], %[pb] offset:32\n\t"
    "global_load_dwordx2 v[44:45], %[vb], %[pb] offset:64\n\t"
    "global_load_dwordx2 v[46:47], %[vb], %[pb] offset:96\n\t"
    "global_load_dwordx2 v[48:49], %[vb], %[pb] offset:128\n\t"
    "global_load_dwordx2 v[50:51], %[vb], %[pb] offset:160\n\t"
    "global_load_dwordx2 v[52:53], %[vb], %[pb] offset:192\n\t"
    "SX_%=:\n\t"
    "global_load_dword v248, %[fo], %[fl] sc1\n\t"
    "s_or_b32 s60, s60, s62\n\t"
    "s_lshr_b32 s62, s60, 1\n\t"
    "s_and_b32 s62, s62, s60\n\t"
    "s_andn2_b32 s62, s62, s64\n\t"
    "s_or_b32 s64, s64, s62\n\t"
    "s_waitcnt vmcnt(1)\n\t"
    "s_bitcmp1_b32 s62, 0\n\t"
    "s_cbranch_scc0 SP0_%=\n\t"
    "v_lshlrev_b32 v128, 16, v64\n\t"
    "v_and_b32 v129, 0xffff0000, v64\n\t"
    "v_lshlrev_b32 v250, 16, v68\n\t"
    "v_and_b32 v251, 0xffff0000, v68\n\t"
    "v_pk_add_f32 v[128:129], v[128:129], v[250:251]\n\t"
    "v_lshlrev_b32 v130, 16, v65\n\t"
    "v_and_b32 v131, 0xffff0000, v65\n\t"
    "v_lshlrev_b32 v250, 16, v69\n\t"
    "v_and_b32 v251, 0xffff0000, v69\n\t"
    "v_pk_add_f32 v[130:131], v[130:131], v[250:251]\n\t"
    "v_lshlrev_b32 v132, 16, v66\n\t"
    "v_and_b32 v133, 0xffff0000, v66\n\t"
    "v_lshlrev_b32 v250, 16, v70\n\t"
    "v_and_b32 v251, 0xffff0000, v70\n\t"
    "v_pk_add_f32 v[132:133], v[132:133], v[250:251]\n\t"
    "v_lshlrev_b32 v134, 16, v67\n\t"
    "v_and_b32 v135, 0xffff0000, v67\n\t"
    "v_lshlrev_b32 v250, 16, v71\n\t"
    "v_and_b32 v251, 0xffff0000, v71\n\t"
    "v_pk_add_f32 v[134:135], v[134:135], v[250:251]\n\t"
    "v_lshlrev_b32 v136, 16, v96\n\t"
    "v_and_b32 v137, 0xffff0000, v96\n\t"
    "v_lshlrev_b32 v250, 16, v100\n\t"
    "v_and_b32 v251, 0xffff0000, v100\n\t"
    "v_pk_add_f32 v[136:137], v[136:137], v[250:251]\n\t"
    "v_lshlrev_b32 v138, 16, v97\n\t"
    "v_and_b32 v139, 0xffff0000, v97\n\t"
    "v_lshlrev_b32 v250, 16, v101\n\t"
    "v_and_b32 v251, 0xffff0000, v101\n\t"
    "v_pk_add_f32 v[138:139], v[138:139], v[250:251]\n\t"
    "v_lshlrev_b32 v140, 16, v98\n\t"
    "v_and_b32 v141, 0xffff0000, v98\n\t"
    "v_lshlrev_b32 v250, 16, v102\n\t"
    "v_and_b32 v251, 0xffff0000, v102\n\t"
    "v_pk_add_f32 v[140:141], v[140:141], v[250:251]\n\t"
    "v_lshlrev_b32 v142, 16, v99\n\t"
    "v_and_b32 v143, 0xffff0000, v99\n\t"
    "v_lshlrev_b32 v250, 16, v103\n\t"
    "v_and_b32 v251, 0xffff0000, v103\n\t"
    "v_pk_add_f32 v[142:143], v[142:143], v[250:251]\n\t"
    "SP0_%=:\n\t"
    "s_bitcmp1_b32 s62, 2\n\t"
    "s_cbranch_scc0 SP1_%=\n\t"
    "v_lshlrev_b32 v144, 16, v72\n\t"
    "v_and_b32 v145, 0xffff0000, v72\n\t"
    "v_lshlrev_b32 v250, 16, v76\n\t"
    "v_and_b32 v251, 0xffff0000, v76\n\t"
    "v_pk_add_f32 v[144:145], v[144:145], v[250:251]\n\t"
    "v_lshlrev_b32 v146, 16, v73\n\t"
    "v_and_b32 v147, 0xffff0000, v73\n\t"
    "v_lshlrev_b32 v250, 16, v77\n\t"
    "v_and_b32 v251, 0xffff0000, v77\n\t"
    "v_pk_add_f32 v[146:147], v[146:147], v[250:251]\n\t"
    "v_lshlrev_b32 v148, 16, v74\n\t"
    "v_and_b32 v149, 0xffff0000, v74\n\t"
    "v_lshlrev_b32 v250, 16, v78\n\t"
    "v_and_b32 v251, 0xffff0000, v78\n\t"
    "v_pk_add_f32 v[148:149], v[148:149], v[250:251]\n\t"
    "v_lshlrev_b32 v150, 16, v75\n\t"
    "v_and_b32 v151, 0xffff0000, v75\n\t"
    "v_lshlrev_b32 v250, 16, v79\n\t"
    "v_and_b32 v251, 0xffff0000, v79\n\t"
    "v_pk_add_f32 v[150:151], v[150:151], v[250:251]\n\t"
    "v_lshlrev_b32 v152, 16, v104\n\t"
    "v_and_b32 v153, 0xffff0000, v104\n\t"
    "v_lshlrev_b32 v250, 16, v108\n\t"
    "v_and_b32 v251, 0xffff0000, v108\n\t"
    "v_pk_add_f32 v[152:153], v[152:153], v[250:251]\n\t"
    "v_lshlrev_b32 v154, 16, v105\n\t"
    "v_and_b32 v155, 0xffff0000, v105\n\t"
    "v_lshlrev_b32 v250, 16, v109\n\t"
    "v_and_b32 v251, 0xffff0000, v109\n\t"
    "v_pk_add_f32 v[154:155], v[154:155], v[250:251]\n\t"
    "v_lshlrev_b32 v156, 16, v106\n\t"
    "v_and_b32 v157, 0xffff0000, v106\n\t"
    "v_lshlrev_b32 v250, 16, v110\n\t"
    "v_and_b32 v251, 0xffff0000, v110\n\t"
    "v_pk_add_f32 v[156:157], v[156:157], v[250:251]\n\t"
    "v_lshlrev_b32 v158, 16, v107\n\t"
    "v_and_b32 v159, 0xffff0000, v107\n\t"
    "v_lshlrev_b32 v250, 16, v111\n\t"
    "v_and_b32 v251, 0xffff0000, v111\n\t"
    "v_pk_add_f32 v[158:159], v[158:159], v[250:251]\n\t"
    "SP1_%=:\n\t"
    "s_bitcmp1_b32 s62, 4\n\t"
    "s_cbranch_scc0 SP2_%=\n\t"
    "v_lshlrev_b32 v160, 16, v80\n\t"
    "v_and_b32 v161, 0xffff0000, v80\n\t"
    "v_lshlrev_b32 v250, 16, v84\n\t"
    "v_and_b32 v251, 0xffff0000, v84\n\t"
    "v_pk_add_f32 v[160:161], v[160:161], v[250:251]\n\t"
    "v_lshlrev_b32 v162, 16, v81\n\t"
    "v_and_b32 v163, 0xffff0000, v81\n\t"
    "v_lshlrev_b32 v250, 16, v85\n\t"
    "v_and_b32 v251, 0xffff0000, v85\n\t"
    "v_pk_add_f32 v[162:163], v[162:163], v[250:251]\n\t"
    "v_lshlrev_b32 v164, 16, v82\n\t"
    "v_and_b32 v165, 0xffff0000, v82\n\t"
    "v_lshlrev_b32 v250, 16, v86\n\t"
    "v_and_b32 v251, 0xffff0000, v86\n\t"
    "v_pk_add_f32 v[164:165], v[164:165], v[250:251]\n\t"
    "v_lshlrev_b32 v166, 16, v83\n\t"
    "v_and_b32 v167, 0xffff0000, v83\n\t"
    "v_lshlrev_b32 v250, 16, v87\n\t"
    "v_and_b32 v251, 0xffff0000, v87\n\t"
    "v_pk_add_f32 v[166:167], v[166:167], v[250:251]\n\t"
    "v_lshlrev_b32 v168, 16, v112\n\t"
    "v_and_b32 v169, 0xffff0000, v112\n\t"
    "v_lshlrev_b32 v250, 16, v116\n\t"
    "v_and_b32 v251, 0xffff0000, v116\n\t"
    "v_pk_add_f32 v[168:169], v[168:169], v[250:251]\n\t"
    "v_lshlrev_b32 v170, 16, v113\n\t"
    "v_and_b32 v171, 0xffff0000, v113\n\t"
    "v_lshlrev_b32 v250, 16, v117\n\t"
    "v_and_b32 v251, 0xffff0000, v117\n\t"
    "v_pk_add_f32 v[170:171], v[170:171], v[250:251]\n\t"
    "v_lshlrev_b32 v172, 16, v114\n\t"
    "v_and_b32 v173, 0xffff0000, v114\n\t"
    "v_lshlrev_b32 v250, 16, v118\n\t"
    "v_and_b32 v251, 0xffff0000, v118\n\t"
    "v_pk_add_f32 v[172:173], v[172:173], v[250:251]\n\t"
    "v_lshlrev_b32 v174, 16, v115\n\t"
    "v_and_b32 v175, 0xffff0000, v115\n\t"
    "v_lshlrev_b32 v250, 16, v119\n\t"
    "v_and_b32 v251, 0xffff0000, v119\n\t"
    "v_pk_add_f32 v[174:175], v[174:175], v[250:251]\n\t"
    "SP2_%=:\n\t"
    "s_bitcmp1_b32 s62, 6\n\t"
    "s_cbranch_scc0 SP3_%=\n\t"
    "v_lshlrev_b32 v176, 16, v88\n\t"
    "v_and_b32 v177, 0xffff0000, v88\n\t"
    "v_lshlrev_b32 v250, 16, v92\n\t"
    "v_and_b32 v251, 0xffff0000, v92\n\t"
    "v_pk_add_f32 v[176:177], v[176:177], v[250:251]\n\t"
    "v_lshlrev_b32 v178, 16, v89\n\t"
    "v_and_b32 v179, 0xffff0000, v89\n\t"
    "v_lshlrev_b32 v250, 16, v93\n\t"
    "v_and_b32 v251, 0xffff0000, v93\n\t"
    "v_pk_add_f32 v[178:179], v[178:179], v[250:251]\n\t"
    "v_lshlrev_b32 v180, 16, v90\n\t"
    "v_and_b32 v181, 0xffff0000, v90\n\t"
    "v_lshlrev_b32 v250, 16, v94\n\t"
    "v_and_b32 v251, 0xffff0000, v94\n\t"
    "v_pk_add_f32 v[180:181], v[180:181], v[250:251]\n\t"
    "v_lshlrev_b32 v182, 16, v91\n\t"
    "v_and_b32 v183, 0xffff0000, v91\n\t"
    "v_lshlrev_b32 v250, 16, v95\n\t"
    "v_and_b32 v251, 0xffff0000, v95\n\t"
    "v_pk_add_f32 v[182:183], v[182:183], v[250:251]\n\t"
    "v_lshlrev_b32 v184, 16, v120\n\t"
    "v_and_b32 v185, 0xffff0000, v120\n\t"
    "v_lshlrev_b32 v250, 16, v124\n\t"
    "v_and_b32 v251, 0xffff0000, v124\n\t"
    "v_pk_add_f32 v[184:185], v[184:185], v[250:251]\n\t"
    "v_lshlrev_b32 v186, 16, v121\n\t"
    "v_and_b32 v187, 0xffff0000, v121\n\t"
    "v_lshlrev_b32 v250, 16, v125\n\t"
    "v_and_b32 v251, 0xffff0000, v125\n\t"
    "v_pk_add_f32 v[186:187], v[186:187], v[250:251]\n\t"
    "v_lshlrev_b32 v188, 16, v122\n\t"
    "v_and_b32 v189, 0xffff0000, v122\n\t"
    "v_lshlrev_b32 v250, 16, v126\n\t"
    "v_and_b32 v251, 0xffff0000, v126\n\t"
    "v_pk_add_f32 v[188:189], v[188:189], v[250:251]\n\t"
    "v_lshlrev_b32 v190, 16, v123\n\t"
    "v_and_b32 v191, 0xffff0000, v123\n\t"
    "v_lshlrev_b32 v250, 16, v127\n\t"
    "v_and_b32 v251, 0xffff0000, v127\n\t"
    "v_pk_add_f32 v[190:191], v[190:191], v[250:251]\n\t"
    "SP3_%=:\n\t"
    "s_cmp_eq_u32 s60, 0xff\n\t"
    "s_cbranch_scc1 SW_%=\n\t"
    "global_load_dword v249, %[fo], %[fl] sc1\n\t"
    "s_waitcnt vmcnt(1)\n\t"
    "v_cmp_ne_u32 vcc, 0, v248\n\t"
    "s_andn2_b32 s62, vcc_lo, s60\n\t"
    "s_and_b32 s62, s62, 0xff\n\t"
    "s_cbranch_scc1 SB_%=\n\t"
    "s_sleep 4\n\t"
    "s_branch SM_%=\n\t"
    "SA_%=:\n\t"
    "global_load_dword v249, %[fo], %[fl] sc1\n\t"
    "s_waitcnt vmcnt(1)\n\t"
    "v_cmp_ne_u32 vcc, 0, v248\n\t"
    "s_andn2_b32 s62, vcc_lo, s60\n\t"
    "s_and_b32 s62, s62, 0xff\n\t"
    "s_cbranch_scc1 SB_%=\n\t"
    "SM_%=:\n\t"
    "global_load_dword v248, %[fo], %[fl] sc1\n\t"
    "s_waitcnt vmcnt(1)\n\t"
    "v_cmp_ne_u32 vcc, 0, v249\n\t"
    "s_andn2_b32 s62, vcc_lo, s60\n\t"
    "s_and_b32 s62, s62, 0xff\n\t"
    "s_cbranch_scc1 SB_%=\n\t"
    "s_sub_u32 s61, s61, 1\n\t"
    "s_cbranch_scc0 SA_%=\n\t"
    "SW_%=:\n\t"
    "s_waitcnt vmcnt(0)\n\t"
    "SF_%=:\n\t"
    "v_pk_add_f32 v[128:129], v[128:129], v[144:145]\n\t"
    "v_pk_add_f32 v[130:131], v[130:131], v[146:147]\n\t"
    "v_pk_add_f32 v[132:133], v[132:133], v[148:149]\n\t"
    "v_pk_add_f32 v[134:135], v[134:135], v[150:151]\n\t"
    "v_pk_add_f32 v[136:137], v[136:137], v[152:153]\n\t"
    "v_pk_add_f32 v[138:139], v[138:139], v[154:155]\n\t"
    "v_pk_add_f32 v[140:141], v[140:141], v[156:157]\n\t"
    "v_pk_add_f32 v[142:143], v[142:143], v[158:159]\n\t"
    "v_pk_add_f32 v[160:161], v[160:161], v[176:177]\n\t"
    "v_pk_add_f32 v[162:163], v[162:163], v[178:179]\n\t"
    "v_pk_add_f32 v[164:165], v[164:165], v[180:181]\n\t"
    "v_pk_add_f32 v[166:167], v[166:167], v[182:183]\n\t"
    "v_pk_add_f32 v[168:169], v[168:169], v[184:185]\n\t"
    "v_pk_add_f32 v[170:171], v[170:171], v[186:187]\n\t"
    "v_pk_add_f32 v[172:173], v[172:173], v[188:189]\n\t"
    "v_pk_add_f32 v[174:175], v[174:175], v[190:191]\n\t"
    "v_pk_add_f32 v[128:129], v[128:129], v[160:161]\n\t"
    "v_pk_add_f32 v[130:131], v[130:131], v[162:163]\n\t"
    "v_pk_add_f32 v[132:133], v[132:133], v[164:165]\n\t"
    "v_pk_add_f32 v[134:135], v[134:135], v[166:167]\n\t"
    "v_pk_add_f32 v[136:137], v[136:137], v[168:169]\n\t"
    "v_pk_add_f32 v[138:139], v[138:139], v[170:171]\n\t"
    "v_pk_add_f32 v[140:141], v[140:141], v[172:173]\n\t"
    "v_pk_add_f32 v[142:143], v[142:143], v[174:175]\n\t"
    : "+{v248}"(pf), "+{v[240:247]}"(xr), "+{v[192:207]}"(mg), "=&{v[208:223]}"(ms), "=&{v[224:239]}"(mc), "=&{a[184:199]}"(gv), "=&{a[176:183]}"(br), "=&{v[40:55]}"(bC), "=&{v[128:143]}"(fs)
    : [fo] "v"(q8 * 4u), [fl] "s"(fl), [flo] "s"(fl + g), [wid] "s"(w), [own] "s"(own), [o0] "v"(fo0), [o1] "v"(fo1), [srd] "s"(mkrsrc(ws + WS_PART, 8u * (unsigned)FBYTES)),
      [m0] "v"(c0 * 4u), [m1] "v"(c0 * 4u + 1024u + oob), [rm] "s"(rm), [sg] "s"(3u * DM * 4u), [c0] "v"(c0 * 2u), [c1] "v"(c0 * 2u + 512u + oob), [rb] "s"(mkrsrc(b2, DM * 2u)), [vb] "v"((m >> 4) * 8u), [pb] "s"(b1 + g * 448u + hs * 224u)
    : "v64", "v65", "v66", "v67", "v68", "v69", "v70", "v71", "v72", "v73", "v74", "v75", "v76", "v77", "v78", "v79", "v80", "v81", "v82", "v83", "v84", "v85", "v86", "v87", "v88", "v89", "v90", "v91", "v92", "v93", "v94", "v95", "v96", "v97", "v98", "v99", "v100", "v101", "v102", "v103", "v104", "v105", "v106", "v107", "v108", "v109", "v110", "v111", "v112", "v113", "v114", "v115", "v116", "v117", "v118", "v119", "v120", "v121", "v122", "v123", "v124", "v125", "v126", "v127", "v144", "v145", "v146", "v147", "v148", "v149", "v150", "v151", "v152", "v153", "v154", "v155", "v156", "v157", "v158", "v159", "v160", "v161", "v162", "v163", "v164", "v165", "v166", "v167", "v168", "v169", "v170", "v171", "v172", "v173", "v174", "v175", "v176", "v177", "v178", "v179", "v180", "v181", "v182", "v183", "v184", "v185", "v186", "v187", "v188", "v189", "v190", "v191", "v249", "v250", "v251", "s60", "s61", "s62", "s63", "s64", "s65", "vcc", "scc", "memory"
  );
  f32x2 f[8];
#pragma unroll
  for (int e = 0; e < 8; ++e) f[e] = up2(xr[e]) + f32x2{mg[2 * e], mg[2 * e + 1]} * f32x2{fs[2 * e], fs[2 * e + 1]};
  f32x2* red = (f32x2*)(lds + 159744);
  f32x2 y[8];
  const f32x2 t1 = ln_stats(f, red, m, w);
#pragma unroll
  for (int e = 0; e < 8; ++e) y[e] = (f[e] * t1[0] + t1[1]) * f32x2{mc[2 * e], mc[2 * e + 1]} + f32x2{ms[2 * e], ms[2 * e + 1]};
  const f32x2 t2 = ln_stats(y, red + 32, m, w);
#pragma unroll
  for (int e = 0; e < 8; ++e) y[e] = y[e] * t2[0] + t2[1];
  {
    const __amdgpu_buffer_rsrc_t sy = mkrsrc(ws + WS_Y, (unsigned)FBYTES);
    __builtin_amdgcn_raw_buffer_store_b128(pack8(y), sy, fo0, 0, 16);
    __builtin_amdgcn_raw_buffer_store_b128(pack8(y + 4), sy, fo1, 0, 16);
  }
  __builtin_amdgcn_sched_barrier(0);
  asm volatile("s_waitcnt vmcnt(0)\n\ts_barrier\n\ts_cmp_lg_u32 %[wid], 0\n\ts_cbranch_scc1 SY_%=\n\ts_mov_b64 exec, 1\n\t"
               "global_atomic_add %[fo], %[one], %[fl]\n\ts_mov_b64 exec, -1\nSY_%=:"
               : "+{a[184:199]}"(gv), "+{a[176:183]}"(br), "+{v[40:55]}"(bC) : [wid] "s"(w), [fo] "v"(q8 * 4u), [one] "v"(1u), [fl] "s"(fl + 32) : "scc", "memory");
  f32x16 ob;
#pragma unroll
  for (int e = 0; e < 8; ++e) { const f32x2 t = f[e] + f32x2{gv[2 * e], gv[2 * e + 1]} * up2(br[e]); ob[2 * e] = t[0]; ob[2 * e + 1] = t[1]; }
  asm volatile("" : "+a"(ob));
  asm volatile(
    "s_nop 4\n\t"
    "s_mov_b32 s60, %[wid]\n\t"
    "s_mul_i32 s63, s60, 7168\n\t"
    "s_add_u32 s63, s63, %[ldsb]\n\t"
    "s_mul_i32 s65, s60, 57344\n\t"
    "s_mul_i32 s66, s60, 50176\n\t"
    "s_add_u32 m0, s63, 0x10000\n\t"
    "s_add_u32 s61, s65, 0x70000\n\t"
    "buffer_load_dwordx4 v8, %[srdw1], s61 offen lds\n\t"
    "buffer_load_dwordx4 v9, %[srdw1], s61 offen offset:1024 lds\n\t"
    "buffer_load_dwordx4 v10, %[srdw1], s61 offen offset:2048 lds\n\t"
    "buffer_load_dwordx4 v11, %[srdw1], s61 offen offset:3072 lds\n\t"
    "s_add_u32 m0, s63, 0x11000\n\t"
    "s_nop 0\n\t"
    "buffer_load_dwordx4 v12, %[srdw1], s61 offen lds\n\t"
    "buffer_load_dwordx4 v13, %[srdw1], s61 offen offset:1024 lds\n\t"
    "buffer_load_dwordx4 v14, %[srdw1], s61 offen offset:2048 lds\n\t"
    "s_add_u32 m0, s63, 0x0\n\t"
    "s_add_u32 s61, s65, 0xa8000\n\t"
    "buffer_load_dwordx4 v8, %[srdw1], s61 offen lds\n\t"
    "buffer_load_dwordx4 v9, %[srdw1], s61 offen offset:1024 lds\n\t"
    "buffer_load_dwordx4 v10, %[srdw1], s61 offen offset:2048 lds\n\t"
    "buffer_load_dwordx4 v11, %[srdw1], s61 offen offset:3072 lds\n\t"
    "s_add_u32 m0, s63, 0x1000\n\t"
    "s_nop 0\n\t"
    "buffer_load_dwordx4 v12, %[srdw1], s61 offen lds\n\t"
    "buffer_load_dwordx4 v13, %[srdw1], s61 offen offset:1024 lds\n\t"
    "buffer_load_dwordx4 v14, %[srdw1], s61 offen offset:2048 lds\n\t"
    "v_add_u32 v160, 0x20000, v20\n\t"
    "v_add_u32 v161, 0x20000, v21\n\t"
    "v_add_u32 v170, 0x20000, v24\n\t"
    "v_add_u32 v171, 0x20000, v25\n\t"
    "v_add_u32 v172, 0x20000, v26\n\t"
    "v_add_u32 v173, 0x20000, v27\n\t"
    "v_add_u32 v162, 0x18000, v20\n\t"
    "v_add_u32 v163, 0x18000, v21\n\t"
    "v_add_u32 v174, 0x18000, v24\n\t"
    "v_add_u32 v175, 0x18000, v25\n\t"
    "v_add_u32 v176, 0x18000, v26\n\t"
    "v_add_u32 v177, 0x18000, v27\n\t"
    "v_add_u32 v164, 0x10000, v20\n\t"
    "v_add_u32 v165, 0x10000, v21\n\t"
    "v_add_u32 v178, 0x10000, v24\n\t"
    "v_add_u32 v179, 0x10000, v25\n\t"
    "v_add_u32 v180, 0x10000, v26\n\t"
    "v_add_u32 v181, 0x10000, v27\n\t"
    "v_add_u32 v166, 0x0, v20\n\t"
    "v_add_u32 v167, 0x0, v21\n\t"
    "v_add_u32 v182, 0x0, v24\n\t"
    "v_add_u32 v183, 0x0, v25\n\t"
    "v_add_u32 v184, 0x0, v26\n\t"
    "v_add_u32 v185, 0x0, v27\n\t"
    "v_add_u32 v168, 0x8000, v20\n\t"
    "v_add_u32 v169, 0x8000, v21\n\t"
    "v_add_u32 v186, 0x8000, v24\n\t"
    "v_add_u32 v187, 0x8000, v25\n\t"
    "v_add_u32 v188, 0x8000, v26\n\t"
    "v_add_u32 v189, 0x8000, v27\n\t"
    "ds_read_b64_tr_b16 a[0:1], v160\n\t"
    "ds_read_b64_tr_b16 a[2:3], v160 offset:3584\n\t"
    "ds_read_b64_tr_b16 a[4:5], v161 offset:32\n\t"
    "ds_read_b64_tr_b16 a[6:7], v161 offset:3616\n\t"
    "ds_read_b64_tr_b16 a[8:9], v160 offset:64\n\t"
    "ds_read_b64_tr_b16 a[10:11], v160 offset:3648\n\t"
    "ds_read_b64_tr_b16 a[12:13], v161 offset:96\n\t"
    "ds_read_b64_tr_b16 a[14:15], v161 offset:3680\n\t"
    "ds_read_b64_tr_b16 a[16:17], v160 offset:128\n\t"
    "ds_read_b64_tr_b16 a[18:19], v160 offset:3712\n\t"
    "ds_read_b64_tr_b16 a[20:21], v161 offset:160\n\t"
    "ds_read_b64_tr_b16 a[22:23], v161 offset:3744\n\t"
    "v_mov_b32 v56, 0xbdd2d3e8\n\t"
    "v_mov_b32 v57, 0xbdd2d3e8\n\t"
    "v_mov_b32 v58, 0xc0135761\n\t"
    "v_mov_b32 v59, 0xc0135761\n\t"
    "v_mov_b32 v60, 0x3f800000\n\t"
    "v_mov_b32 v61, 0x3f800000\n\t"
    "v_lshlrev_b32 v76, 16, v40\n\t"
    "v_and_b32 v77, 0xffff0000, v40\n\t"
    "v_lshlrev_b32 v78, 16, v41\n\t"
    "v_and_b32 v79, 0xffff0000, v41\n\t"
    "v_lshlrev_b32 v80, 16, v42\n\t"
    "v_and_b32 v81, 0xffff0000, v42\n\t"
    "v_lshlrev_b32 v82, 16, v43\n\t"
    "v_and_b32 v83, 0xffff0000, v43\n\t"
    "v_lshlrev_b32 v84, 16, v44\n\t"
    "v_and_b32 v85, 0xffff0000, v44\n\t"
    "v_lshlrev_b32 v86, 16, v45\n\t"
    "v_and_b32 v87, 0xffff0000, v45\n\t"
    "v_lshlrev_b32 v88, 16, v46\n\t"
    "v_and_b32 v89, 0xffff0000, v46\n\t"
    "v_lshlrev_b32 v90, 16, v47\n\t"
    "v_and_b32 v91, 0xffff0000, v47\n\t"
    "v_lshlrev_b32 v92, 16, v48\n\t"
    "v_and_b32 v93, 0xffff0000, v48\n\t"
    "v_lshlrev_b32 v94, 16, v49\n\t"
    "v_and_b32 v95, 0xffff0000, v49\n\t"
    "v_lshlrev_b32 v96, 16, v50\n\t"
    "v_and_b32 v97, 0xffff0000, v50\n\t"
    "v_lshlrev_b32 v98, 16, v51\n\t"
    "v_and_b32 v99, 0xffff0000, v51\n\t"
    "v_lshlrev_b32 v100, 16, v52\n\t"
    "v_and_b32 v101, 0xffff0000, v52\n\t"
    "v_lshlrev_b32 v102, 16, v53\n\t"
    "v_and_b32 v103, 0xffff0000, v53\n\t"
    "v_mov_b32 v192, 0\n\t"
    "s_cmp_lg_u32 s60, 0\n\t"
    "s_cbranch_scc1 LPD_%=\n\t"
    "s_mov_b32 s62, 0x200000\n\t"
    "global_load_dword v193, v192, %[flag] sc1\n\t"
    "s_sleep 4\n\t"
    "LPL_%=:\n\t"
    "global_load_dword v194, v192, %[flag] sc1\n\t"
    "s_waitcnt vmcnt(1)\n\t"
    "v_readfirstlane_b32 s61, v193\n\t"
    "s_cmp_ge_u32 s61, 8\n\t"
    "s_cbranch_scc1 LPD_%=\n\t"
    "global_load_dword v193, v192, %[flag] sc1\n\t"
    "s_waitcnt vmcnt(1)\n\t"
    "v_readfirstlane_b32 s61, v194\n\t"
    "s_cmp_ge_u32 s61, 8\n\t"
    "s_cbranch_scc1 LPD_%=\n\t"
    "s_sub_u32 s62, s62, 1\n\t"
    "s_cbranch_scc0 LPL_%=\n\t"
    "LPD_%=:\n\t"
    "s_barrier\n\t"
    "buffer_load_dwordx4 a[64:67], v23, %[srdy], 0 offen sc1\n\t"
    "buffer_load_dwordx4 a[68:71], v23, %[srdy], 0 offen offset:256 sc1\n\t"
    "buffer_load_dwordx4 a[72:75], v23, %[srdy], 0 offen offset:2048 sc1\n\t"
    "buffer_load_dwordx4 a[76:79], v23, %[srdy], 0 offen offset:2304 sc1\n\t"
    "s_movk_i32 s62, 0x1000\n\t"
    "buffer_load_dwordx4 a[80:83], v23, %[srdy], s62 offen sc1\n\t"
    "buffer_load_dwordx4 a[84:87], v23, %[srdy], s62 offen offset:256 sc1\n\t"
    "buffer_load_dwordx4 a[88:91], v23, %[srdy], s62 offen offset:2048 sc1\n\t"
    "buffer_load_dwordx4 a[92:95], v23, %[srdy], s62 offen offset:2304 sc1\n\t"
    "s_movk_i32 s62, 0x2000\n\t"
    "buffer_load_dwordx4 a[96:99], v23, %[srdy], s62 offen sc1\n\t"
    "buffer_load_dwordx4 a[100:103], v23, %[srdy], s62 offen offset:256 sc1\n\t"
    "s_waitcnt vmcnt(8)\n\t"
    "s_waitcnt lgkmcnt(10)\n\t"
    "v_mfma_f32_16x16x32_bf16 v[104:107], a[0:3], a[64:67], v[76:79]\n\t"
    "ds_read_b64_tr_b16 a[24:25], v160 offset:192\n\t"
    "buffer_load_dwordx4 a[104:107], v23, %[srdy], s62 offen offset:2048 sc1\n\t"
    "v_mfma_f32_16x16x32_bf16 v[108:111], a[0:3], a[68:71], v[76:79]\n\t"
    "ds_read_b64_tr_b16 a[26:27], v160 offset:3776\n\t"
    "s_waitcnt lgkmcnt(10)\n\t"
    "v_mfma_f32_16x16x32_bf16 v[112:115], a[4:7], a[64:67], v[80:83]\n\t"
    "ds_read_b64_tr_b16 a[28:29], v160 offset:14336\n\t"
    "buffer_load_dwordx4 a[108:111], v23, %[srdy], s62 offen offset:2304 sc1\n\t"
    "v_mfma_f32_16x16x32_bf16 v[116:119], a[4:7], a[68:71], v[80:83]\n\t"
    "ds_read_b64_tr_b16 a[30:31], v160 offset:17920\n\t"
    "s_waitcnt lgkmcnt(10)\n\t"
    "v_mfma_f32_16x16x32_bf16 v[120:123], a[8:11], a[64:67], v[84:87]\n\t"
    "ds_read_b64_tr_b16 a[32:33], v161 offset:14368\n\t"
    "s_movk_i32 s62, 0x3000\n\t"
    "buffer_load_dwordx4 a[112:115], v23, %[srdy], s62 offen sc1\n\t"
    "v_mfma_f32_16x16x32_bf16 v[124:127], a[8:11], a[68:71], v[84:87]\n\t"
    "ds_read_b64_tr_b16 a[34:35], v161 offset:17952\n\t"
    "s_waitcnt lgkmcnt(10)\n\t"
    "v_mfma_f32_16x16x32_bf16 v[128:131], a[12:15], a[64:67], v[88:91]\n\t"
    "ds_read_b64_tr_b16 a[36:37], v160 offset:14400\n\t"
    "buffer_load_dwordx4 a[116:119], v23, %[srdy], s62 offen offset:256 sc1\n\t"
    "v_mfma_f32_16x16x32_bf16 v[132:135], a[12:15], a[68:71], v[88:91]\n\t"
    "ds_read_b64_tr_b16 a[38:39], v160 offset:17984\n\t"
    "s_waitcnt lgkmcnt(10)\n\t"
    "v_mfma_f32_16x16x32_bf16 v[136:139], a[16:19], a[64:67], v[92:95]\n\t"
    "ds_read_b64_tr_b16 a[40:41], v161 offset:14432\n\t"
    "buffer_load_dwordx4 a[120:123], v23, %[srdy], s62 offen offset:2048 sc1\n\t"
    "v_mfma_f32_16x16x32_bf16 v[140:143], a[16:19], a[68:71], v[92:95]\n\t"
    "ds_read_b64_tr_b16 a[42:43], v161 offset:18016\n\t"
    "s_waitcnt lgkmcnt(10)\n\t"
    "v_mfma_f32_16x16x32_bf16 v[144:147], a[20:23], a[64:67], v[96:99]\n\t"
    "ds_read_b64_tr_b16 a[44:45], v160 offset:14464\n\t"
    "buffer_load_dwordx4 a[124:127], v23, %[srdy], s62 offen offset:2304 sc1\n\t"
    "v_mfma_f32_16x16x32_bf16 v[148:151], a[20:23], a[68:71], v[96:99]\n\t"
    "ds_read_b64_tr_b16 a[46:47], v160 offset:18048\n\t"
    "s_waitcnt lgkmcnt(10)\n\t"
    "v_mfma_f32_16x16x32_bf16 v[152:155], a[24:27], a[64:67], v[100:103]\n\t"
    "ds_read_b64_tr_b16 a[48:49], v161 offset:14496\n\t"
    "s_movk_i32 s62, 0x4000\n\t"
    "buffer_load_dwordx4 a[128:131], v23, %[srdy], s62 offen sc1\n\t"
    "v_mfma_f32_16x16x32_bf16 v[156:159], a[24:27], a[68:71], v[100:103]\n\t"
    "ds_read_b64_tr_b16 a[50:51], v161 offset:18080\n\t"
    "s_waitcnt vmcnt(13)\n\t"
    "s_waitcnt lgkmcnt(10)\n\t"
    "v_mfma_f32_16x16x32_bf16 v[104:107], a[28:31], a[72:75], v[104:107]\n\t"
    "ds_read_b64_tr_b16 a[52:53], v160 offset:14528\n\t"
    "buffer_load_dwordx4 a[132:135], v23, %[srdy], s62 offen offset:256 sc1\n\t"
    "v_mfma_f32_16x16x32_bf16 v[108:111], a[28:31], a[76:79], v[108:111]\n\t"
    "ds_read_b64_tr_b16 a[54:55], v160 offset:18112\n\t"
    "s_waitcnt lgkmcnt(10)\n\t"
    "v_mfma_f32_16x16x32_bf16 v[112:115], a[32:35], a[72:75], v[112:115]\n\t"
    "ds_read_b64_tr_b16 a[56:57], v162\n\t"
    "buffer_load_dwordx4 a[136:139], v23, %[srdy], s62 offen offset:2048 sc1\n\t"
    "v_mfma_f32_16x16x32_bf16 v[116:119], a[32:35], a[76:79], v[116:119]\n\t"
    "ds_read_b64_tr_b16 a[58:59], v162 offset:3584\n\t"
    "s_waitcnt lgkmcnt(10)\n\t"
    "v_mfma_f32_16x16x32_bf16 v[120:123], a[36:39], a[72:75], v[120:123]\n\t"
    "ds_read_b64_tr_b16 a[60:61], v163 offset:32\n\t"
    "buffer_load_dwordx4 a[140:143], v23, %[srdy], s62 offen offset:2304 sc1\n\t"
    "v_mfma_f32_16x16x32_bf16 v[124:127], a[36:39], a[76:79], v[124:127]\n\t"
    "ds_read_b64_tr_b16 a[62:63], v163 offset:3616\n\t"
    "s_waitcnt lgkmcnt(10)\n\t"
    "v_mfma_f32_16x16x32_bf16 v[128:131], a[40:43], a[72:75], v[128:131]\n\t"
    "ds_read_b64_tr_b16 a[0:1], v162 offset:64\n\t"
    "s_movk_i32 s62, 0x5000\n\t"
    "buffer_load_dwordx4 a[144:147], v23, %[srdy], s62 offen sc1\n\t"
    "v_mfma_f32_16x16x32_bf16 v[132:135], a[40:43], a[76:79], v[132:135]\n\t"
    "ds_read_b64_tr_b16 a[2:3], v162 offset:3648\n\t"
    "s_waitcnt lgkmcnt(10)\n\t"
    "v_mfma_f32_16x16x32_bf16 v[136:139], a[44:47], a[72:75], v[136:139]\n\t"
    "ds_read_b64_tr_b16 a[4:5], v163 offset:96\n\t"
    "buffer_load_dwordx4 a[148:151], v23, %[srdy], s62 offen offset:256 sc1\n\t"
    "v_mfma_f32_16x16x32_bf16 v[140:143], a[44:47], a[76:79], v[140:143]\n\t"
    "ds_read_b64_tr_b16 a[6:7], v163 offset:3680\n\t"
    "s_waitcnt lgkmcnt(10)\n\t"
    "v_mfma_f32_16x16x32_bf16 v[144:147], a[48:51], a[72:75], v[144:147]\n\t"
    "ds_read_b64_tr_b16 a[8:9], v162 offset:128\n\t"
    "buffer_load_dwordx4 a[152:155], v23, %[srdy], s62 offen offset:2048 sc1\n\t"
    "v_mfma_f32_16x16x32_bf16 v[148:151], a[48:51], a[76:79], v[148:151]\n\t"
    "ds_read_b64_tr_b16 a[10:11], v162 offset:3712\n\t"
    "s_waitcnt lgkmcnt(10)\n\t"
    "v_mfma_f32_16x16x32_bf16 v[152:155], a[52:55], a[72:75], v[152:155]\n\t"
    "ds_read_b64_tr_b16 a[12:13], v163 offset:160\n\t"
    "buffer_load_dwordx4 a[156:159], v23, %[srdy], s62 offen offset:2304 sc1\n\t"
    "v_mfma_f32_16x16x32_bf16 v[156:159], a[52:55], a[76:79], v[156:159]\n\t"
    "ds_read_b64_tr_b16 a[14:15], v163 offset:3744\n\t"
    "s_waitcnt vmcnt(18)\n\t"
    "s_waitcnt lgkmcnt(10)\n\t"
    "v_mfma_f32_16x16x32_bf16 v[104:107], a[56:59], a[80:83], v[104:107]\n\t"
    "s_add_u32 m0, s63, 0x8000\n\t"
    "s_add_u32 s61, s65, 0xe0000\n\t"
    "ds_read_b64_tr_b16 a[16:17], v162 offset:192\n\t"
    "s_movk_i32 s62, 0x6000\n\t"
    "buffer_load_dwordx4 a[160:163], v23, %[srdy], s62 offen sc1\n\t"
    "v_mfma_f32_16x16x32_bf16 v[108:111], a[56:59], a[84:87], v[108:111]\n\t"
    "buffer_load_dwordx4 v8, %[srdw1], s61 offen lds\n\t"
    "ds_read_b64_tr_b16 a[18:19], v162 offset:3776\n\t"
    "s_waitcnt lgkmcnt(10)\n\t"
    "v_mfma_f32_16x16x32_bf16 v[112:115], a[60:63], a[80:83], v[112:115]\n\t"
    "buffer_load_dwordx4 v9, %[srdw1], s61 offen offset:1024 lds\n\t"
    "ds_read_b64_tr_b16 a[20:21], v162 offset:14336\n\t"
    "buffer_load_dwordx4 a[164:167], v23, %[srdy], s62 offen offset:256 sc1\n\t"
    "v_mfma_f32_16x16x32_bf16 v[116:119], a[60:63], a[84:87], v[116:119]\n\t"
    "ds_read_b64_tr_b16 a[22:23], v162 offset:17920\n\t"
    "s_waitcnt lgkmcnt(10)\n\t"
    "v_mfma_f32_16x16x32_bf16 v[120:123], a[0:3], a[80:83], v[120:123]\n\t"
    "buffer_load_dwordx4 v10, %[srdw1], s61 offen offset:2048 lds\n\t"
    "ds_read_b64_tr_b16 a[24:25], v163 offset:14368\n\t"
    "buffer_load_dwordx4 a[168:171], v23, %[srdy], s62 offen offset:2048 sc1\n\t"
    "v_mfma_f32_16x16x32_bf16 v[124:127], a[0:3], a[84:87], v[124:127]\n\t"
    "ds_read_b64_tr_b16 a[26:27], v163 offset:17952\n\t"
    "s_waitcnt lgkmcnt(10)\n\t"
    "v_mfma_f32_16x16x32_bf16 v[128:131], a[4:7], a[80:83], v[128:131]\n\t"
    "buffer_load_dwordx4 v11, %[srdw1], s61 offen offset:3072 lds\n\t"
    "ds_read_b64_tr_b16 a[28:29], v162 offset:14400\n\t"
    "buffer_load_dwordx4 a[172:175], v23, %[srdy], s62 offen offset:2304 sc1\n\t"
    "v_mfma_f32_16x16x32_bf16 v[132:135], a[4:7], a[84:87], v[132:135]\n\t"
    "s_add_u32 m0, s63, 0x9000\n\t"
    "ds_read_b64_tr_b16 a[30:31], v162 offset:17984\n\t"
    "s_waitcnt lgkmcnt(10)\n\t"
    "v_mfma_f32_16x16x32_bf16 v[136:139], a[8:11], a[80:83], v[136:139]\n\t"
    "buffer_load_dwordx4 v12, %[srdw1], s61 offen lds\n\t"
    "ds_read_b64_tr_b16 a[32:33], v163 offset:14432\n\t"
    "v_mfma_f32_16x16x32_bf16 v[140:143], a[8:11], a[84:87], v[140:143]\n\t"
    "ds_read_b64_tr_b16 a[34:35], v163 offset:18016\n\t"
    "s_waitcnt lgkmcnt(10)\n\t"
    "v_mfma_f32_16x16x32_bf16 v[144:147], a[12:15], a[80:83], v[144:147]\n\t"
    "buffer_load_dwordx4 v13, %[srdw1], s61 offen offset:1024 lds\n\t"
    "ds_read_b64_tr_b16 a[36:37], v162 offset:14464\n\t"
    "v_mfma_f32_16x16x32_bf16 v[148:151], a[12:15], a[84:87], v[148:151]\n\t"
    "ds_read_b64_tr_b16 a[38:39], v162 offset:18048\n\t"
    "s_waitcnt lgkmcnt(10)\n\t"
    "v_mfma_f32_16x16x32_bf16 v[152:155], a[16:19], a[80:83], v[152:155]\n\t"
    "buffer_load_dwordx4 v14, %[srdw1], s61 offen offset:2048 lds\n\t"
    "ds_read_b64_tr_b16 a[40:41], v163 offset:14496\n\t"
    "v_mfma_f32_16x16x32_bf16 v[156:159], a[16:19], a[84:87], v[156:159]\n\t"
    "ds_read_b64_tr_b16 a[42:43], v163 offset:18080\n\t"
    "s_waitcnt vmcnt(27)\n\t"
    "s_waitcnt lgkmcnt(10)\n\t"
    "v_mfma_f32_16x16x32_bf16 v[104:107], a[20:23], a[88:91], v[104:107]\n\t"
    "s_barrier\n\t"
    "ds_read_b64_tr_b16 a[44:45], v162 offset:14528\n\t"
    "v_mfma_f32_16x16x32_bf16 v[108:111], a[20:23], a[92:95], v[108:111]\n\t"
    "s_add_u32 m0, s63, 0x20000\n\t"
    "s_add_u32 s61, s65, 0x118000\n\t"
    "ds_read_b64_tr_b16 a[46:47], v162 offset:18112\n\t"
    "s_waitcnt lgkmcnt(10)\n\t"
    "v_mfma_f32_16x16x32_bf16 v[112:115], a[24:27], a[88:91], v[112:115]\n\t"
    "buffer_load_dwordx4 v8, %[srdw1], s61 offen lds\n\t"
    "ds_read_b64_tr_b16 a[48:49], v164\n\t"
    "v_mfma_f32_16x16x32_bf16 v[116:119], a[24:27], a[92:95], v[116:119]\n\t"
    "ds_read_b64_tr_b16 a[50:51], v164 offset:3584\n\t"
    "s_waitcnt lgkmcnt(10)\n\t"
    "v_mfma_f32_16x16x32_bf16 v[120:123], a[28:31], a[88:91], v[120:123]\n\t"
    "ds_read_b64_tr_b16 a[52:53], v165 offset:32\n\t"
    "v_mfma_f32_16x16x32_bf16 v[124:127], a[28:31], a[92:95], v[124:127]\n\t"
    "ds_read_b64_tr_b16 a[54:55], v165 offset:3616\n\t"
    "s_waitcnt lgkmcnt(10)\n\t"
    "v_mfma_f32_16x16x32_bf16 v[128:131], a[32:35], a[88:91], v[128:131]\n\t"
    "buffer_load_dwordx4 v9, %[srdw1], s61 offen offset:1024 lds\n\t"
    "ds_read_b64_tr_b16 a[56:57], v164 offset:64\n\t"
    "v_mfma_f32_16x16x32_bf16 v[132:135], a[32:35], a[92:95], v[132:135]\n\t"
    "ds_read_b64_tr_b16 a[58:59], v164 offset:3648\n\t"
    "s_waitcnt lgkmcnt(10)\n\t"
    "v_mfma_f32_16x16x32_bf16 v[136:139], a[36:39], a[88:91], v[136:139]\n\t"
    "ds_read_b64_tr_b16 a[60:61], v165 offset:96\n\t"
    "v_mfma_f32_16x16x32_bf16 v[140:143], a[36:39], a[92:95], v[140:143]\n\t"
    "ds_read_b64_tr_b16 a[62:63], v165 offset:3680\n\t"
    "s_waitcnt lgkmcnt(10)\n\t"
    "v_mfma_f32_16x16x32_bf16 v[144:147], a[40:43], a[88:91], v[144:147]\n\t"
    "buffer_load_dwordx4 v10, %[srdw1], s61 offen offset:2048 lds\n\t"
    "ds_read_b64_tr_b16 a[0:1], v164 offset:128\n\t"
    "v_mfma_f32_16x16x32_bf16 v[148:151], a[40:43], a[92:95], v[148:151]\n\t"
    "ds_read_b64_tr_b16 a[2:3], v164 offset:3712\n\t"
    "s_waitcnt lgkmcnt(10)\n\t"
    "v_mfma_f32_16x16x32_bf16 v[152:155], a[44:47], a[88:91], v[152:155]\n\t"
    "ds_read_b64_tr_b16 a[4:5], v165 offset:160\n\t"
    "v_mfma_f32_16x16x32_bf16 v[156:159], a[44:47], a[92:95], v[156:159]\n\t"
    "ds_read_b64_tr_b16 a[6:7], v165 offset:3744\n\t"
    "s_waitcnt vmcnt(28)\n\t"
    "s_waitcnt lgkmcnt(10)\n\t"
    "v_mfma_f32_16x16x32_bf16 v[104:107], a[48:51], a[96:99], v[104:107]\n\t"
    "ds_read_b64_tr_b16 a[8:9], v164 offset:192\n\t"
    "v_mfma_f32_16x16x32_bf16 v[108:111], a[48:51], a[100:103], v[108:111]\n\t"
    "buffer_load_dwordx4 v11, %[srdw1], s61 offen offset:3072 lds\n\t"
    "ds_read_b64_tr_b16 a[10:11], v164 offset:3776\n\t"
    "s_waitcnt lgkmcnt(10)\n\t"
    "v_mfma_f32_16x16x32_bf16 v[112:115], a[52:55], a[96:99], v[112:115]\n\t"
    "ds_read_b64_tr_b16 a[12:13], v164 offset:14336\n\t"
    "v_mfma_f32_16x16x32_bf16 v[116:119], a[52:55], a[100:103], v[116:119]\n\t"
    "s_add_u32 m0, s63, 0x21000\n\t"
    "ds_read_b64_tr_b16 a[14:15], v164 offset:17920\n\t"
    "s_waitcnt lgkmcnt(10)\n\t"
    "v_mfma_f32_16x16x32_bf16 v[120:123], a[56:59], a[96:99], v[120:123]\n\t"
    "buffer_load_dwordx4 v12, %[srdw1], s61 offen lds\n\t"
    "ds_read_b64_tr_b16 a[16:17], v165 offset:14368\n\t"
    "v_mfma_f32_16x16x32_bf16 v[124:127], a[56:59], a[100:103], v[124:127]\n\t"
    "ds_read_b64_tr_b16 a[18:19], v165 offset:17952\n\t"
    "s_waitcnt lgkmcnt(10)\n\t"
    "v_mfma_f32_16x16x32_bf16 v[128:131], a[60:63], a[96:99], v[128:131]\n\t"
    "ds_read_b64_tr_b16 a[20:21], v164 offset:14400\n\t"
    "v_mfma_f32_16x16x32_bf16 v[132:135], a[60:63], a[100:103], v[132:135]\n\t"
    "buffer_load_dwordx4 v13, %[srdw1], s61 offen offset:1024 lds\n\t"
    "ds_read_b64_tr_b16 a[22:23], v164 offset:17984\n\t"
    "s_waitcnt lgkmcnt(10)\n\t"
    "v_mfma_f32_16x16x32_bf16 v[136:139], a[0:3], a[96:99], v[136:139]\n\t"
    "ds_read_b64_tr_b16 a[24:25], v165 offset:14432\n\t"
    "v_mfma_f32_16x16x32_bf16 v[140:143], a[0:3], a[100:103], v[140:143]\n\t"
    "ds_read_b64_tr_b16 a[26:27], v165 offset:18016\n\t"
    "s_waitcnt lgkmcnt(10)\n\t"
    "v_mfma_f32_16x16x32_bf16 v[144:147], a[4:7], a[96:99], v[144:147]\n\t"
    "buffer_load_dwordx4 v14, %[srdw1], s61 offen offset:2048 lds\n\t"
    "ds_read_b64_tr_b16 a[28:29], v164 offset:14464\n\t"
    "v_mfma_f32_16x16x32_bf16 v[148:151], a[4:7], a[100:103], v[148:151]\n\t"
    "ds_read_b64_tr_b16 a[30:31], v164 offset:18048\n\t"
    "s_waitcnt lgkmcnt(10)\n\t"
    "v_mfma_f32_16x16x32_bf16 v[152:155], a[8:11], a[96:99], v[152:155]\n\t"
    "ds_read_b64_tr_b16 a[32:33], v165 offset:14496\n\t"
    "v_mfma_f32_16x16x32_bf16 v[156:159], a[8:11], a[100:103], v[156:159]\n\t"
    "ds_read_b64_tr_b16 a[34:35], v165 offset:18080\n\t"
    "s_waitcnt vmcnt(30)\n\t"
    "s_waitcnt lgkmcnt(10)\n\t"
    "v_mfma_f32_16x16x32_bf16 v[104:107], a[12:15], a[104:107], v[104:107]\n\t"
    "s_barrier\n\t"
    "ds_read_b64_tr_b16 a[36:37], v164 offset:14528\n\t"
    "v_mfma_f32_16x16x32_bf16 v[108:111], a[12:15], a[108:111], v[108:111]\n\t"
    "s_add_u32 m0, s63, 0x18000\n\t"
    "s_add_u32 s61, s65, 0x150000\n\t"
    "ds_read_b64_tr_b16 a[38:39], v164 offset:18112\n\t"
    "s_waitcnt lgkmcnt(10)\n\t"
    "v_mfma_f32_16x16x32_bf16 v[112:115], a[16:19], a[104:107], v[112:115]\n\t"
    "buffer_load_dwordx4 v8, %[srdw1], s61 offen lds\n\t"
    "ds_read_b64_tr_b16 a[40:41], v166\n\t"
    "v_mfma_f32_16x16x32_bf16 v[116:119], a[16:19], a[108:111], v[116:119]\n\t"
    "ds_read_b64_tr_b16 a[42:43], v166 offset:3584\n\t"
    "s_waitcnt lgkmcnt(10)\n\t"
    "v_mfma_f32_16x16x32_bf16 v[120:123], a[20:23], a[104:107], v[120:123]\n\t"
    "ds_read_b64_tr_b16 a[44:45], v167 offset:32\n\t"
    "v_mfma_f32_16x16x32_bf16 v[124:127], a[20:23], a[108:111], v[124:127]\n\t"
    "ds_read_b64_tr_b16 a[46:47], v167 offset:3616\n\t"
    "s_waitcnt lgkmcnt(10)\n\t"
    "v_mfma_f32_16x16x32_bf16 v[128:131], a[24:27], a[104:107], v[128:131]\n\t"
    "buffer_load_dwordx4 v9, %[srdw1], s61 offen offset:1024 lds\n\t"
    "ds_read_b64_tr_b16 a[48:49], v166 offset:64\n\t"
    "v_mfma_f32_16x16x32_bf16 v[132:135], a[24:27], a[108:111], v[132:135]\n\t"
    "ds_read_b64_tr_b16 a[50:51], v166 offset:3648\n\t"
    "s_waitcnt lgkmcnt(10)\n\t"
    "v_mfma_f32_16x16x32_bf16 v[136:139], a[28:31], a[104:107], v[136:139]\n\t"
    "ds_read_b64_tr_b16 a[52:53], v167 offset:96\n\t"
    "v_mfma_f32_16x16x32_bf16 v[140:143], a[28:31], a[108:111], v[140:143]\n\t"
    "ds_read_b64_tr_b16 a[54:55], v167 offset:3680\n\t"
    "s_waitcnt lgkmcnt(10)\n\t"
    "v_mfma_f32_16x16x32_bf16 v[144:147], a[32:35], a[104:107], v[144:147]\n\t"
    "buffer_load_dwordx4 v10, %[srdw1], s61 offen offset:2048 lds\n\t"
    "ds_read_b64_tr_b16 a[56:57], v166 offset:128\n\t"
    "v_mfma_f32_16x16x32_bf16 v[148:151], a[32:35], a[108:111], v[148:151]\n\t"
    "ds_read_b64_tr_b16 a[58:59], v166 offset:3712\n\t"
    "s_waitcnt lgkmcnt(10)\n\t"
    "v_mfma_f32_16x16x32_bf16 v[152:155], a[36:39], a[104:107], v[152:155]\n\t"
    "ds_read_b64_tr_b16 a[60:61], v167 offset:160\n\t"
    "v_mfma_f32_16x16x32_bf16 v[156:159], a[36:39], a[108:111], v[156:159]\n\t"
    "ds_read_b64_tr_b16 a[62:63], v167 offset:3744\n\t"
    "s_waitcnt vmcnt(31)\n\t"
    "s_waitcnt lgkmcnt(10)\n\t"
    "v_mfma_f32_16x16x32_bf16 v[104:107], a[40:43], a[112:115], v[104:107]\n\t"
    "ds_read_b64_tr_b16 a[0:1], v166 offset:192\n\t"
    "v_mfma_f32_16x16x32_bf16 v[108:111], a[40:43], a[116:119], v[108:111]\n\t"
    "buffer_load_dwordx4 v11, %[srdw1], s61 offen offset:3072 lds\n\t"
    "ds_read_b64_tr_b16 a[2:3], v166 offset:3776\n\t"
    "s_waitcnt lgkmcnt(10)\n\t"
    "v_mfma_f32_16x16x32_bf16 v[112:115], a[44:47], a[112:115], v[112:115]\n\t"
    "ds_read_b64_tr_b16 a[4:5], v166 offset:14336\n\t"
    "v_mfma_f32_16x16x32_bf16 v[116:119], a[44:47], a[116:119], v[116:119]\n\t"
    "s_add_u32 m0, s63, 0x19000\n\t"
    "ds_read_b64_tr_b16 a[6:7], v166 offset:17920\n\t"
    "s_waitcnt lgkmcnt(10)\n\t"
    "v_mfma_f32_16x16x32_bf16 v[120:123], a[48:51], a[112:115], v[120:123]\n\t"
    "buffer_load_dwordx4 v12, %[srdw1], s61 offen lds\n\t"
    "ds_read_b64_tr_b16 a[8:9], v167 offset:14368\n\t"
    "v_mfma_f32_16x16x32_bf16 v[124:127], a[48:51], a[116:119], v[124:127]\n\t"
    "ds_read_b64_tr_b16 a[10:11], v167 offset:17952\n\t"
    "s_waitcnt lgkmcnt(10)\n\t"
    "v_mfma_f32_16x16x32_bf16 v[128:131], a[52:55], a[112:115], v[128:131]\n\t"
    "ds_read_b64_tr_b16 a[12:13], v166 offset:14400\n\t"
    "v_mfma_f32_16x16x32_bf16 v[132:135], a[52:55], a[116:119], v[132:135]\n\t"
    "buffer_load_dwordx4 v13, %[srdw1], s61 offen offset:1024 lds\n\t"
    "ds_read_b64_tr_b16 a[14:15], v166 offset:17984\n\t"
    "s_waitcnt lgkmcnt(10)\n\t"
    "v_mfma_f32_16x16x32_bf16 v[136:139], a[56:59], a[112:115], v[136:139]\n\t"
    "ds_read_b64_tr_b16 a[16:17], v167 offset:14432\n\t"
    "v_mfma_f32_16x16x32_bf16 v[140:143], a[56:59], a[116:119], v[140:143]\n\t"
    "ds_read_b64_tr_b16 a[18:19], v167 offset:18016\n\t"
    "s_waitcnt lgkmcnt(10)\n\t"
    "v_mfma_f32_16x16x32_bf16 v[144:147], a[60:63], a[112:115], v[144:147]\n\t"
    "buffer_load_dwordx4 v14, %[srdw1], s61 offen offset:2048 lds\n\t"
    "ds_read_b64_tr_b16 a[20:21], v166 offset:14464\n\t"
    "v_mfma_f32_16x16x32_bf16 v[148:151], a[60:63], a[116:119], v[148:151]\n\t"
    "ds_read_b64_tr_b16 a[22:23], v166 offset:18048\n\t"
    "s_waitcnt lgkmcnt(10)\n\t"
    "v_mfma_f32_16x16x32_bf16 v[152:155], a[0:3], a[112:115], v[152:155]\n\t"
    "ds_read_b64_tr_b16 a[24:25], v167 offset:14496\n\t"
    "v_mfma_f32_16x16x32_bf16 v[156:159], a[0:3], a[116:119], v[156:159]\n\t"
    "ds_read_b64_tr_b16 a[26:27], v167 offset:18080\n\t"
    "s_waitcnt vmcnt(33)\n\t"
    "s_waitcnt lgkmcnt(10)\n\t"
    "v_mfma_f32_16x16x32_bf16 v[104:107], a[4:7], a[120:123], v[104:107]\n\t"
    "s_waitcnt vmcnt(14)\n\t"
    "s_barrier\n\t"
    "ds_read_b64_tr_b16 a[28:29], v166 offset:14528\n\t"
    "v_mfma_f32_16x16x32_bf16 v[108:111], a[4:7], a[124:127], v[108:111]\n\t"
    "s_add_u32 m0, s63, 0x10000\n\t"
    "s_add_u32 s61, s66, 0x0\n\t"
    "ds_read_b64_tr_b16 a[30:31], v166 offset:18112\n\t"
    "s_waitcnt lgkmcnt(10)\n\t"
    "v_mfma_f32_16x16x32_bf16 v[112:115], a[8:11], a[120:123], v[112:115]\n\t"
    "buffer_load_dwordx4 v16, %[srdw2], s61 offen lds\n\t"
    "ds_read_b64_tr_b16 a[32:33], v168\n\t"
    "v_mfma_f32_16x16x32_bf16 v[116:119], a[8:11], a[124:127], v[116:119]\n\t"
    "ds_read_b64_tr_b16 a[34:35], v168 offset:3584\n\t"
    "s_waitcnt lgkmcnt(10)\n\t"
    "v_mfma_f32_16x16x32_bf16 v[120:123], a[12:15], a[120:123], v[120:123]\n\t"
    "ds_read_b64_tr_b16 a[36:37], v169 offset:32\n\t"
    "v_mfma_f32_16x16x32_bf16 v[124:127], a[12:15], a[124:127], v[124:127]\n\t"
    "ds_read_b64_tr_b16 a[38:39], v169 offset:3616\n\t"
    "s_waitcnt lgkmcnt(10)\n\t"
    "v_mfma_f32_16x16x32_bf16 v[128:131], a[16:19], a[120:123], v[128:131]\n\t"
    "buffer_load_dwordx4 v17, %[srdw2], s61 offen offset:1024 lds\n\t"
    "ds_read_b64_tr_b16 a[40:41], v168 offset:64\n\t"
    "v_mfma_f32_16x16x32_bf16 v[132:135], a[16:19], a[124:127], v[132:135]\n\t"
    "ds_read_b64_tr_b16 a[42:43], v168 offset:3648\n\t"
    "s_waitcnt lgkmcnt(10)\n\t"
    "v_mfma_f32_16x16x32_bf16 v[136:139], a[20:23], a[120:123], v[136:139]\n\t"
    "ds_read_b64_tr_b16 a[44:45], v169 offset:96\n\t"
    "v_mfma_f32_16x16x32_bf16 v[140:143], a[20:23], a[124:127], v[140:143]\n\t"
    "ds_read_b64_tr_b16 a[46:47], v169 offset:3680\n\t"
    "s_waitcnt lgkmcnt(10)\n\t"
    "v_mfma_f32_16x16x32_bf16 v[144:147], a[24:27], a[120:123], v[144:147]\n\t"
    "buffer_load_dwordx4 v18, %[srdw2], s61 offen offset:2048 lds\n\t"
    "ds_read_b64_tr_b16 a[48:49], v168 offset:128\n\t"
    "v_mfma_f32_16x16x32_bf16 v[148:151], a[24:27], a[124:127], v[148:151]\n\t"
    "ds_read_b64_tr_b16 a[50:51], v168 offset:3712\n\t"
    "s_waitcnt lgkmcnt(10)\n\t"
    "v_mfma_f32_16x16x32_bf16 v[152:155], a[28:31], a[120:123], v[152:155]\n\t"
    "ds_read_b64_tr_b16 a[52:53], v169 offset:160\n\t"
    "v_mfma_f32_16x16x32_bf16 v[156:159], a[28:31], a[124:127], v[156:159]\n\t"
    "ds_read_b64_tr_b16 a[54:55], v169 offset:3744\n\t"
    "s_waitcnt lgkmcnt(10)\n\t"
    "v_mfma_f32_16x16x32_bf16 v[104:107], a[32:35], a[128:131], v[104:107]\n\t"
    "ds_read_b64_tr_b16 a[56:57], v168 offset:192\n\t"
    "v_mfma_f32_16x16x32_bf16 v[108:111], a[32:35], a[132:135], v[108:111]\n\t"
    "buffer_load_dwordx4 v19, %[srdw2], s61 offen offset:3072 lds\n\t"
    "ds_read_b64_tr_b16 a[58:59], v168 offset:3776\n\t"
    "s_waitcnt lgkmcnt(10)\n\t"
    "v_mfma_f32_16x16x32_bf16 v[112:115], a[36:39], a[128:131], v[112:115]\n\t"
    "ds_read_b64_tr_b16 a[60:61], v168 offset:14336\n\t"
    "v_mfma_f32_16x16x32_bf16 v[116:119], a[36:39], a[132:135], v[116:119]\n\t"
    "s_add_u32 m0, s63, 0x11000\n\t"
    "s_add_u32 s61, s66, 0x7000\n\t"
    "ds_read_b64_tr_b16 a[62:63], v168 offset:17920\n\t"
    "s_waitcnt lgkmcnt(10)\n\t"
    "v_mfma_f32_16x16x32_bf16 v[120:123], a[40:43], a[128:131], v[120:123]\n\t"
    "buffer_load_dwordx4 v16, %[srdw2], s61 offen lds\n\t"
    "ds_read_b64_tr_b16 a[0:1], v169 offset:14368\n\t"
    "v_mfma_f32_16x16x32_bf16 v[124:127], a[40:43], a[132:135], v[124:127]\n\t"
    "ds_read_b64_tr_b16 a[2:3], v169 offset:17952\n\t"
    "s_waitcnt lgkmcnt(10)\n\t"
    "v_mfma_f32_16x16x32_bf16 v[128:131], a[44:47], a[128:131], v[128:131]\n\t"
    "ds_read_b64_tr_b16 a[4:5], v168 offset:14400\n\t"
    "v_mfma_f32_16x16x32_bf16 v[132:135], a[44:47], a[132:135], v[132:135]\n\t"
    "buffer_load_dwordx4 v17, %[srdw2], s61 offen offset:1024 lds\n\t"
    "ds_read_b64_tr_b16 a[6:7], v168 offset:17984\n\t"
    "s_waitcnt lgkmcnt(10)\n\t"
    "v_mfma_f32_16x16x32_bf16 v[136:139], a[48:51], a[128:131], v[136:139]\n\t"
    "ds_read_b64_tr_b16 a[8:9], v169 offset:14432\n\t"
    "v_mfma_f32_16x16x32_bf16 v[140:143], a[48:51], a[132:135], v[140:143]\n\t"
    "ds_read_b64_tr_b16 a[10:11], v169 offset:18016\n\t"
    "s_waitcnt lgkmcnt(10)\n\t"
    "v_mfma_f32_16x16x32_bf16 v[144:147], a[52:55], a[128:131], v[144:147]\n\t"
    "buffer_load_dwordx4 v18, %[srdw2], s61 offen offset:2048 lds\n\t"
    "ds_read_b64_tr_b16 a[12:13], v168 offset:14464\n\t"
    "v_mfma_f32_16x16x32_bf16 v[148:151], a[52:55], a[132:135], v[148:151]\n\t"
    "ds_read_b64_tr_b16 a[14:15], v168 offset:18048\n\t"
    "s_waitcnt lgkmcnt(10)\n\t"
    "v_mfma_f32_16x16x32_bf16 v[152:155], a[56:59], a[128:131], v[152:155]\n\t"
    "ds_read_b64_tr_b16 a[16:17], v169 offset:14496\n\t"
    "v_mfma_f32_16x16x32_bf16 v[156:159], a[56:59], a[132:135], v[156:159]\n\t"
    "ds_read_b64_tr_b16 a[18:19], v169 offset:18080\n\t"
    "s_waitcnt lgkmcnt(10)\n\t"
    "v_mfma_f32_16x16x32_bf16 v[104:107], a[60:63], a[136:139], v[104:107]\n\t"
    "s_waitcnt vmcnt(14)\n\t"
    "s_barrier\n\t"
    "ds_read_b64_tr_b16 a[20:21], v168 offset:14528\n\t"
    "v_mfma_f32_16x16x32_bf16 v[108:111], a[60:63], a[140:143], v[108:111]\n\t"
    "s_add_u32 m0, s63, 0x0\n\t"
    "s_add_u32 s61, s66, 0x80\n\t"
    "ds_read_b64_tr_b16 a[22:23], v168 offset:18112\n\t"
    "s_waitcnt lgkmcnt(10)\n\t"
    "v_mfma_f32_16x16x32_bf16 v[112:115], a[0:3], a[136:139], v[112:115]\n\t"
    "buffer_load_dwordx4 v16, %[srdw2], s61 offen lds\n\t"
    "ds_read_b64_tr_b16 a[24:25], v160\n\t"
    "v_mfma_f32_16x16x32_bf16 v[116:119], a[0:3], a[140:143], v[116:119]\n\t"
    "ds_read_b64_tr_b16 a[26:27], v160 offset:3584\n\t"
    "s_waitcnt lgkmcnt(10)\n\t"
    "v_mfma_f32_16x16x32_bf16 v[120:123], a[4:7], a[136:139], v[120:123]\n\t"
    "ds_read_b64_tr_b16 a[28:29], v161 offset:32\n\t"
    "v_mfma_f32_16x16x32_bf16 v[124:127], a[4:7], a[140:143], v[124:127]\n\t"
    "ds_read_b64_tr_b16 a[30:31], v161 offset:3616\n\t"
    "s_waitcnt lgkmcnt(10)\n\t"
    "v_mfma_f32_16x16x32_bf16 v[128:131], a[8:11], a[136:139], v[128:131]\n\t"
    "buffer_load_dwordx4 v17, %[srdw2], s61 offen offset:1024 lds\n\t"
    "ds_read_b64_tr_b16 a[32:33], v160 offset:64\n\t"
    "v_mfma_f32_16x16x32_bf16 v[132:135], a[8:11], a[140:143], v[132:135]\n\t"
    "ds_read_b64_tr_b16 a[34:35], v160 offset:3648\n\t"
    "s_waitcnt lgkmcnt(10)\n\t"
    "v_mfma_f32_16x16x32_bf16 v[136:139], a[12:15], a[136:139], v[136:139]\n\t"
    "ds_read_b64_tr_b16 a[36:37], v161 offset:96\n\t"
    "v_mfma_f32_16x16x32_bf16 v[140:143], a[12:15], a[140:143], v[140:143]\n\t"
    "ds_read_b64_tr_b16 a[38:39], v161 offset:3680\n\t"
    "s_waitcnt lgkmcnt(10)\n\t"
    "v_mfma_f32_16x16x32_bf16 v[144:147], a[16:19], a[136:139], v[144:147]\n\t"
    "buffer_load_dwordx4 v18, %[srdw2], s61 offen offset:2048 lds\n\t"
    "ds_read_b64_tr_b16 a[40:41], v160 offset:128\n\t"
    "v_mfma_f32_16x16x32_bf16 v[148:151], a[16:19], a[140:143], v[148:151]\n\t"
    "ds_read_b64_tr_b16 a[42:43], v160 offset:3712\n\t"
    "s_waitcnt lgkmcnt(10)\n\t"
    "v_mfma_f32_16x16x32_bf16 v[152:155], a[20:23], a[136:139], v[152:155]\n\t"
    "ds_read_b64_tr_b16 a[44:45], v161 offset:160\n\t"
    "v_mfma_f32_16x16x32_bf16 v[156:159], a[20:23], a[140:143], v[156:159]\n\t"
    "ds_read_b64_tr_b16 a[46:47], v161 offset:3744\n\t"
    "s_waitcnt lgkmcnt(10)\n\t"
    "v_mfma_f32_16x16x32_bf16 v[104:107], a[24:27], a[144:147], v[104:107]\n\t"
    "ds_read_b64_tr_b16 a[48:49], v160 offset:192\n\t"
    "v_mfma_f32_16x16x32_bf16 v[108:111], a[24:27], a[148:151], v[108:111]\n\t"
    "buffer_load_dwordx4 v19, %[srdw2], s61 offen offset:3072 lds\n\t"
    "ds_read_b64_tr_b16 a[50:51], v160 offset:3776\n\t"
    "s_waitcnt lgkmcnt(10)\n\t"
    "v_mfma_f32_16x16x32_bf16 v[112:115], a[28:31], a[144:147], v[112:115]\n\t"
    "ds_read_b64_tr_b16 a[52:53], v160 offset:14336\n\t"
    "v_mfma_f32_16x16x32_bf16 v[116:119], a[28:31], a[148:151], v[116:119]\n\t"
    "s_add_u32 m0, s63, 0x1000\n\t"
    "s_add_u32 s61, s66, 0x7080\n\t"
    "ds_read_b64_tr_b16 a[54:55], v160 offset:17920\n\t"
    "s_waitcnt lgkmcnt(10)\n\t"
    "v_mfma_f32_16x16x32_bf16 v[120:123], a[32:35], a[144:147], v[120:123]\n\t"
    "buffer_load_dwordx4 v16, %[srdw2], s61 offen lds\n\t"
    "ds_read_b64_tr_b16 a[56:57], v161 offset:14368\n\t"
    "v_mfma_f32_16x16x32_bf16 v[124:127], a[32:35], a[148:151], v[124:127]\n\t"
    "ds_read_b64_tr_b16 a[58:59], v161 offset:17952\n\t"
    "s_waitcnt lgkmcnt(10)\n\t"
    "v_mfma_f32_16x16x32_bf16 v[128:131], a[36:39], a[144:147], v[128:131]\n\t"
    "ds_read_b64_tr_b16 a[60:61], v160 offset:14400\n\t"
    "v_mfma_f32_16x16x32_bf16 v[132:135], a[36:39], a[148:151], v[132:135]\n\t"
    "buffer_load_dwordx4 v17, %[srdw2], s61 offen offset:1024 lds\n\t"
    "ds_read_b64_tr_b16 a[62:63], v160 offset:17984\n\t"
    "s_waitcnt lgkmcnt(10)\n\t"
    "v_mfma_f32_16x16x32_bf16 v[136:139], a[40:43], a[144:147], v[136:139]\n\t"
    "ds_read_b64_tr_b16 a[0:1], v161 offset:14432\n\t"
    "v_mfma_f32_16x16x32_bf16 v[140:143], a[40:43], a[148:151], v[140:143]\n\t"
    "ds_read_b64_tr_b16 a[2:3], v161 offset:18016\n\t"
    "s_waitcnt lgkmcnt(10)\n\t"
    "v_mfma_f32_16x16x32_bf16 v[144:147], a[44:47], a[144:147], v[144:147]\n\t"
    "buffer_load_dwordx4 v18, %[srdw2], s61 offen offset:2048 lds\n\t"
    "ds_read_b64_tr_b16 a[4:5], v160 offset:14464\n\t"
    "v_mfma_f32_16x16x32_bf16 v[148:151], a[44:47], a[148:151], v[148:151]\n\t"
    "ds_read_b64_tr_b16 a[6:7], v160 offset:18048\n\t"
    "s_waitcnt lgkmcnt(10)\n\t"
    "v_mfma_f32_16x16x32_bf16 v[152:155], a[48:51], a[144:147], v[152:155]\n\t"
    "ds_read_b64_tr_b16 a[8:9], v161 offset:14496\n\t"
    "v_mfma_f32_16x16x32_bf16 v[156:159], a[48:51], a[148:151], v[156:159]\n\t"
    "ds_read_b64_tr_b16 a[10:11], v161 offset:18080\n\t"
    "s_waitcnt lgkmcnt(10)\n\t"
    "v_mfma_f32_16x16x32_bf16 v[104:107], a[52:55], a[152:155], v[104:107]\n\t"
    "s_waitcnt vmcnt(14)\n\t"
    "s_barrier\n\t"
    "ds_read_b64_tr_b16 a[12:13], v160 offset:14528\n\t"
    "v_mfma_f32_16x16x32_bf16 v[108:111], a[52:55], a[156:159], v[108:111]\n\t"
    "s_add_u32 m0, s63, 0x8000\n\t"
    "s_add_u32 s61, s66, 0x100\n\t"
    "ds_read_b64_tr_b16 a[14:15], v160 offset:18112\n\t"
    "s_waitcnt lgkmcnt(10)\n\t"
    "v_mfma_f32_16x16x32_bf16 v[112:115], a[56:59], a[152:155], v[112:115]\n\t"
    "buffer_load_dwordx4 v16, %[srdw2], s61 offen lds\n\t"
    "ds_read_b64_tr_b16 a[16:17], v162\n\t"
    "v_mfma_f32_16x16x32_bf16 v[116:119], a[56:59], a[156:159], v[116:119]\n\t"
    "ds_read_b64_tr_b16 a[18:19], v162 offset:3584\n\t"
    "s_waitcnt lgkmcnt(10)\n\t"
    "v_mfma_f32_16x16x32_bf16 v[120:123], a[60:63], a[152:155], v[120:123]\n\t"
    "ds_read_b64_tr_b16 a[20:21], v163 offset:32\n\t"
    "v_mfma_f32_16x16x32_bf16 v[124:127], a[60:63], a[156:159], v[124:127]\n\t"
    "ds_read_b64_tr_b16 a[22:23], v163 offset:3616\n\t"
    "s_waitcnt lgkmcnt(10)\n\t"
    "v_mfma_f32_16x16x32_bf16 v[128:131], a[0:3], a[152:155], v[128:131]\n\t"
    "buffer_load_dwordx4 v17, %[srdw2], s61 offen offset:1024 lds\n\t"
    "ds_read_b64_tr_b16 a[24:25], v162 offset:64\n\t"
    "v_mfma_f32_16x16x32_bf16 v[132:135], a[0:3], a[156:159], v[132:135]\n\t"
    "ds_read_b64_tr_b16 a[26:27], v162 offset:3648\n\t"
    "s_waitcnt lgkmcnt(10)\n\t"
    "v_mfma_f32_16x16x32_bf16 v[136:139], a[4:7], a[152:155], v[136:139]\n\t"
    "ds_read_b64_tr_b16 a[28:29], v163 offset:96\n\t"
    "v_mfma_f32_16x16x32_bf16 v[140:143], a[4:7], a[156:159], v[140:143]\n\t"
    "ds_read_b64_tr_b16 a[30:31], v163 offset:3680\n\t"
    "s_waitcnt lgkmcnt(10)\n\t"
    "v_mfma_f32_16x16x32_bf16 v[144:147], a[8:11], a[152:155], v[144:147]\n\t"
    "buffer_load_dwordx4 v18, %[srdw2], s61 offen offset:2048 lds\n\t"
    "ds_read_b64_tr_b16 a[32:33], v162 offset:128\n\t"
    "v_mfma_f32_16x16x32_bf16 v[148:151], a[8:11], a[156:159], v[148:151]\n\t"
    "ds_read_b64_tr_b16 a[34:35], v162 offset:3712\n\t"
    "s_waitcnt lgkmcnt(10)\n\t"
    "v_mfma_f32_16x16x32_bf16 v[152:155], a[12:15], a[152:155], v[152:155]\n\t"
    "ds_read_b64_tr_b16 a[36:37], v163 offset:160\n\t"
    "v_mfma_f32_16x16x32_bf16 v[156:159], a[12:15], a[156:159], v[156:159]\n\t"
    "ds_read_b64_tr_b16 a[38:39], v163 offset:3744\n\t"
    "s_waitcnt lgkmcnt(10)\n\t"
    "v_mfma_f32_16x16x32_bf16 v[104:107], a[16:19], a[160:163], v[104:107]\n\t"
    "ds_read_b64_tr_b16 a[40:41], v162 offset:192\n\t"
    "v_mfma_f32_16x16x32_bf16 v[108:111], a[16:19], a[164:167], v[108:111]\n\t"
    "buffer_load_dwordx4 v19, %[srdw2], s61 offen offset:3072 lds\n\t"
    "ds_read_b64_tr_b16 a[42:43], v162 offset:3776\n\t"
    "s_waitcnt lgkmcnt(10)\n\t"
    "v_mfma_f32_16x16x32_bf16 v[112:115], a[20:23], a[160:163], v[112:115]\n\t"
    "ds_read_b64_tr_b16 a[44:45], v162 offset:14336\n\t"
    "v_mfma_f32_16x16x32_bf16 v[116:119], a[20:23], a[164:167], v[116:119]\n\t"
    "s_add_u32 m0, s63, 0x9000\n\t"
    "s_add_u32 s61, s66, 0x7100\n\t"
    "ds_read_b64_tr_b16 a[46:47], v162 offset:17920\n\t"
    "s_waitcnt lgkmcnt(10)\n\t"
    "v_mfma_f32_16x16x32_bf16 v[120:123], a[24:27], a[160:163], v[120:123]\n\t"
    "buffer_load_dwordx4 v16, %[srdw2], s61 offen lds\n\t"
    "ds_read_b64_tr_b16 a[48:49], v163 offset:14368\n\t"
    "v_mfma_f32_16x16x32_bf16 v[124:127], a[24:27], a[164:167], v[124:127]\n\t"
    "ds_read_b64_tr_b16 a[50:51], v163 offset:17952\n\t"
    "s_waitcnt lgkmcnt(10)\n\t"
    "v_mfma_f32_16x16x32_bf16 v[128:131], a[28:31], a[160:163], v[128:131]\n\t"
    "ds_read_b64_tr_b16 a[52:53], v162 offset:14400\n\t"
    "v_mfma_f32_16x16x32_bf16 v[132:135], a[28:31], a[164:167], v[132:135]\n\t"
    "buffer_load_dwordx4 v17, %[srdw2], s61 offen offset:1024 lds\n\t"
    "ds_read_b64_tr_b16 a[54:55], v162 offset:17984\n\t"
    "s_waitcnt lgkmcnt(10)\n\t"
    "v_mfma_f32_16x16x32_bf16 v[136:139], a[32:35], a[160:163], v[136:139]\n\t"
    "ds_read_b64_tr_b16 a[56:57], v163 offset:14432\n\t"
    "v_mfma_f32_16x16x32_bf16 v[140:143], a[32:35], a[164:167], v[140:143]\n\t"
    "ds_read_b64_tr_b16 a[58:59], v163 offset:18016\n\t"
    "s_waitcnt lgkmcnt(10)\n\t"
    "v_mfma_f32_16x16x32_bf16 v[144:147], a[36:39], a[160:163], v[144:147]\n\t"
    "buffer_load_dwordx4 v18, %[srdw2], s61 offen offset:2048 lds\n\t"
    "ds_read_b64_tr_b16 a[60:61], v162 offset:14464\n\t"
    "v_mfma_f32_16x16x32_bf16 v[148:151], a[36:39], a[164:167], v[148:151]\n\t"
    "ds_read_b64_tr_b16 a[62:63], v162 offset:18048\n\t"
    "s_waitcnt lgkmcnt(10)\n\t"
    "v_mfma_f32_16x16x32_bf16 v[152:155], a[40:43], a[160:163], v[152:155]\n\t"
    "ds_read_b64_tr_b16 a[0:1], v163 offset:14496\n\t"
    "v_mfma_f32_16x16x32_bf16 v[156:159], a[40:43], a[164:167], v[156:159]\n\t"
    "ds_read_b64_tr_b16 a[2:3], v163 offset:18080\n\t"
    "s_waitcnt lgkmcnt(10)\n\t"
    "v_mfma_f32_16x16x32_bf16 v[104:107], a[44:47], a[168:171], v[104:107]\n\t"
    "s_waitcnt vmcnt(14)\n\t"
    "s_barrier\n\t"
    "ds_read_b64_tr_b16 a[4:5], v162 offset:14528\n\t"
    "v_mfma_f32_16x16x32_bf16 v[108:111], a[44:47], a[172:175], v[108:111]\n\t"
    "ds_read_b64_tr_b16 a[6:7], v162 offset:18112\n\t"
    "s_waitcnt lgkmcnt(10)\n\t"
    "v_mfma_f32_16x16x32_bf16 v[112:115], a[48:51], a[168:171], v[112:115]\n\t"
    "ds_read_b64_tr_b16 a[8:9], v178\n\t"
    "v_mfma_f32_16x16x32_bf16 v[116:119], a[48:51], a[172:175], v[116:119]\n\t"
    "ds_read_b64_tr_b16 a[10:11], v178 offset:2048\n\t"
    "s_waitcnt lgkmcnt(10)\n\t"
    "v_mfma_f32_16x16x32_bf16 v[120:123], a[52:55], a[168:171], v[120:123]\n\t"
    "ds_read_b64_tr_b16 a[12:13], v179\n\t"
    "v_mfma_f32_16x16x32_bf16 v[124:127], a[52:55], a[172:175], v[124:127]\n\t"
    "ds_read_b64_tr_b16 a[14:15], v179 offset:2048\n\t"
    "s_waitcnt lgkmcnt(10)\n\t"
    "v_mfma_f32_16x16x32_bf16 v[128:131], a[56:59], a[168:171], v[128:131]\n\t"
    "ds_read_b64_tr_b16 a[16:17], v178 offset:4096\n\t"
    "v_mfma_f32_16x16x32_bf16 v[132:135], a[56:59], a[172:175], v[132:135]\n\t"
    "ds_read_b64_tr_b16 a[18:19], v178 offset:6144\n\t"
    "s_waitcnt lgkmcnt(10)\n\t"
    "v_mfma_f32_16x16x32_bf16 v[136:139], a[60:63], a[168:171], v[136:139]\n\t"
    "ds_read_b64_tr_b16 a[20:21], v179 offset:4096\n\t"
    "v_mfma_f32_16x16x32_bf16 v[140:143], a[60:63], a[172:175], v[140:143]\n\t"
    "ds_read_b64_tr_b16 a[22:23], v179 offset:6144\n\t"
    "s_waitcnt lgkmcnt(10)\n\t"
    "v_mfma_f32_16x16x32_bf16 v[144:147], a[0:3], a[168:171], v[144:147]\n\t"
    "ds_read_b64_tr_b16 a[24:25], v178 offset:8192\n\t"
    "v_mfma_f32_16x16x32_bf16 v[148:151], a[0:3], a[172:175], v[148:151]\n\t"
    "ds_read_b64_tr_b16 a[26:27], v178 offset:10240\n\t"
    "s_waitcnt lgkmcnt(10)\n\t"
    "v_mfma_f32_16x16x32_bf16 v[152:155], a[4:7], a[168:171], v[152:155]\n\t"
    "ds_read_b64_tr_b16 a[28:29], v179 offset:8192\n\t"
    "v_mfma_f32_16x16x32_bf16 v[156:159], a[4:7], a[172:175], v[156:159]\n\t"
    "ds_read_b64_tr_b16 a[30:31], v179 offset:10240\n\t"
    "s_nop 7\n\t"
    "v_pk_mul_f32 v[192:193], v[104:105], v[104:105]\n\t"
    "v_pk_mul_f32 v[194:195], v[106:107], v[106:107]\n\t"
    "v_pk_mul_f32 v[196:197], v[108:109], v[108:109]\n\t"
    "v_pk_mul_f32 v[198:199], v[110:111], v[110:111]\n\t"
    "v_pk_fma_f32 v[192:193], v[192:193], v[56:57], v[58:59]\n\t"
    "v_pk_fma_f32 v[194:195], v[194:195], v[56:57], v[58:59]\n\t"
    "v_pk_fma_f32 v[196:197], v[196:197], v[56:57], v[58:59]\n\t"
    "v_pk_fma_f32 v[198:199], v[198:199], v[56:57], v[58:59]\n\t"
    "v_pk_mul_f32 v[192:193], v[192:193], v[104:105]\n\t"
    "v_pk_mul_f32 v[194:195], v[194:195], v[106:107]\n\t"
    "v_pk_mul_f32 v[196:197], v[196:197], v[108:109]\n\t"
    "v_pk_mul_f32 v[198:199], v[198:199], v[110:111]\n\t"
    "v_exp_f32 v192, v192\n\t"
    "v_exp_f32 v193, v193\n\t"
    "v_pk_mul_f32 v[200:201], v[112:113], v[112:113]\n\t"
    "v_exp_f32 v194, v194\n\t"
    "v_exp_f32 v195, v195\n\t"
    "v_pk_mul_f32 v[202:203], v[114:115], v[114:115]\n\t"
    "v_exp_f32 v196, v196\n\t"
    "v_exp_f32 v197, v197\n\t"
    "v_pk_mul_f32 v[204:205], v[116:117], v[116:117]\n\t"
    "v_exp_f32 v198, v198\n\t"
    "v_exp_f32 v199, v199\n\t"
    "v_pk_mul_f32 v[206:207], v[118:119], v[118:119]\n\t"
    "v_pk_fma_f32 v[200:201], v[200:201], v[56:57], v[58:59]\n\t"
    "v_pk_fma_f32 v[202:203], v[202:203], v[56:57], v[58:59]\n\t"
    "v_pk_fma_f32 v[204:205], v[204:205], v[56:57], v[58:59]\n\t"
    "v_pk_fma_f32 v[206:207], v[206:207], v[56:57], v[58:59]\n\t"
    "v_pk_mul_f32 v[200:201], v[200:201], v[112:113]\n\t"
    "v_pk_mul_f32 v[202:203], v[202:203], v[114:115]\n\t"
    "v_pk_mul_f32 v[204:205], v[204:205], v[116:117]\n\t"
    "v_pk_mul_f32 v[206:207], v[206:207], v[118:119]\n\t"
    "v_pk_add_f32 v[192:193], v[192:193], v[60:61]\n\t"
    "v_pk_add_f32 v[194:195], v[194:195], v[60:61]\n\t"
    "v_pk_add_f32 v[196:197], v[196:197], v[60:61]\n\t"
    "v_pk_add_f32 v[198:199], v[198:199], v[60:61]\n\t"
    "s_barrier\n\t"
    "s_add_u32 m0, s63, 0x18000\n\t"
    "s_add_u32 s61, s66, 0x180\n\t"
    "buffer_load_dwordx4 v16, %[srdw2], s61 offen lds\n\t"
    "buffer_load_dwordx4 v17, %[srdw2], s61 offen offset:1024 lds\n\t"
    "v_exp_f32 v200, v200\n\t"
    "v_exp_f32 v201, v201\n\t"
    "v_pk_mul_f32 v[208:209], v[120:121], v[120:121]\n\t"
    "v_exp_f32 v202, v202\n\t"
    "v_exp_f32 v203, v203\n\t"
    "v_pk_mul_f32 v[210:211], v[122:123], v[122:123]\n\t"
    "v_exp_f32 v204, v204\n\t"
    "v_exp_f32 v205, v205\n\t"
    "v_pk_mul_f32 v[212:213], v[124:125], v[124:125]\n\t"
    "v_exp_f32 v206, v206\n\t"
    "v_exp_f32 v207, v207\n\t"
    "v_pk_mul_f32 v[214:215], v[126:127], v[126:127]\n\t"
    "v_rcp_f32 v192, v192\n\t"
    "v_pk_fma_f32 v[208:209], v[208:209], v[56:57], v[58:59]\n\t"
    "v_rcp_f32 v193, v193\n\t"
    "v_pk_fma_f32 v[210:211], v[210:211], v[56:57], v[58:59]\n\t"
    "v_pk_fma_f32 v[212:213], v[212:213], v[56:57], v[58:59]\n\t"
    "v_rcp_f32 v194, v194\n\t"
    "v_pk_fma_f32 v[214:215], v[214:215], v[56:57], v[58:59]\n\t"
    "v_rcp_f32 v195, v195\n\t"
    "v_pk_mul_f32 v[208:209], v[208:209], v[120:121]\n\t"
    "v_pk_mul_f32 v[210:211], v[210:211], v[122:123]\n\t"
    "v_rcp_f32 v196, v196\n\t"
    "v_pk_mul_f32 v[212:213], v[212:213], v[124:125]\n\t"
    "v_rcp_f32 v197, v197\n\t"
    "v_pk_mul_f32 v[214:215], v[214:215], v[126:127]\n\t"
    "v_pk_add_f32 v[200:201], v[200:201], v[60:61]\n\t"
    "v_rcp_f32 v198, v198\n\t"
    "v_pk_add_f32 v[202:203], v[202:203], v[60:61]\n\t"
    "v_rcp_f32 v199, v199\n\t"
    "v_pk_add_f32 v[204:205], v[204:205], v[60:61]\n\t"
    "v_pk_add_f32 v[206:207], v[206:207], v[60:61]\n\t"
    "buffer_load_dwordx4 v18, %[srdw2], s61 offen offset:2048 lds\n\t"
    "buffer_load_dwordx4 v19, %[srdw2], s61 offen offset:3072 lds\n\t"
    "v_exp_f32 v208, v208\n\t"
    "v_pk_mul_f32 v[192:193], v[192:193], v[104:105]\n\t"
    "v_exp_f32 v209, v209\n\t"
    "v_pk_mul_f32 v[194:195], v[194:195], v[106:107]\n\t"
    "v_pk_mul_f32 v[196:197], v[196:197], v[108:109]\n\t"
    "v_exp_f32 v210, v210\n\t"
    "v_pk_mul_f32 v[198:199], v[198:199], v[110:111]\n\t"
    "v_exp_f32 v211, v211\n\t"
    "v_cvt_pk_bf16_f32 v224, v192, v193\n\t"
    "v_cvt_pk_bf16_f32 v225, v194, v195\n\t"
    "v_exp_f32 v212, v212\n\t"
    "v_cvt_pk_bf16_f32 v76, v196, v197\n\t"
    "v_exp_f32 v213, v213\n\t"
    "v_cvt_pk_bf16_f32 v77, v198, v199\n\t"
    "v_pk_mul_f32 v[216:217], v[128:129], v[128:129]\n\t"
    "v_exp_f32 v214, v214\n\t"
    "v_pk_mul_f32 v[218:219], v[130:131], v[130:131]\n\t"
    "v_exp_f32 v215, v215\n\t"
    "v_pk_mul_f32 v[220:221], v[132:133], v[132:133]\n\t"
    "v_pk_mul_f32 v[222:223], v[134:135], v[134:135]\n\t"
    "v_rcp_f32 v200, v200\n\t"
    "v_pk_fma_f32 v[216:217], v[216:217], v[56:57], v[58:59]\n\t"
    "v_rcp_f32 v201, v201\n\t"
    "v_pk_fma_f32 v[218:219], v[218:219], v[56:57], v[58:59]\n\t"
    "v_pk_fma_f32 v[220:221], v[220:221], v[56:57], v[58:59]\n\t"
    "v_rcp_f32 v202, v202\n\t"
    "v_pk_fma_f32 v[222:223], v[222:223], v[56:57], v[58:59]\n\t"
    "v_rcp_f32 v203, v203\n\t"
    "v_pk_mul_f32 v[216:217], v[216:217], v[128:129]\n\t"
    "v_pk_mul_f32 v[218:219], v[218:219], v[130:131]\n\t"
    "v_rcp_f32 v204, v204\n\t"
    "v_pk_mul_f32 v[220:221], v[220:221], v[132:133]\n\t"
    "v_rcp_f32 v205, v205\n\t"
    "v_pk_mul_f32 v[222:223], v[222:223], v[134:135]\n\t"
    "v_pk_add_f32 v[208:209], v[208:209], v[60:61]\n\t"
    "v_rcp_f32 v206, v206\n\t"
    "v_pk_add_f32 v[210:211], v[210:211], v[60:61]\n\t"
    "v_rcp_f32 v207, v207\n\t"
    "v_pk_add_f32 v[212:213], v[212:213], v[60:61]\n\t"
    "v_pk_add_f32 v[214:215], v[214:215], v[60:61]\n\t"
    "s_add_u32 m0, s63, 0x19000\n\t"
    "s_add_u32 s61, s66, 0x7180\n\t"
    "buffer_load_dwordx4 v16, %[srdw2], s61 offen lds\n\t"
    "buffer_load_dwordx4 v17, %[srdw2], s61 offen offset:1024 lds\n\t"
    "v_exp_f32 v216, v216\n\t"
    "v_pk_mul_f32 v[200:201], v[200:201], v[112:113]\n\t"
    "v_exp_f32 v217, v217\n\t"
    "v_pk_mul_f32 v[202:203], v[202:203], v[114:115]\n\t"
    "v_pk_mul_f32 v[204:205], v[204:205], v[116:117]\n\t"
    "v_exp_f32 v218, v218\n\t"
    "v_pk_mul_f32 v[206:207], v[206:207], v[118:119]\n\t"
    "v_cvt_pk_bf16_f32 v226, v200, v201\n\t"
    "v_exp_f32 v219, v219\n\t"
    "v_cvt_pk_bf16_f32 v227, v202, v203\n\t"
    "v_cvt_pk_bf16_f32 v78, v204, v205\n\t"
    "v_exp_f32 v220, v220\n\t"
    "v_cvt_pk_bf16_f32 v79, v206, v207\n\t"
    "v_exp_f32 v221, v221\n\t"
    "ds_write_b128 v29, v[224:227]\n\t"
    "ds_write_b128 v29, v[76:79] offset:3072\n\t"
    "v_exp_f32 v222, v222\n\t"
    "v_pk_mul_f32 v[192:193], v[136:137], v[136:137]\n\t"
    "v_pk_mul_f32 v[194:195], v[138:139], v[138:139]\n\t"
    "v_exp_f32 v223, v223\n\t"
    "v_pk_mul_f32 v[196:197], v[140:141], v[140:141]\n\t"
    "v_pk_mul_f32 v[198:199], v[142:143], v[142:143]\n\t"
    "v_rcp_f32 v208, v208\n\t"
    "v_pk_fma_f32 v[192:193], v[192:193], v[56:57], v[58:59]\n\t"
    "v_rcp_f32 v209, v209\n\t"
    "v_pk_fma_f32 v[194:195], v[194:195], v[56:57], v[58:59]\n\t"
    "v_pk_fma_f32 v[196:197], v[196:197], v[56:57], v[58:59]\n\t"
    "v_rcp_f32 v210, v210\n\t"
    "v_pk_fma_f32 v[198:199], v[198:199], v[56:57], v[58:59]\n\t"
    "v_rcp_f32 v211, v211\n\t"
    "v_pk_mul_f32 v[192:193], v[192:193], v[136:137]\n\t"
    "v_pk_mul_f32 v[194:195], v[194:195], v[138:139]\n\t"
    "v_rcp_f32 v212, v212\n\t"
    "v_pk_mul_f32 v[196:197], v[196:197], v[140:141]\n\t"
    "v_rcp_f32 v213, v213\n\t"
    "v_pk_mul_f32 v[198:199], v[198:199], v[142:143]\n\t"
    "v_pk_add_f32 v[216:217], v[216:217], v[60:61]\n\t"
    "v_rcp_f32 v214, v214\n\t"
    "v_pk_add_f32 v[218:219], v[218:219], v[60:61]\n\t"
    "v_rcp_f32 v215, v215\n\t"
    "v_pk_add_f32 v[220:221], v[220:221], v[60:61]\n\t"
    "v_pk_add_f32 v[222:223], v[222:223], v[60:61]\n\t"
    "buffer_load_dwordx4 v18, %[srdw2], s61 offen offset:2048 lds\n\t"
    "v_exp_f32 v192, v192\n\t"
    "v_pk_mul_f32 v[208:209], v[208:209], v[120:121]\n\t"
    "v_exp_f32 v193, v193\n\t"
    "v_pk_mul_f32 v[210:211], v[210:211], v[122:123]\n\t"
    "v_pk_mul_f32 v[212:213], v[212:213], v[124:125]\n\t"
    "v_exp_f32 v194, v194\n\t"
    "v_pk_mul_f32 v[214:215], v[214:215], v[126:127]\n\t"
    "v_exp_f32 v195, v195\n\t"
    "v_cvt_pk_bf16_f32 v228, v208, v209\n\t"
    "v_cvt_pk_bf16_f32 v229, v210, v211\n\t"
    "v_exp_f32 v196, v196\n\t"
    "v_cvt_pk_bf16_f32 v80, v212, v213\n\t"
    "v_exp_f32 v197, v197\n\t"
    "v_cvt_pk_bf16_f32 v81, v214, v215\n\t"
    "v_pk_mul_f32 v[200:201], v[144:145], v[144:145]\n\t"
    "v_exp_f32 v198, v198\n\t"
    "v_pk_mul_f32 v[202:203], v[146:147], v[146:147]\n\t"
    "v_exp_f32 v199, v199\n\t"
    "v_pk_mul_f32 v[204:205], v[148:149], v[148:149]\n\t"
    "v_pk_mul_f32 v[206:207], v[150:151], v[150:151]\n\t"
    "v_rcp_f32 v216, v216\n\t"
    "v_pk_fma_f32 v[200:201], v[200:201], v[56:57], v[58:59]\n\t"
    "v_rcp_f32 v217, v217\n\t"
    "v_pk_fma_f32 v[202:203], v[202:203], v[56:57], v[58:59]\n\t"
    "v_pk_fma_f32 v[204:205], v[204:205], v[56:57], v[58:59]\n\t"
    "v_rcp_f32 v218, v218\n\t"
    "v_pk_fma_f32 v[206:207], v[206:207], v[56:57], v[58:59]\n\t"
    "v_rcp_f32 v219, v219\n\t"
    "v_pk_mul_f32 v[200:201], v[200:201], v[144:145]\n\t"
    "v_pk_mul_f32 v[202:203], v[202:203], v[146:147]\n\t"
    "v_rcp_f32 v220, v220\n\t"
    "v_pk_mul_f32 v[204:205], v[204:205], v[148:149]\n\t"
    "v_rcp_f32 v221, v221\n\t"
    "v_pk_mul_f32 v[206:207], v[206:207], v[150:151]\n\t"
    "v_pk_add_f32 v[192:193], v[192:193], v[60:61]\n\t"
    "v_rcp_f32 v222, v222\n\t"
    "v_pk_add_f32 v[194:195], v[194:195], v[60:61]\n\t"
    "v_rcp_f32 v223, v223\n\t"
    "v_pk_add_f32 v[196:197], v[196:197], v[60:61]\n\t"
    "v_pk_add_f32 v[198:199], v[198:199], v[60:61]\n\t"
    "v_exp_f32 v200, v200\n\t"
    "v_pk_mul_f32 v[216:217], v[216:217], v[128:129]\n\t"
    "v_exp_f32 v201, v201\n\t"
    "v_pk_mul_f32 v[218:219], v[218:219], v[130:131]\n\t"
    "v_pk_mul_f32 v[220:221], v[220:221], v[132:133]\n\t"
    "v_exp_f32 v202, v202\n\t"
    "v_pk_mul_f32 v[222:223], v[222:223], v[134:135]\n\t"
    "v_cvt_pk_bf16_f32 v230, v216, v217\n\t"
    "v_exp_f32 v203, v203\n\t"
    "v_cvt_pk_bf16_f32 v231, v218, v219\n\t"
    "v_cvt_pk_bf16_f32 v82, v220, v221\n\t"
    "v_exp_f32 v204, v204\n\t"
    "v_cvt_pk_bf16_f32 v83, v222, v223\n\t"
    "v_exp_f32 v205, v205\n\t"
    "ds_write_b128 v29, v[228:231] offset:1024\n\t"
    "ds_write_b128 v29, v[80:83] offset:4096\n\t"
    "v_exp_f32 v206, v206\n\t"
    "v_pk_mul_f32 v[208:209], v[152:153], v[152:153]\n\t"
    "v_pk_mul_f32 v[210:211], v[154:155], v[154:155]\n\t"
    "v_exp_f32 v207, v207\n\t"
    "v_pk_mul_f32 v[212:213], v[156:157], v[156:157]\n\t"
    "v_pk_mul_f32 v[214:215], v[158:159], v[158:159]\n\t"
    "v_rcp_f32 v192, v192\n\t"
    "v_pk_fma_f32 v[208:209], v[208:209], v[56:57], v[58:59]\n\t"
    "v_rcp_f32 v193, v193\n\t"
    "v_pk_fma_f32 v[210:211], v[210:211], v[56:57], v[58:59]\n\t"
    "v_pk_fma_f32 v[212:213], v[212:213], v[56:57], v[58:59]\n\t"
    "v_rcp_f32 v194, v194\n\t"
    "v_pk_fma_f32 v[214:215], v[214:215], v[56:57], v[58:59]\n\t"
    "v_rcp_f32 v195, v195\n\t"
    "v_pk_mul_f32 v[208:209], v[208:209], v[152:153]\n\t"
    "v_pk_mul_f32 v[210:211], v[210:211], v[154:155]\n\t"
    "v_rcp_f32 v196, v196\n\t"
    "v_pk_mul_f32 v[212:213], v[212:213], v[156:157]\n\t"
    "v_rcp_f32 v197, v197\n\t"
    "v_pk_mul_f32 v[214:215], v[214:215], v[158:159]\n\t"
    "v_pk_add_f32 v[200:201], v[200:201], v[60:61]\n\t"
    "v_rcp_f32 v198, v198\n\t"
    "v_pk_add_f32 v[202:203], v[202:203], v[60:61]\n\t"
    "v_rcp_f32 v199, v199\n\t"
    "v_pk_add_f32 v[204:205], v[204:205], v[60:61]\n\t"
    "v_pk_add_f32 v[206:207], v[206:207], v[60:61]\n\t"
    "v_exp_f32 v208, v208\n\t"
    "v_pk_mul_f32 v[192:193], v[192:193], v[136:137]\n\t"
    "v_exp_f32 v209, v209\n\t"
    "v_pk_mul_f32 v[194:195], v[194:195], v[138:139]\n\t"
    "v_exp_f32 v210, v210\n\t"
    "v_pk_mul_f32 v[196:197], v[196:197], v[140:141]\n\t"
    "v_exp_f32 v211, v211\n\t"
    "v_pk_mul_f32 v[198:199], v[198:199], v[142:143]\n\t"
    "v_exp_f32 v212, v212\n\t"
    "v_cvt_pk_bf16_f32 v232, v192, v193\n\t"
    "v_exp_f32 v213, v213\n\t"
    "v_cvt_pk_bf16_f32 v233, v194, v195\n\t"
    "v_exp_f32 v214, v214\n\t"
    "v_cvt_pk_bf16_f32 v84, v196, v197\n\t"
    "v_exp_f32 v215, v215\n\t"
    "v_cvt_pk_bf16_f32 v85, v198, v199\n\t"
    "v_rcp_f32 v200, v200\n\t"
    "v_rcp_f32 v201, v201\n\t"
    "v_pk_add_f32 v[208:209], v[208:209], v[60:61]\n\t"
    "v_rcp_f32 v202, v202\n\t"
    "v_rcp_f32 v203, v203\n\t"
    "v_pk_add_f32 v[210:211], v[210:211], v[60:61]\n\t"
    "v_rcp_f32 v204, v204\n\t"
    "v_rcp_f32 v205, v205\n\t"
    "v_pk_add_f32 v[212:213], v[212:213], v[60:61]\n\t"
    "v_rcp_f32 v206, v206\n\t"
    "v_rcp_f32 v207, v207\n\t"
    "v_pk_add_f32 v[214:215], v[214:215], v[60:61]\n\t"
    "v_pk_mul_f32 v[200:201], v[200:201], v[144:145]\n\t"
    "v_pk_mul_f32 v[202:203], v[202:203], v[146:147]\n\t"
    "v_pk_mul_f32 v[204:205], v[204:205], v[148:149]\n\t"
    "v_pk_mul_f32 v[206:207], v[206:207], v[150:151]\n\t"
    "v_cvt_pk_bf16_f32 v234, v200, v201\n\t"
    "v_cvt_pk_bf16_f32 v235, v202, v203\n\t"
    "v_cvt_pk_bf16_f32 v86, v204, v205\n\t"
    "v_cvt_pk_bf16_f32 v87, v206, v207\n\t"
    "ds_write_b128 v29, v[232:235] offset:2048\n\t"
    "ds_write_b128 v29, v[84:87] offset:5120\n\t"
    "v_rcp_f32 v208, v208\n\t"
    "v_rcp_f32 v209, v209\n\t"
    "v_rcp_f32 v210, v210\n\t"
    "v_rcp_f32 v211, v211\n\t"
    "v_rcp_f32 v212, v212\n\t"
    "v_rcp_f32 v213, v213\n\t"
    "v_rcp_f32 v214, v214\n\t"
    "v_rcp_f32 v215, v215\n\t"
    "s_nop 0\n\t"
    "v_pk_mul_f32 v[208:209], v[208:209], v[152:153]\n\t"
    "v_pk_mul_f32 v[210:211], v[210:211], v[154:155]\n\t"
    "v_pk_mul_f32 v[212:213], v[212:213], v[156:157]\n\t"
    "v_pk_mul_f32 v[214:215], v[214:215], v[158:159]\n\t"
    "v_cvt_pk_bf16_f32 v236, v208, v209\n\t"
    "v_cvt_pk_bf16_f32 v237, v210, v211\n\t"
    "v_cvt_pk_bf16_f32 v88, v212, v213\n\t"
    "v_cvt_pk_bf16_f32 v89, v214, v215\n\t"
    "ds_write_b64 v30, v[236:237]\n\t"
    "ds_write_b64 v30, v[88:89] offset:512\n\t"
    "s_nop 0\n\t"
    "s_waitcnt lgkmcnt(0)\n\t"
    "s_barrier\n\t"
    "ds_read_b64 v[238:239], v32\n\t"
    "ds_read_b64 v[90:91], v32 offset:512\n\t"
    "ds_read_b128 v[240:243], v31\n\t"
    "ds_read_b128 v[92:95], v31 offset:3072\n\t"
    "ds_read_b128 v[244:247], v31 offset:1024\n\t"
    "ds_read_b128 v[96:99], v31 offset:4096\n\t"
    "ds_read_b128 v[248:251], v31 offset:2048\n\t"
    "ds_read_b128 v[100:103], v31 offset:5120\n\t"
    "v_mfma_f32_16x16x32_bf16 v[104:107], a[8:11], v[224:227], 0\n\t"
    "ds_read_b64_tr_b16 a[32:33], v178 offset:12288\n\t"
    "v_mfma_f32_16x16x32_bf16 v[108:111], a[8:11], v[76:79], 0\n\t"
    "ds_read_b64_tr_b16 a[34:35], v180 offset:12288\n\t"
    "v_mfma_f32_16x16x32_bf16 v[112:115], a[12:15], v[224:227], 0\n\t"
    "ds_read_b64_tr_b16 a[36:37], v179 offset:12288\n\t"
    "v_mfma_f32_16x16x32_bf16 v[116:119], a[12:15], v[76:79], 0\n\t"
    "ds_read_b64_tr_b16 a[38:39], v181 offset:12288\n\t"
    "v_mfma_f32_16x16x32_bf16 v[104:107], a[16:19], v[228:231], v[104:107]\n\t"
    "ds_read_b64_tr_b16 a[40:41], v180\n\t"
    "v_mfma_f32_16x16x32_bf16 v[108:111], a[16:19], v[80:83], v[108:111]\n\t"
    "ds_read_b64_tr_b16 a[42:43], v180 offset:2048\n\t"
    "v_mfma_f32_16x16x32_bf16 v[112:115], a[20:23], v[228:231], v[112:115]\n\t"
    "ds_read_b64_tr_b16 a[44:45], v181\n\t"
    "v_mfma_f32_16x16x32_bf16 v[116:119], a[20:23], v[80:83], v[116:119]\n\t"
    "ds_read_b64_tr_b16 a[46:47], v181 offset:2048\n\t"
    "v_mfma_f32_16x16x32_bf16 v[104:107], a[24:27], v[232:235], v[104:107]\n\t"
    "ds_read_b64_tr_b16 a[48:49], v180 offset:4096\n\t"
    "v_mfma_f32_16x16x32_bf16 v[108:111], a[24:27], v[84:87], v[108:111]\n\t"
    "ds_read_b64_tr_b16 a[50:51], v180 offset:6144\n\t"
    "v_mfma_f32_16x16x32_bf16 v[112:115], a[28:31], v[232:235], v[112:115]\n\t"
    "ds_read_b64_tr_b16 a[52:53], v181 offset:4096\n\t"
    "v_mfma_f32_16x16x32_bf16 v[116:119], a[28:31], v[84:87], v[116:119]\n\t"
    "ds_read_b64_tr_b16 a[54:55], v181 offset:6144\n\t"
    "s_waitcnt lgkmcnt(10)\n\t"
    "v_mfma_f32_16x16x32_bf16 v[104:107], a[32:35], v[236:239], v[104:107]\n\t"
    "ds_read_b64_tr_b16 a[56:57], v180 offset:8192\n\t"
    "v_mfma_f32_16x16x32_bf16 v[108:111], a[32:35], v[88:91], v[108:111]\n\t"
    "ds_read_b64_tr_b16 a[58:59], v180 offset:10240\n\t"
    "s_waitcnt lgkmcnt(10)\n\t"
    "v_mfma_f32_16x16x32_bf16 v[112:115], a[36:39], v[236:239], v[112:115]\n\t"
    "s_waitcnt vmcnt(14)\n\t"
    "s_barrier\n\t"
    "ds_read_b64_tr_b16 a[60:61], v181 offset:8192\n\t"
    "v_mfma_f32_16x16x32_bf16 v[116:119], a[36:39], v[88:91], v[116:119]\n\t"
    "s_add_u32 m0, s63, 0x20000\n\t"
    "s_add_u32 s61, s66, 0x200\n\t"
    "ds_read_b64_tr_b16 a[62:63], v181 offset:10240\n\t"
    "s_waitcnt lgkmcnt(10)\n\t"
    "v_mfma_f32_16x16x32_bf16 v[104:107], a[40:43], v[240:243], v[104:107]\n\t"
    "buffer_load_dwordx4 v16, %[srdw2], s61 offen lds\n\t"
    "ds_read_b64_tr_b16 a[0:1], v182\n\t"
    "v_mfma_f32_16x16x32_bf16 v[108:111], a[40:43], v[92:95], v[108:111]\n\t"
    "ds_read_b64_tr_b16 a[2:3], v182 offset:2048\n\t"
    "s_waitcnt lgkmcnt(10)\n\t"
    "v_mfma_f32_16x16x32_bf16 v[112:115], a[44:47], v[240:243], v[112:115]\n\t"
    "ds_read_b64_tr_b16 a[4:5], v183\n\t"
    "v_mfma_f32_16x16x32_bf16 v[116:119], a[44:47], v[92:95], v[116:119]\n\t"
    "ds_read_b64_tr_b16 a[6:7], v183 offset:2048\n\t"
    "s_waitcnt lgkmcnt(10)\n\t"
    "v_mfma_f32_16x16x32_bf16 v[104:107], a[48:51], v[244:247], v[104:107]\n\t"
    "buffer_load_dwordx4 v17, %[srdw2], s61 offen offset:1024 lds\n\t"
    "ds_read_b64_tr_b16 a[8:9], v182 offset:4096\n\t"
    "v_mfma_f32_16x16x32_bf16 v[108:111], a[48:51], v[96:99], v[108:111]\n\t"
    "ds_read_b64_tr_b16 a[10:11], v182 offset:6144\n\t"
    "s_waitcnt lgkmcnt(10)\n\t"
    "v_mfma_f32_16x16x32_bf16 v[112:115], a[52:55], v[244:247], v[112:115]\n\t"
    "ds_read_b64_tr_b16 a[12:13], v183 offset:4096\n\t"
    "v_mfma_f32_16x16x32_bf16 v[116:119], a[52:55], v[96:99], v[116:119]\n\t"
    "ds_read_b64_tr_b16 a[14:15], v183 offset:6144\n\t"
    "s_waitcnt lgkmcnt(10)\n\t"
    "v_mfma_f32_16x16x32_bf16 v[104:107], a[56:59], v[248:251], v[104:107]\n\t"
    "buffer_load_dwordx4 v18, %[srdw2], s61 offen offset:2048 lds\n\t"
    "ds_read_b64_tr_b16 a[16:17], v182 offset:8192\n\t"
    "v_mfma_f32_16x16x32_bf16 v[108:111], a[56:59], v[100:103], v[108:111]\n\t"
    "ds_read_b64_tr_b16 a[18:19], v182 offset:10240\n\t"
    "s_waitcnt lgkmcnt(10)\n\t"
    "v_mfma_f32_16x16x32_bf16 v[112:115], a[60:63], v[248:251], v[112:115]\n\t"
    "ds_read_b64_tr_b16 a[20:21], v183 offset:8192\n\t"
    "v_mfma_f32_16x16x32_bf16 v[116:119], a[60:63], v[100:103], v[116:119]\n\t"
    "ds_read_b64_tr_b16 a[22:23], v183 offset:10240\n\t"
    "s_waitcnt lgkmcnt(10)\n\t"
    "v_mfma_f32_16x16x32_bf16 v[120:123], a[0:3], v[224:227], 0\n\t"
    "ds_read_b64_tr_b16 a[24:25], v182 offset:12288\n\t"
    "v_mfma_f32_16x16x32_bf16 v[124:127], a[0:3], v[76:79], 0\n\t"
    "buffer_load_dwordx4 v19, %[srdw2], s61 offen offset:3072 lds\n\t"
    "ds_read_b64_tr_b16 a[26:27], v184 offset:12288\n\t"
    "s_waitcnt lgkmcnt(10)\n\t"
    "v_mfma_f32_16x16x32_bf16 v[128:131], a[4:7], v[224:227], 0\n\t"
    "ds_read_b64_tr_b16 a[28:29], v183 offset:12288\n\t"
    "v_mfma_f32_16x16x32_bf16 v[132:135], a[4:7], v[76:79], 0\n\t"
    "s_add_u32 m0, s63, 0x21000\n\t"
    "s_add_u32 s61, s66, 0x7200\n\t"
    "ds_read_b64_tr_b16 a[30:31], v185 offset:12288\n\t"
    "v_cvt_pk_bf16_f32 v136, v104, v105\n\t"
    "s_waitcnt lgkmcnt(10)\n\t"
    "v_mfma_f32_16x16x32_bf16 v[120:123], a[8:11], v[228:231], v[120:123]\n\t"
    "buffer_load_dwordx4 v16, %[srdw2], s61 offen lds\n\t"
    "ds_read_b64_tr_b16 a[32:33], v184\n\t"
    "v_cvt_pk_bf16_f32 v137, v106, v107\n\t"
    "v_mfma_f32_16x16x32_bf16 v[124:127], a[8:11], v[80:83], v[124:127]\n\t"
    "ds_read_b64_tr_b16 a[34:35], v184 offset:2048\n\t"
    "v_cvt_pk_bf16_f32 v138, v112, v113\n\t"
    "s_waitcnt lgkmcnt(10)\n\t"
    "v_mfma_f32_16x16x32_bf16 v[128:131], a[12:15], v[228:231], v[128:131]\n\t"
    "ds_read_b64_tr_b16 a[36:37], v185\n\t"
    "v_cvt_pk_bf16_f32 v139, v114, v115\n\t"
    "v_mfma_f32_16x16x32_bf16 v[132:135], a[12:15], v[80:83], v[132:135]\n\t"
    "buffer_load_dwordx4 v17, %[srdw2], s61 offen offset:1024 lds\n\t"
    "ds_read_b64_tr_b16 a[38:39], v185 offset:2048\n\t"
    "v_cvt_pk_bf16_f32 v140, v108, v109\n\t"
    "s_waitcnt lgkmcnt(10)\n\t"
    "v_mfma_f32_16x16x32_bf16 v[120:123], a[16:19], v[232:235], v[120:123]\n\t"
    "ds_read_b64_tr_b16 a[40:41], v184 offset:4096\n\t"
    "v_cvt_pk_bf16_f32 v141, v110, v111\n\t"
    "v_mfma_f32_16x16x32_bf16 v[124:127], a[16:19], v[84:87], v[124:127]\n\t"
    "ds_read_b64_tr_b16 a[42:43], v184 offset:6144\n\t"
    "v_cvt_pk_bf16_f32 v142, v116, v117\n\t"
    "s_waitcnt lgkmcnt(10)\n\t"
    "v_mfma_f32_16x16x32_bf16 v[128:131], a[20:23], v[232:235], v[128:131]\n\t"
    "buffer_load_dwordx4 v18, %[srdw2], s61 offen offset:2048 lds\n\t"
    "ds_read_b64_tr_b16 a[44:45], v185 offset:4096\n\t"
    "v_cvt_pk_bf16_f32 v143, v118, v119\n\t"
    "s_movk_i32 s62, 0x0\n\t"
    "v_mfma_f32_16x16x32_bf16 v[132:135], a[20:23], v[84:87], v[132:135]\n\t"
    "ds_read_b64_tr_b16 a[46:47], v185 offset:6144\n\t"
    "s_waitcnt lgkmcnt(10)\n\t"
    "v_mfma_f32_16x16x32_bf16 v[120:123], a[24:27], v[236:239], v[120:123]\n\t"
    "ds_read_b64_tr_b16 a[48:49], v184 offset:8192\n\t"
    "buffer_store_dwordx4 v[136:139], v23, %[srdpt], s62 offen sc1\n\t"
    "v_mfma_f32_16x16x32_bf16 v[124:127], a[24:27], v[88:91], v[124:127]\n\t"
    "ds_read_b64_tr_b16 a[50:51], v184 offset:10240\n\t"
    "buffer_store_dwordx4 v[140:143], v23, %[srdpt], s62 offen offset:256 sc1\n\t"
    "s_waitcnt lgkmcnt(10)\n\t"
    "v_mfma_f32_16x16x32_bf16 v[128:131], a[28:31], v[236:239], v[128:131]\n\t"
    "s_waitcnt vmcnt(16)\n\t"
    "s_barrier\n\t"
    "ds_read_b64_tr_b16 a[52:53], v185 offset:8192\n\t"
    "v_mfma_f32_16x16x32_bf16 v[132:135], a[28:31], v[88:91], v[132:135]\n\t"
    "s_add_u32 m0, s63, 0x10000\n\t"
    "s_add_u32 s61, s66, 0x280\n\t"
    "ds_read_b64_tr_b16 a[54:55], v185 offset:10240\n\t"
    "s_waitcnt lgkmcnt(10)\n\t"
    "v_mfma_f32_16x16x32_bf16 v[120:123], a[32:35], v[240:243], v[120:123]\n\t"
    "buffer_load_dwordx4 v16, %[srdw2], s61 offen lds\n\t"
    "ds_read_b64_tr_b16 a[56:57], v186\n\t"
    "v_mfma_f32_16x16x32_bf16 v[124:127], a[32:35], v[92:95], v[124:127]\n\t"
    "ds_read_b64_tr_b16 a[58:59], v186 offset:2048\n\t"
    "s_waitcnt lgkmcnt(10)\n\t"
    "v_mfma_f32_16x16x32_bf16 v[128:131], a[36:39], v[240:243], v[128:131]\n\t"
    "ds_read_b64_tr_b16 a[60:61], v187\n\t"
    "v_mfma_f32_16x16x32_bf16 v[132:135], a[36:39], v[92:95], v[132:135]\n\t"
    "ds_read_b64_tr_b16 a[62:63], v187 offset:2048\n\t"
    "s_waitcnt lgkmcnt(10)\n\t"
    "v_mfma_f32_16x16x32_bf16 v[120:123], a[40:43], v[244:247], v[120:123]\n\t"
    "buffer_load_dwordx4 v17, %[srdw2], s61 offen offset:1024 lds\n\t"
    "ds_read_b64_tr_b16 a[0:1], v186 offset:4096\n\t"
    "v_mfma_f32_16x16x32_bf16 v[124:127], a[40:43], v[96:99], v[124:127]\n\t"
    "ds_read_b64_tr_b16 a[2:3], v186 offset:6144\n\t"
    "s_waitcnt lgkmcnt(10)\n\t"
    "v_mfma_f32_16x16x32_bf16 v[128:131], a[44:47], v[244:247], v[128:131]\n\t"
    "ds_read_b64_tr_b16 a[4:5], v187 offset:4096\n\t"
    "v_mfma_f32_16x16x32_bf16 v[132:135], a[44:47], v[96:99], v[132:135]\n\t"
    "ds_read_b64_tr_b16 a[6:7], v187 offset:6144\n\t"
    "s_waitcnt lgkmcnt(10)\n\t"
    "v_mfma_f32_16x16x32_bf16 v[120:123], a[48:51], v[248:251], v[120:123]\n\t"
    "buffer_load_dwordx4 v18, %[srdw2], s61 offen offset:2048 lds\n\t"
    "ds_read_b64_tr_b16 a[8:9], v186 offset:8192\n\t"
    "v_mfma_f32_16x16x32_bf16 v[124:127], a[48:51], v[100:103], v[124:127]\n\t"
    "ds_read_b64_tr_b16 a[10:11], v186 offset:10240\n\t"
    "s_waitcnt lgkmcnt(10)\n\t"
    "v_mfma_f32_16x16x32_bf16 v[128:131], a[52:55], v[248:251], v[128:131]\n\t"
    "ds_read_b64_tr_b16 a[12:13], v187 offset:8192\n\t"
    "v_mfma_f32_16x16x32_bf16 v[132:135], a[52:55], v[100:103], v[132:135]\n\t"
    "ds_read_b64_tr_b16 a[14:15], v187 offset:10240\n\t"
    "s_waitcnt lgkmcnt(10)\n\t"
    "v_mfma_f32_16x16x32_bf16 v[104:107], a[56:59], v[224:227], 0\n\t"
    "ds_read_b64_tr_b16 a[16:17], v186 offset:12288\n\t"
    "v_mfma_f32_16x16x32_bf16 v[108:111], a[56:59], v[76:79], 0\n\t"
    "buffer_load_dwordx4 v19, %[srdw2], s61 offen offset:3072 lds\n\t"
    "ds_read_b64_tr_b16 a[18:19], v188 offset:12288\n\t"
    "s_waitcnt lgkmcnt(10)\n\t"
    "v_mfma_f32_16x16x32_bf16 v[112:115], a[60:63], v[224:227], 0\n\t"
    "ds_read_b64_tr_b16 a[20:21], v187 offset:12288\n\t"
    "v_mfma_f32_16x16x32_bf16 v[116:119], a[60:63], v[76:79], 0\n\t"
    "s_add_u32 m0, s63, 0x11000\n\t"
    "s_add_u32 s61, s66, 0x7280\n\t"
    "ds_read_b64_tr_b16 a[22:23], v189 offset:12288\n\t"
    "v_cvt_pk_bf16_f32 v144, v120, v121\n\t"
    "s_waitcnt lgkmcnt(10)\n\t"
    "v_mfma_f32_16x16x32_bf16 v[104:107], a[0:3], v[228:231], v[104:107]\n\t"
    "buffer_load_dwordx4 v16, %[srdw2], s61 offen lds\n\t"
    "ds_read_b64_tr_b16 a[24:25], v188\n\t"
    "v_cvt_pk_bf16_f32 v145, v122, v123\n\t"
    "v_mfma_f32_16x16x32_bf16 v[108:111], a[0:3], v[80:83], v[108:111]\n\t"
    "ds_read_b64_tr_b16 a[26:27], v188 offset:2048\n\t"
    "v_cvt_pk_bf16_f32 v146, v128, v129\n\t"
    "s_waitcnt lgkmcnt(10)\n\t"
    "v_mfma_f32_16x16x32_bf16 v[112:115], a[4:7], v[228:231], v[112:115]\n\t"
    "ds_read_b64_tr_b16 a[28:29], v189\n\t"
    "v_cvt_pk_bf16_f32 v147, v130, v131\n\t"
    "v_mfma_f32_16x16x32_bf16 v[116:119], a[4:7], v[80:83], v[116:119]\n\t"
    "buffer_load_dwordx4 v17, %[srdw2], s61 offen offset:1024 lds\n\t"
    "ds_read_b64_tr_b16 a[30:31], v189 offset:2048\n\t"
    "v_cvt_pk_bf16_f32 v148, v124, v125\n\t"
    "s_waitcnt lgkmcnt(10)\n\t"
    "v_mfma_f32_16x16x32_bf16 v[104:107], a[8:11], v[232:235], v[104:107]\n\t"
    "ds_read_b64_tr_b16 a[32:33], v188 offset:4096\n\t"
    "v_cvt_pk_bf16_f32 v149, v126, v127\n\t"
    "v_mfma_f32_16x16x32_bf16 v[108:111], a[8:11], v[84:87], v[108:111]\n\t"
    "ds_read_b64_tr_b16 a[34:35], v188 offset:6144\n\t"
    "v_cvt_pk_bf16_f32 v150, v132, v133\n\t"
    "s_waitcnt lgkmcnt(10)\n\t"
    "v_mfma_f32_16x16x32_bf16 v[112:115], a[12:15], v[232:235], v[112:115]\n\t"
    "buffer_load_dwordx4 v18, %[srdw2], s61 offen offset:2048 lds\n\t"
    "ds_read_b64_tr_b16 a[36:37], v189 offset:4096\n\t"
    "v_cvt_pk_bf16_f32 v151, v134, v135\n\t"
    "s_movk_i32 s62, 0x1000\n\t"
    "v_mfma_f32_16x16x32_bf16 v[116:119], a[12:15], v[84:87], v[116:119]\n\t"
    "ds_read_b64_tr_b16 a[38:39], v189 offset:6144\n\t"
    "s_waitcnt lgkmcnt(10)\n\t"
    "v_mfma_f32_16x16x32_bf16 v[104:107], a[16:19], v[236:239], v[104:107]\n\t"
    "ds_read_b64_tr_b16 a[40:41], v188 offset:8192\n\t"
    "buffer_store_dwordx4 v[144:147], v23, %[srdpt], s62 offen sc1\n\t"
    "v_mfma_f32_16x16x32_bf16 v[108:111], a[16:19], v[88:91], v[108:111]\n\t"
    "ds_read_b64_tr_b16 a[42:43], v188 offset:10240\n\t"
    "buffer_store_dwordx4 v[148:151], v23, %[srdpt], s62 offen offset:256 sc1\n\t"
    "s_waitcnt lgkmcnt(10)\n\t"
    "v_mfma_f32_16x16x32_bf16 v[112:115], a[20:23], v[236:239], v[112:115]\n\t"
    "s_waitcnt vmcnt(18)\n\t"
    "s_barrier\n\t"
    "ds_read_b64_tr_b16 a[44:45], v189 offset:8192\n\t"
    "v_mfma_f32_16x16x32_bf16 v[116:119], a[20:23], v[88:91], v[116:119]\n\t"
    "s_add_u32 m0, s63, 0x0\n\t"
    "s_add_u32 s61, s66, 0x300\n\t"
    "ds_read_b64_tr_b16 a[46:47], v189 offset:10240\n\t"
    "s_waitcnt lgkmcnt(10)\n\t"
    "v_mfma_f32_16x16x32_bf16 v[104:107], a[24:27], v[240:243], v[104:107]\n\t"
    "buffer_load_dwordx4 v16, %[srdw2], s61 offen lds\n\t"
    "ds_read_b64_tr_b16 a[48:49], v174\n\t"
    "v_mfma_f32_16x16x32_bf16 v[108:111], a[24:27], v[92:95], v[108:111]\n\t"
    "ds_read_b64_tr_b16 a[50:51], v174 offset:2048\n\t"
    "s_waitcnt lgkmcnt(10)\n\t"
    "v_mfma_f32_16x16x32_bf16 v[112:115], a[28:31], v[240:243], v[112:115]\n\t"
    "ds_read_b64_tr_b16 a[52:53], v175\n\t"
    "v_mfma_f32_16x16x32_bf16 v[116:119], a[28:31], v[92:95], v[116:119]\n\t"
    "ds_read_b64_tr_b16 a[54:55], v175 offset:2048\n\t"
    "s_waitcnt lgkmcnt(10)\n\t"
    "v_mfma_f32_16x16x32_bf16 v[104:107], a[32:35], v[244:247], v[104:107]\n\t"
    "buffer_load_dwordx4 v17, %[srdw2], s61 offen offset:1024 lds\n\t"
    "ds_read_b64_tr_b16 a[56:57], v174 offset:4096\n\t"
    "v_mfma_f32_16x16x32_bf16 v[108:111], a[32:35], v[96:99], v[108:111]\n\t"
    "ds_read_b64_tr_b16 a[58:59], v174 offset:6144\n\t"
    "s_waitcnt lgkmcnt(10)\n\t"
    "v_mfma_f32_16x16x32_bf16 v[112:115], a[36:39], v[244:247], v[112:115]\n\t"
    "ds_read_b64_tr_b16 a[60:61], v175 offset:4096\n\t"
    "v_mfma_f32_16x16x32_bf16 v[116:119], a[36:39], v[96:99], v[116:119]\n\t"
    "ds_read_b64_tr_b16 a[62:63], v175 offset:6144\n\t"
    "s_waitcnt lgkmcnt(10)\n\t"
    "v_mfma_f32_16x16x32_bf16 v[104:107], a[40:43], v[248:251], v[104:107]\n\t"
    "buffer_load_dwordx4 v18, %[srdw2], s61 offen offset:2048 lds\n\t"
    "ds_read_b64_tr_b16 a[0:1], v174 offset:8192\n\t"
    "v_mfma_f32_16x16x32_bf16 v[108:111], a[40:43], v[100:103], v[108:111]\n\t"
    "ds_read_b64_tr_b16 a[2:3], v174 offset:10240\n\t"
    "s_waitcnt lgkmcnt(10)\n\t"
    "v_mfma_f32_16x16x32_bf16 v[112:115], a[44:47], v[248:251], v[112:115]\n\t"
    "ds_read_b64_tr_b16 a[4:5], v175 offset:8192\n\t"
    "v_mfma_f32_16x16x32_bf16 v[116:119], a[44:47], v[100:103], v[116:119]\n\t"
    "ds_read_b64_tr_b16 a[6:7], v175 offset:10240\n\t"
    "s_waitcnt lgkmcnt(10)\n\t"
    "v_mfma_f32_16x16x32_bf16 v[120:123], a[48:51], v[224:227], 0\n\t"
    "ds_read_b64_tr_b16 a[8:9], v174 offset:12288\n\t"
    "v_mfma_f32_16x16x32_bf16 v[124:127], a[48:51], v[76:79], 0\n\t"
    "buffer_load_dwordx4 v19, %[srdw2], s61 offen offset:3072 lds\n\t"
    "ds_read_b64_tr_b16 a[10:11], v176 offset:12288\n\t"
    "s_waitcnt lgkmcnt(10)\n\t"
    "v_mfma_f32_16x16x32_bf16 v[128:131], a[52:55], v[224:227], 0\n\t"
    "ds_read_b64_tr_b16 a[12:13], v175 offset:12288\n\t"
    "v_mfma_f32_16x16x32_bf16 v[132:135], a[52:55], v[76:79], 0\n\t"
    "s_add_u32 m0, s63, 0x1000\n\t"
    "s_add_u32 s61, s66, 0x7300\n\t"
    "ds_read_b64_tr_b16 a[14:15], v177 offset:12288\n\t"
    "v_cvt_pk_bf16_f32 v136, v104, v105\n\t"
    "s_waitcnt lgkmcnt(10)\n\t"
    "v_mfma_f32_16x16x32_bf16 v[120:123], a[56:59], v[228:231], v[120:123]\n\t"
    "buffer_load_dwordx4 v16, %[srdw2], s61 offen lds\n\t"
    "ds_read_b64_tr_b16 a[16:17], v176\n\t"
    "v_cvt_pk_bf16_f32 v137, v106, v107\n\t"
    "v_mfma_f32_16x16x32_bf16 v[124:127], a[56:59], v[80:83], v[124:127]\n\t"
    "ds_read_b64_tr_b16 a[18:19], v176 offset:2048\n\t"
    "v_cvt_pk_bf16_f32 v138, v112, v113\n\t"
    "s_waitcnt lgkmcnt(10)\n\t"
    "v_mfma_f32_16x16x32_bf16 v[128:131], a[60:63], v[228:231], v[128:131]\n\t"
    "ds_read_b64_tr_b16 a[20:21], v177\n\t"
    "v_cvt_pk_bf16_f32 v139, v114, v115\n\t"
    "v_mfma_f32_16x16x32_bf16 v[132:135], a[60:63], v[80:83], v[132:135]\n\t"
    "buffer_load_dwordx4 v17, %[srdw2], s61 offen offset:1024 lds\n\t"
    "ds_read_b64_tr_b16 a[22:23], v177 offset:2048\n\t"
    "v_cvt_pk_bf16_f32 v140, v108, v109\n\t"
    "s_waitcnt lgkmcnt(10)\n\t"
    "v_mfma_f32_16x16x32_bf16 v[120:123], a[0:3], v[232:235], v[120:123]\n\t"
    "ds_read_b64_tr_b16 a[24:25], v176 offset:4096\n\t"
    "v_cvt_pk_bf16_f32 v141, v110, v111\n\t"
    "v_mfma_f32_16x16x32_bf16 v[124:127], a[0:3], v[84:87], v[124:127]\n\t"
    "ds_read_b64_tr_b16 a[26:27], v176 offset:6144\n\t"
    "v_cvt_pk_bf16_f32 v142, v116, v117\n\t"
    "s_waitcnt lgkmcnt(10)\n\t"
    "v_mfma_f32_16x16x32_bf16 v[128:131], a[4:7], v[232:235], v[128:131]\n\t"
    "buffer_load_dwordx4 v18, %[srdw2], s61 offen offset:2048 lds\n\t"
    "ds_read_b64_tr_b16 a[28:29], v177 offset:4096\n\t"
    "v_cvt_pk_bf16_f32 v143, v118, v119\n\t"
    "s_movk_i32 s62, 0x2000\n\t"
    "v_mfma_f32_16x16x32_bf16 v[132:135], a[4:7], v[84:87], v[132:135]\n\t"
    "ds_read_b64_tr_b16 a[30:31], v177 offset:6144\n\t"
    "s_waitcnt lgkmcnt(10)\n\t"
    "v_mfma_f32_16x16x32_bf16 v[120:123], a[8:11], v[236:239], v[120:123]\n\t"
    "ds_read_b64_tr_b16 a[32:33], v176 offset:8192\n\t"
    "buffer_store_dwordx4 v[136:139], v23, %[srdpt], s62 offen sc1\n\t"
    "v_mfma_f32_16x16x32_bf16 v[124:127], a[8:11], v[88:91], v[124:127]\n\t"
    "ds_read_b64_tr_b16 a[34:35], v176 offset:10240\n\t"
    "buffer_store_dwordx4 v[140:143], v23, %[srdpt], s62 offen offset:256 sc1\n\t"
    "s_waitcnt lgkmcnt(10)\n\t"
    "v_mfma_f32_16x16x32_bf16 v[128:131], a[12:15], v[236:239], v[128:131]\n\t"
    "s_waitcnt vmcnt(20)\n\t"
    "s_barrier\n\t"
    "ds_read_b64_tr_b16 a[36:37], v177 offset:8192\n\t"
    "v_mfma_f32_16x16x32_bf16 v[132:135], a[12:15], v[88:91], v[132:135]\n\t"
    "ds_read_b64_tr_b16 a[38:39], v177 offset:10240\n\t"
    "s_waitcnt lgkmcnt(10)\n\t"
    "v_mfma_f32_16x16x32_bf16 v[120:123], a[16:19], v[240:243], v[120:123]\n\t"
    "ds_read_b64_tr_b16 a[40:41], v170\n\t"
    "v_mfma_f32_16x16x32_bf16 v[124:127], a[16:19], v[92:95], v[124:127]\n\t"
    "ds_read_b64_tr_b16 a[42:43], v170 offset:2048\n\t"
    "s_waitcnt lgkmcnt(10)\n\t"
    "v_mfma_f32_16x16x32_bf16 v[128:131], a[20:23], v[240:243], v[128:131]\n\t"
    "ds_read_b64_tr_b16 a[44:45], v171\n\t"
    "v_mfma_f32_16x16x32_bf16 v[132:135], a[20:23], v[92:95], v[132:135]\n\t"
    "ds_read_b64_tr_b16 a[46:47], v171 offset:2048\n\t"
    "s_waitcnt lgkmcnt(10)\n\t"
    "v_mfma_f32_16x16x32_bf16 v[120:123], a[24:27], v[244:247], v[120:123]\n\t"
    "ds_read_b64_tr_b16 a[48:49], v170 offset:4096\n\t"
    "v_mfma_f32_16x16x32_bf16 v[124:127], a[24:27], v[96:99], v[124:127]\n\t"
    "ds_read_b64_tr_b16 a[50:51], v170 offset:6144\n\t"
    "s_waitcnt lgkmcnt(10)\n\t"
    "v_mfma_f32_16x16x32_bf16 v[128:131], a[28:31], v[244:247], v[128:131]\n\t"
    "ds_read_b64_tr_b16 a[52:53], v171 offset:4096\n\t"
    "v_mfma_f32_16x16x32_bf16 v[132:135], a[28:31], v[96:99], v[132:135]\n\t"
    "ds_read_b64_tr_b16 a[54:55], v171 offset:6144\n\t"
    "s_waitcnt lgkmcnt(10)\n\t"
    "v_mfma_f32_16x16x32_bf16 v[120:123], a[32:35], v[248:251], v[120:123]\n\t"
    "ds_read_b64_tr_b16 a[56:57], v170 offset:8192\n\t"
    "v_mfma_f32_16x16x32_bf16 v[124:127], a[32:35], v[100:103], v[124:127]\n\t"
    "ds_read_b64_tr_b16 a[58:59], v170 offset:10240\n\t"
    "s_waitcnt lgkmcnt(10)\n\t"
    "v_mfma_f32_16x16x32_bf16 v[128:131], a[36:39], v[248:251], v[128:131]\n\t"
    "ds_read_b64_tr_b16 a[60:61], v171 offset:8192\n\t"
    "v_mfma_f32_16x16x32_bf16 v[132:135], a[36:39], v[100:103], v[132:135]\n\t"
    "ds_read_b64_tr_b16 a[62:63], v171 offset:10240\n\t"
    "s_waitcnt lgkmcnt(10)\n\t"
    "v_mfma_f32_16x16x32_bf16 v[104:107], a[40:43], v[224:227], 0\n\t"
    "ds_read_b64_tr_b16 a[0:1], v170 offset:12288\n\t"
    "v_mfma_f32_16x16x32_bf16 v[108:111], a[40:43], v[76:79], 0\n\t"
    "ds_read_b64_tr_b16 a[2:3], v172 offset:12288\n\t"
    "s_waitcnt lgkmcnt(10)\n\t"
    "v_mfma_f32_16x16x32_bf16 v[112:115], a[44:47], v[224:227], 0\n\t"
    "ds_read_b64_tr_b16 a[4:5], v171 offset:12288\n\t"
    "v_mfma_f32_16x16x32_bf16 v[116:119], a[44:47], v[76:79], 0\n\t"
    "ds_read_b64_tr_b16 a[6:7], v173 offset:12288\n\t"
    "v_cvt_pk_bf16_f32 v144, v120, v121\n\t"
    "s_waitcnt lgkmcnt(10)\n\t"
    "v_mfma_f32_16x16x32_bf16 v[104:107], a[48:51], v[228:231], v[104:107]\n\t"
    "ds_read_b64_tr_b16 a[8:9], v172\n\t"
    "v_cvt_pk_bf16_f32 v145, v122, v123\n\t"
    "v_mfma_f32_16x16x32_bf16 v[108:111], a[48:51], v[80:83], v[108:111]\n\t"
    "ds_read_b64_tr_b16 a[10:11], v172 offset:2048\n\t"
    "v_cvt_pk_bf16_f32 v146, v128, v129\n\t"
    "s_waitcnt lgkmcnt(10)\n\t"
    "v_mfma_f32_16x16x32_bf16 v[112:115], a[52:55], v[228:231], v[112:115]\n\t"
    "ds_read_b64_tr_b16 a[12:13], v173\n\t"
    "v_cvt_pk_bf16_f32 v147, v130, v131\n\t"
    "v_mfma_f32_16x16x32_bf16 v[116:119], a[52:55], v[80:83], v[116:119]\n\t"
    "ds_read_b64_tr_b16 a[14:15], v173 offset:2048\n\t"
    "v_cvt_pk_bf16_f32 v148, v124, v125\n\t"
    "s_waitcnt lgkmcnt(10)\n\t"
    "v_mfma_f32_16x16x32_bf16 v[104:107], a[56:59], v[232:235], v[104:107]\n\t"
    "ds_read_b64_tr_b16 a[16:17], v172 offset:4096\n\t"
    "v_cvt_pk_bf16_f32 v149, v126, v127\n\t"
    "v_mfma_f32_16x16x32_bf16 v[108:111], a[56:59], v[84:87], v[108:111]\n\t"
    "ds_read_b64_tr_b16 a[18:19], v172 offset:6144\n\t"
    "v_cvt_pk_bf16_f32 v150, v132, v133\n\t"
    "s_waitcnt lgkmcnt(10)\n\t"
    "v_mfma_f32_16x16x32_bf16 v[112:115], a[60:63], v[232:235], v[112:115]\n\t"
    "ds_read_b64_tr_b16 a[20:21], v173 offset:4096\n\t"
    "v_cvt_pk_bf16_f32 v151, v134, v135\n\t"
    "s_movk_i32 s62, 0x3000\n\t"
    "v_mfma_f32_16x16x32_bf16 v[116:119], a[60:63], v[84:87], v[116:119]\n\t"
    "ds_read_b64_tr_b16 a[22:23], v173 offset:6144\n\t"
    "s_waitcnt lgkmcnt(10)\n\t"
    "v_mfma_f32_16x16x32_bf16 v[104:107], a[0:3], v[236:239], v[104:107]\n\t"
    "ds_read_b64_tr_b16 a[24:25], v172 offset:8192\n\t"
    "buffer_store_dwordx4 v[144:147], v23, %[srdpt], s62 offen sc1\n\t"
    "v_mfma_f32_16x16x32_bf16 v[108:111], a[0:3], v[88:91], v[108:111]\n\t"
    "ds_read_b64_tr_b16 a[26:27], v172 offset:10240\n\t"
    "buffer_store_dwordx4 v[148:151], v23, %[srdpt], s62 offen offset:256 sc1\n\t"
    "s_waitcnt lgkmcnt(10)\n\t"
    "v_mfma_f32_16x16x32_bf16 v[112:115], a[4:7], v[236:239], v[112:115]\n\t"
    "s_waitcnt vmcnt(13)\n\t"
    "s_barrier\n\t"
    "ds_read_b64_tr_b16 a[28:29], v173 offset:8192\n\t"
    "v_mfma_f32_16x16x32_bf16 v[116:119], a[4:7], v[88:91], v[116:119]\n\t"
    "ds_read_b64_tr_b16 a[30:31], v173 offset:10240\n\t"
    "s_waitcnt lgkmcnt(10)\n\t"
    "v_mfma_f32_16x16x32_bf16 v[104:107], a[8:11], v[240:243], v[104:107]\n\t"
    "ds_read_b64_tr_b16 a[32:33], v178\n\t"
    "v_mfma_f32_16x16x32_bf16 v[108:111], a[8:11], v[92:95], v[108:111]\n\t"
    "ds_read_b64_tr_b16 a[34:35], v178 offset:2048\n\t"
    "s_waitcnt lgkmcnt(10)\n\t"
    "v_mfma_f32_16x16x32_bf16 v[112:115], a[12:15], v[240:243], v[112:115]\n\t"
    "ds_read_b64_tr_b16 a[36:37], v179\n\t"
    "v_mfma_f32_16x16x32_bf16 v[116:119], a[12:15], v[92:95], v[116:119]\n\t"
    "ds_read_b64_tr_b16 a[38:39], v179 offset:2048\n\t"
    "s_waitcnt lgkmcnt(10)\n\t"
    "v_mfma_f32_16x16x32_bf16 v[104:107], a[16:19], v[244:247], v[104:107]\n\t"
    "ds_read_b64_tr_b16 a[40:41], v178 offset:4096\n\t"
    "v_mfma_f32_16x16x32_bf16 v[108:111], a[16:19], v[96:99], v[108:111]\n\t"
    "ds_read_b64_tr_b16 a[42:43], v178 offset:6144\n\t"
    "s_waitcnt lgkmcnt(10)\n\t"
    "v_mfma_f32_16x16x32_bf16 v[112:115], a[20:23], v[244:247], v[112:115]\n\t"
    "ds_read_b64_tr_b16 a[44:45], v179 offset:4096\n\t"
    "v_mfma_f32_16x16x32_bf16 v[116:119], a[20:23], v[96:99], v[116:119]\n\t"
    "ds_read_b64_tr_b16 a[46:47], v179 offset:6144\n\t"
    "s_waitcnt lgkmcnt(10)\n\t"
    "v_mfma_f32_16x16x32_bf16 v[104:107], a[24:27], v[248:251], v[104:107]\n\t"
    "ds_read_b64_tr_b16 a[48:49], v178 offset:8192\n\t"
    "v_mfma_f32_16x16x32_bf16 v[108:111], a[24:27], v[100:103], v[108:111]\n\t"
    "ds_read_b64_tr_b16 a[50:51], v178 offset:10240\n\t"
    "s_waitcnt lgkmcnt(10)\n\t"
    "v_mfma_f32_16x16x32_bf16 v[112:115], a[28:31], v[248:251], v[112:115]\n\t"
    "ds_read_b64_tr_b16 a[52:53], v179 offset:8192\n\t"
    "v_mfma_f32_16x16x32_bf16 v[116:119], a[28:31], v[100:103], v[116:119]\n\t"
    "ds_read_b64_tr_b16 a[54:55], v179 offset:10240\n\t"
    "s_waitcnt lgkmcnt(10)\n\t"
    "v_mfma_f32_16x16x32_bf16 v[120:123], a[32:35], v[224:227], 0\n\t"
    "ds_read_b64_tr_b16 a[56:57], v178 offset:12288\n\t"
    "v_mfma_f32_16x16x32_bf16 v[124:127], a[32:35], v[76:79], 0\n\t"
    "ds_read_b64_tr_b16 a[58:59], v180 offset:12288\n\t"
    "s_waitcnt lgkmcnt(10)\n\t"
    "v_mfma_f32_16x16x32_bf16 v[128:131], a[36:39], v[224:227], 0\n\t"
    "ds_read_b64_tr_b16 a[60:61], v179 offset:12288\n\t"
    "v_mfma_f32_16x16x32_bf16 v[132:135], a[36:39], v[76:79], 0\n\t"
    "ds_read_b64_tr_b16 a[62:63], v181 offset:12288\n\t"
    "v_cvt_pk_bf16_f32 v136, v104, v105\n\t"
    "s_waitcnt lgkmcnt(10)\n\t"
    "v_mfma_f32_16x16x32_bf16 v[120:123], a[40:43], v[228:231], v[120:123]\n\t"
    "ds_read_b64_tr_b16 a[0:1], v180\n\t"
    "v_cvt_pk_bf16_f32 v137, v106, v107\n\t"
    "v_mfma_f32_16x16x32_bf16 v[124:127], a[40:43], v[80:83], v[124:127]\n\t"
    "ds_read_b64_tr_b16 a[2:3], v180 offset:2048\n\t"
    "v_cvt_pk_bf16_f32 v138, v112, v113\n\t"
    "s_waitcnt lgkmcnt(10)\n\t"
    "v_mfma_f32_16x16x32_bf16 v[128:131], a[44:47], v[228:231], v[128:131]\n\t"
    "ds_read_b64_tr_b16 a[4:5], v181\n\t"
    "v_cvt_pk_bf16_f32 v139, v114, v115\n\t"
    "v_mfma_f32_16x16x32_bf16 v[132:135], a[44:47], v[80:83], v[132:135]\n\t"
    "ds_read_b64_tr_b16 a[6:7], v181 offset:2048\n\t"
    "v_cvt_pk_bf16_f32 v140, v108, v109\n\t"
    "s_waitcnt lgkmcnt(10)\n\t"
    "v_mfma_f32_16x16x32_bf16 v[120:123], a[48:51], v[232:235], v[120:123]\n\t"
    "ds_read_b64_tr_b16 a[8:9], v180 offset:4096\n\t"
    "v_cvt_pk_bf16_f32 v141, v110, v111\n\t"
    "v_mfma_f32_16x16x32_bf16 v[124:127], a[48:51], v[84:87], v[124:127]\n\t"
    "ds_read_b64_tr_b16 a[10:11], v180 offset:6144\n\t"
    "v_cvt_pk_bf16_f32 v142, v116, v117\n\t"
    "s_waitcnt lgkmcnt(10)\n\t"
    "v_mfma_f32_16x16x32_bf16 v[128:131], a[52:55], v[232:235], v[128:131]\n\t"
    "ds_read_b64_tr_b16 a[12:13], v181 offset:4096\n\t"
    "v_cvt_pk_bf16_f32 v143, v118, v119\n\t"
    "s_movk_i32 s62, 0x4000\n\t"
    "v_mfma_f32_16x16x32_bf16 v[132:135], a[52:55], v[84:87], v[132:135]\n\t"
    "ds_read_b64_tr_b16 a[14:15], v181 offset:6144\n\t"
    "s_waitcnt lgkmcnt(10)\n\t"
    "v_mfma_f32_16x16x32_bf16 v[120:123], a[56:59], v[236:239], v[120:123]\n\t"
    "ds_read_b64_tr_b16 a[16:17], v180 offset:8192\n\t"
    "buffer_store_dwordx4 v[136:139], v23, %[srdpt], s62 offen sc1\n\t"
    "v_mfma_f32_16x16x32_bf16 v[124:127], a[56:59], v[88:91], v[124:127]\n\t"
    "ds_read_b64_tr_b16 a[18:19], v180 offset:10240\n\t"
    "buffer_store_dwordx4 v[140:143], v23, %[srdpt], s62 offen offset:256 sc1\n\t"
    "s_waitcnt lgkmcnt(10)\n\t"
    "v_mfma_f32_16x16x32_bf16 v[128:131], a[60:63], v[236:239], v[128:131]\n\t"
    "s_waitcnt vmcnt(6)\n\t"
    "s_barrier\n\t"
    "ds_read_b64_tr_b16 a[20:21], v181 offset:8192\n\t"
    "v_mfma_f32_16x16x32_bf16 v[132:135], a[60:63], v[88:91], v[132:135]\n\t"
    "ds_read_b64_tr_b16 a[22:23], v181 offset:10240\n\t"
    "s_waitcnt lgkmcnt(10)\n\t"
    "v_mfma_f32_16x16x32_bf16 v[120:123], a[0:3], v[240:243], v[120:123]\n\t"
    "ds_read_b64_tr_b16 a[24:25], v182\n\t"
    "v_mfma_f32_16x16x32_bf16 v[124:127], a[0:3], v[92:95], v[124:127]\n\t"
    "ds_read_b64_tr_b16 a[26:27], v182 offset:2048\n\t"
    "s_waitcnt lgkmcnt(10)\n\t"
    "v_mfma_f32_16x16x32_bf16 v[128:131], a[4:7], v[240:243], v[128:131]\n\t"
    "ds_read_b64_tr_b16 a[28:29], v183\n\t"
    "v_mfma_f32_16x16x32_bf16 v[132:135], a[4:7], v[92:95], v[132:135]\n\t"
    "ds_read_b64_tr_b16 a[30:31], v183 offset:2048\n\t"
    "s_waitcnt lgkmcnt(10)\n\t"
    "v_mfma_f32_16x16x32_bf16 v[120:123], a[8:11], v[244:247], v[120:123]\n\t"
    "ds_read_b64_tr_b16 a[32:33], v182 offset:4096\n\t"
    "v_mfma_f32_16x16x32_bf16 v[124:127], a[8:11], v[96:99], v[124:127]\n\t"
    "ds_read_b64_tr_b16 a[34:35], v182 offset:6144\n\t"
    "s_waitcnt lgkmcnt(10)\n\t"
    "v_mfma_f32_16x16x32_bf16 v[128:131], a[12:15], v[244:247], v[128:131]\n\t"
    "ds_read_b64_tr_b16 a[36:37], v183 offset:4096\n\t"
    "v_mfma_f32_16x16x32_bf16 v[132:135], a[12:15], v[96:99], v[132:135]\n\t"
    "ds_read_b64_tr_b16 a[38:39], v183 offset:6144\n\t"
    "s_waitcnt lgkmcnt(10)\n\t"
    "v_mfma_f32_16x16x32_bf16 v[120:123], a[16:19], v[248:251], v[120:123]\n\t"
    "ds_read_b64_tr_b16 a[40:41], v182 offset:8192\n\t"
    "v_mfma_f32_16x16x32_bf16 v[124:127], a[16:19], v[100:103], v[124:127]\n\t"
    "ds_read_b64_tr_b16 a[42:43], v182 offset:10240\n\t"
    "s_waitcnt lgkmcnt(10)\n\t"
    "v_mfma_f32_16x16x32_bf16 v[128:131], a[20:23], v[248:251], v[128:131]\n\t"
    "ds_read_b64_tr_b16 a[44:45], v183 offset:8192\n\t"
    "v_mfma_f32_16x16x32_bf16 v[132:135], a[20:23], v[100:103], v[132:135]\n\t"
    "ds_read_b64_tr_b16 a[46:47], v183 offset:10240\n\t"
    "s_waitcnt lgkmcnt(10)\n\t"
    "v_mfma_f32_16x16x32_bf16 v[104:107], a[24:27], v[224:227], 0\n\t"
    "ds_read_b64_tr_b16 a[48:49], v182 offset:12288\n\t"
    "v_mfma_f32_16x16x32_bf16 v[108:111], a[24:27], v[76:79], 0\n\t"
    "ds_read_b64_tr_b16 a[50:51], v184 offset:12288\n\t"
    "s_waitcnt lgkmcnt(10)\n\t"
    "v_mfma_f32_16x16x32_bf16 v[112:115], a[28:31], v[224:227], 0\n\t"
    "ds_read_b64_tr_b16 a[52:53], v183 offset:12288\n\t"
    "v_mfma_f32_16x16x32_bf16 v[116:119], a[28:31], v[76:79], 0\n\t"
    "ds_read_b64_tr_b16 a[54:55], v185 offset:12288\n\t"
    "v_cvt_pk_bf16_f32 v144, v120, v121\n\t"
    "s_waitcnt lgkmcnt(10)\n\t"
    "v_mfma_f32_16x16x32_bf16 v[104:107], a[32:35], v[228:231], v[104:107]\n\t"
    "ds_read_b64_tr_b16 a[56:57], v184\n\t"
    "v_cvt_pk_bf16_f32 v145, v122, v123\n\t"
    "v_mfma_f32_16x16x32_bf16 v[108:111], a[32:35], v[80:83], v[108:111]\n\t"
    "ds_read_b64_tr_b16 a[58:59], v184 offset:2048\n\t"
    "v_cvt_pk_bf16_f32 v146, v128, v129\n\t"
    "s_waitcnt lgkmcnt(10)\n\t"
    "v_mfma_f32_16x16x32_bf16 v[112:115], a[36:39], v[228:231], v[112:115]\n\t"
    "ds_read_b64_tr_b16 a[60:61], v185\n\t"
    "v_cvt_pk_bf16_f32 v147, v130, v131\n\t"
    "v_mfma_f32_16x16x32_bf16 v[116:119], a[36:39], v[80:83], v[116:119]\n\t"
    "ds_read_b64_tr_b16 a[62:63], v185 offset:2048\n\t"
    "v_cvt_pk_bf16_f32 v148, v124, v125\n\t"
    "s_waitcnt lgkmcnt(10)\n\t"
    "v_mfma_f32_16x16x32_bf16 v[104:107], a[40:43], v[232:235], v[104:107]\n\t"
    "ds_read_b64_tr_b16 a[0:1], v184 offset:4096\n\t"
    "v_cvt_pk_bf16_f32 v149, v126, v127\n\t"
    "v_mfma_f32_16x16x32_bf16 v[108:111], a[40:43], v[84:87], v[108:111]\n\t"
    "ds_read_b64_tr_b16 a[2:3], v184 offset:6144\n\t"
    "v_cvt_pk_bf16_f32 v150, v132, v133\n\t"
    "s_waitcnt lgkmcnt(10)\n\t"
    "v_mfma_f32_16x16x32_bf16 v[112:115], a[44:47], v[232:235], v[112:115]\n\t"
    "ds_read_b64_tr_b16 a[4:5], v185 offset:4096\n\t"
    "v_cvt_pk_bf16_f32 v151, v134, v135\n\t"
    "s_movk_i32 s62, 0x5000\n\t"
    "v_mfma_f32_16x16x32_bf16 v[116:119], a[44:47], v[84:87], v[116:119]\n\t"
    "ds_read_b64_tr_b16 a[6:7], v185 offset:6144\n\t"
    "s_waitcnt lgkmcnt(10)\n\t"
    "v_mfma_f32_16x16x32_bf16 v[104:107], a[48:51], v[236:239], v[104:107]\n\t"
    "ds_read_b64_tr_b16 a[8:9], v184 offset:8192\n\t"
    "buffer_store_dwordx4 v[144:147], v23, %[srdpt], s62 offen sc1\n\t"
    "v_mfma_f32_16x16x32_bf16 v[108:111], a[48:51], v[88:91], v[108:111]\n\t"
    "ds_read_b64_tr_b16 a[10:11], v184 offset:10240\n\t"
    "buffer_store_dwordx4 v[148:151], v23, %[srdpt], s62 offen offset:256 sc1\n\t"
    "s_waitcnt lgkmcnt(10)\n\t"
    "v_mfma_f32_16x16x32_bf16 v[112:115], a[52:55], v[236:239], v[112:115]\n\t"
    "ds_read_b64_tr_b16 a[12:13], v185 offset:8192\n\t"
    "v_mfma_f32_16x16x32_bf16 v[116:119], a[52:55], v[88:91], v[116:119]\n\t"
    "ds_read_b64_tr_b16 a[14:15], v185 offset:10240\n\t"
    "s_waitcnt lgkmcnt(10)\n\t"
    "v_mfma_f32_16x16x32_bf16 v[104:107], a[56:59], v[240:243], v[104:107]\n\t"
    "v_mfma_f32_16x16x32_bf16 v[108:111], a[56:59], v[92:95], v[108:111]\n\t"
    "s_waitcnt lgkmcnt(8)\n\t"
    "v_mfma_f32_16x16x32_bf16 v[112:115], a[60:63], v[240:243], v[112:115]\n\t"
    "v_mfma_f32_16x16x32_bf16 v[116:119], a[60:63], v[92:95], v[116:119]\n\t"
    "s_waitcnt lgkmcnt(6)\n\t"
    "v_mfma_f32_16x16x32_bf16 v[104:107], a[0:3], v[244:247], v[104:107]\n\t"
    "v_mfma_f32_16x16x32_bf16 v[108:111], a[0:3], v[96:99], v[108:111]\n\t"
    "s_waitcnt lgkmcnt(4)\n\t"
    "v_mfma_f32_16x16x32_bf16 v[112:115], a[4:7], v[244:247], v[112:115]\n\t"
    "v_mfma_f32_16x16x32_bf16 v[116:119], a[4:7], v[96:99], v[116:119]\n\t"
    "s_waitcnt lgkmcnt(2)\n\t"
    "v_mfma_f32_16x16x32_bf16 v[104:107], a[8:11], v[248:251], v[104:107]\n\t"
    "v_mfma_f32_16x16x32_bf16 v[108:111], a[8:11], v[100:103], v[108:111]\n\t"
    "s_waitcnt lgkmcnt(0)\n\t"
    "v_mfma_f32_16x16x32_bf16 v[112:115], a[12:15], v[248:251], v[112:115]\n\t"
    "v_mfma_f32_16x16x32_bf16 v[116:119], a[12:15], v[100:103], v[116:119]\n\t"
    "s_nop 7\n\t"
    "v_cvt_pk_bf16_f32 v136, v104, v105\n\t"
    "v_cvt_pk_bf16_f32 v137, v106, v107\n\t"
    "v_cvt_pk_bf16_f32 v138, v112, v113\n\t"
    "v_cvt_pk_bf16_f32 v139, v114, v115\n\t"
    "v_cvt_pk_bf16_f32 v140, v108, v109\n\t"
    "v_cvt_pk_bf16_f32 v141, v110, v111\n\t"
    "v_cvt_pk_bf16_f32 v142, v116, v117\n\t"
    "v_cvt_pk_bf16_f32 v143, v118, v119\n\t"
    "s_movk_i32 s62, 0x6000\n\t"
    "buffer_store_dwordx4 v[136:139], v23, %[srdpt], s62 offen sc1\n\t"
    "buffer_store_dwordx4 v[140:143], v23, %[srdpt], s62 offen offset:256 sc1\n\t"
    "s_nop 1\n\t"
    :
    : [srdw1] "s"(srdw1), [srdw2] "s"(mkrsrc(w2 + g * 200704u, 200704u)), [srdy] "s"(mkrsrc(ws + WS_Y + rb32 * 28672u, 28672u)),
      [srdpt] "s"(mkrsrc(ws + WS_PART2 + g * FBYTES + rb32 * 28672u + hs * 2048u, 28672u - hs * 2048u)),
      [flag] "s"(fl + 32), [wid] "s"(w), [ldsb] "s"(lb), "{v[8:23]}"(bA), "{v[24:39]}"(bB), "{v[40:55]}"(bC)
    : "v56", "v57", "v58", "v59", "v60", "v61", "v62", "v63", "v64", "v65", "v66", "v67", "v68", "v69", "v70", "v71", "v72", "v73", "v74", "v75", "v76", "v77", "v78", "v79", "v80", "v81", "v82", "v83", "v84", "v85", "v86", "v87", "v88", "v89", "v90", "v91", "v92", "v93", "v94", "v95", "v96", "v97", "v98", "v99", "v100", "v101", "v102", "v103", "v104", "v105", "v106", "v107", "v108", "v109", "v110", "v111", "v112", "v113", "v114", "v115", "v116", "v117", "v118", "v119", "v120", "v121", "v122", "v123", "v124", "v125", "v126", "v127", "v128", "v129", "v130", "v131", "v132", "v133", "v134", "v135", "v136", "v137", "v138", "v139", "v140", "v141", "v142", "v143", "v144", "v145", "v146", "v147", "v148", "v149", "v150", "v151", "v152", "v153", "v154", "v155", "v156", "v157", "v158", "v159", "v160", "v161", "v162", "v163", "v164", "v165", "v166", "v167", "v168", "v169", "v170", "v171", "v172", "v173", "v174", "v175", "v176", "v177", "v178", "v179", "v180", "v181", "v182", "v183", "v184", "v185", "v186", "v187", "v188", "v189", "v190", "v191", "v192", "v193", "v194", "v195", "v196", "v197", "v198", "v199", "v200", "v201", "v202", "v203", "v204", "v205", "v206", "v207", "v208", "v209", "v210", "v211", "v212", "v213", "v214", "v215", "v216", "v217", "v218", "v219", "v220", "v221", "v222", "v223", "v224", "v225", "v226", "v227", "v228", "v229", "v230", "v231", "v232", "v233", "v234", "v235", "v236", "v237", "v238", "v239", "v240", "v241", "v242", "v243", "v244", "v245", "v246", "v247", "v248", "v249", "v250", "v251", "v252", "v253", "v254", "v255", "a0", "a1", "a2", "a3", "a4", "a5", "a6", "a7", "a8", "a9", "a10", "a11", "a12", "a13", "a14", "a15", "a16", "a17", "a18", "a19", "a20", "a21", "a22", "a23", "a24", "a25", "a26", "a27", "a28", "a29", "a30", "a31", "a32", "a33", "a34", "a35", "a36", "a37", "a38", "a39", "a40", "a41", "a42", "a43", "a44", "a45", "a46", "a47", "a48", "a49", "a50", "a51", "a52", "a53", "a54", "a55", "a56", "a57", "a58", "a59", "a60", "a61", "a62", "a63", "a64", "a65", "a66", "a67", "a68", "a69", "a70", "a71", "a72", "a73", "a74", "a75", "a76", "a77", "a78", "a79", "a80", "a81", "a82", "a83", "a84", "a85", "a86", "a87", "a88", "a89", "a90", "a91", "a92", "a93", "a94", "a95", "a96", "a97", "a98", "a99", "a100", "a101", "a102", "a103", "a104", "a105", "a106", "a107", "a108", "a109", "a110", "a111", "a112", "a113", "a114", "a115", "a116", "a117", "a118", "a119", "a120", "a121", "a122", "a123", "a124", "a125", "a126", "a127", "a128", "a129", "a130", "a131", "a132", "a133", "a134", "a135", "a136", "a137", "a138", "a139", "a140", "a141", "a142", "a143", "a144", "a145", "a146", "a147", "a148", "a149", "a150", "a151", "a152", "a153", "a154", "a155", "a156", "a157", "a158", "a159", "a160", "a161", "a162", "a163", "a164", "a165", "a166", "a167", "a168", "a169", "a170", "a171", "a172", "a173", "a174", "a175", "s60", "s61", "s62", "s63", "s64", "s65", "s66", "s67", "s68", "s69", "s70", "s71", "s72", "s73", "s74", "s75", "s98", "s99", "vcc", "scc", "memory"
  );
  asm volatile(
    "global_load_dword v248, %[fo], %[fl] sc1\n\t"
    "s_mov_b32 s60, 0\n\t"
    "s_mov_b32 s64, 0\n\t"
    "s_mov_b32 s61, 0x100000\n\t"
    "v_mov_b32 v250, 1\n\t"
    "s_waitcnt vmcnt(0)\n\t"
    "s_barrier\n\t"
    "s_cmp_lg_u32 %[wid], 0\n\t"
    "s_cbranch_scc1 SH_%=\n\t"
    "s_mov_b64 exec, 1\n\t"
    "global_store_dword %[fo], v250, %[flo] sc1\n\t"
    "s_mov_b64 exec, -1\n\t"
    "SH_%=:\n\t"
    "v_cmp_ne_u32 vcc, 0, v248\n\t"
    "s_or_b32 s62, vcc_lo, %[own]\n\t"
    "s_and_b32 s62, s62, 0xff\n\t"
    "s_cmp_eq_u32 s62, 0xff\n\t"
    "s_cbranch_scc0 SS_%=\n\t"
    "SQ_%=:\n\t"
    "s_mov_b32 s63, 0x0\n\t"
    "buffer_load_dwordx4 v[64:67], %[o0], %[srd], s63 offen sc1\n\t"
    "buffer_load_dwordx4 v[96:99], %[o1], %[srd], s63 offen sc1\n\t"
    "s_mov_b32 s63, 0x1c0000\n\t"
    "buffer_load_dwordx4 v[68:71], %[o0], %[srd], s63 offen sc1\n\t"
    "buffer_load_dwordx4 v[100:103], %[o1], %[srd], s63 offen sc1\n\t"
    "s_mov_b32 s63, 0x380000\n\t"
    "buffer_load_dwordx4 v[72:75], %[o0], %[srd], s63 offen sc1\n\t"
    "buffer_load_dwordx4 v[104:107], %[o1], %[srd], s63 offen sc1\n\t"
    "s_mov_b32 s63, 0x540000\n\t"
    "buffer_load_dwordx4 v[76:79], %[o0], %[srd], s63 offen sc1\n\t"
    "buffer_load_dwordx4 v[108:111], %[o1], %[srd], s63 offen sc1\n\t"
    "s_mov_b32 s63, 0x700000\n\t"
    "buffer_load_dwordx4 v[80:83], %[o0], %[srd], s63 offen sc1\n\t"
    "buffer_load_dwordx4 v[112:115], %[o1], %[srd], s63 offen sc1\n\t"
    "s_mov_b32 s63, 0x8c0000\n\t"
    "buffer_load_dwordx4 v[84:87], %[o0], %[srd], s63 offen sc1\n\t"
    "buffer_load_dwordx4 v[116:119], %[o1], %[srd], s63 offen sc1\n\t"
    "s_mov_b32 s63, 0xa80000\n\t"
    "buffer_load_dwordx4 v[88:91], %[o0], %[srd], s63 offen sc1\n\t"
    "buffer_load_dwordx4 v[120:123], %[o1], %[srd], s63 offen sc1\n\t"
    "s_mov_b32 s63, 0xc40000\n\t"
    "buffer_load_dwordx4 v[92:95], %[o0], %[srd], s63 offen sc1\n\t"
    "buffer_load_dwordx4 v[124:127], %[o1], %[srd], s63 offen sc1\n\t"
    "s_waitcnt vmcnt(12)\n\t"
    "v_lshlrev_b32 v128, 16, v64\n\t"
    "v_and_b32 v129, 0xffff0000, v64\n\t"
    "v_lshlrev_b32 v250, 16, v68\n\t"
    "v_and_b32 v251, 0xffff0000, v68\n\t"
    "v_pk_add_f32 v[128:129], v[128:129], v[250:251]\n\t"
    "v_lshlrev_b32 v130, 16, v65\n\t"
    "v_and_b32 v131, 0xffff0000, v65\n\t"
    "v_lshlrev_b32 v250, 16, v69\n\t"
    "v_and_b32 v251, 0xffff0000, v69\n\t"
    "v_pk_add_f32 v[130:131], v[130:131], v[250:251]\n\t"
    "v_lshlrev_b32 v132, 16, v66\n\t"
    "v_and_b32 v133, 0xffff0000, v66\n\t"
    "v_lshlrev_b32 v250, 16, v70\n\t"
    "v_and_b32 v251, 0xffff0000, v70\n\t"
    "v_pk_add_f32 v[132:133], v[132:133], v[250:251]\n\t"
    "v_lshlrev_b32 v134, 16, v67\n\t"
    "v_and_b32 v135, 0xffff0000, v67\n\t"
    "v_lshlrev_b32 v250, 16, v71\n\t"
    "v_and_b32 v251, 0xffff0000, v71\n\t"
    "v_pk_add_f32 v[134:135], v[134:135], v[250:251]\n\t"
    "v_lshlrev_b32 v136, 16, v96\n\t"
    "v_and_b32 v137, 0xffff0000, v96\n\t"
    "v_lshlrev_b32 v250, 16, v100\n\t"
    "v_and_b32 v251, 0xffff0000, v100\n\t"
    "v_pk_add_f32 v[136:137], v[136:137], v[250:251]\n\t"
    "v_lshlrev_b32 v138, 16, v97\n\t"
    "v_and_b32 v139, 0xffff0000, v97\n\t"
    "v_lshlrev_b32 v250, 16, v101\n\t"
    "v_and_b32 v251, 0xffff0000, v101\n\t"
    "v_pk_add_f32 v[138:139], v[138:139], v[250:251]\n\t"
    "v_lshlrev_b32 v140, 16, v98\n\t"
    "v_and_b32 v141, 0xffff0000, v98\n\t"
    "v_lshlrev_b32 v250, 16, v102\n\t"
    "v_and_b32 v251, 0xffff0000, v102\n\t"
    "v_pk_add_f32 v[140:141], v[140:141], v[250:251]\n\t"
    "v_lshlrev_b32 v142, 16, v99\n\t"
    "v_and_b32 v143, 0xffff0000, v99\n\t"
    "v_lshlrev_b32 v250, 16, v103\n\t"
    "v_and_b32 v251, 0xffff0000, v103\n\t"
    "v_pk_add_f32 v[142:143], v[142:143], v[250:251]\n\t"
    "s_waitcnt vmcnt(8)\n\t"
    "v_lshlrev_b32 v144, 16, v72\n\t"
    "v_and_b32 v145, 0xffff0000, v72\n\t"
    "v_lshlrev_b32 v250, 16, v76\n\t"
    "v_and_b32 v251, 0xffff0000, v76\n\t"
    "v_pk_add_f32 v[144:145], v[144:145], v[250:251]\n\t"
    "v_lshlrev_b32 v146, 16, v73\n\t"
    "v_and_b32 v147, 0xffff0000, v73\n\t"
    "v_lshlrev_b32 v250, 16, v77\n\t"
    "v_and_b32 v251, 0xffff0000, v77\n\t"
    "v_pk_add_f32 v[146:147], v[146:147], v[250:251]\n\t"
    "v_lshlrev_b32 v148, 16, v74\n\t"
    "v_and_b32 v149, 0xffff0000, v74\n\t"
    "v_lshlrev_b32 v250, 16, v78\n\t"
    "v_and_b32 v251, 0xffff0000, v78\n\t"
    "v_pk_add_f32 v[148:149], v[148:149], v[250:251]\n\t"
    "v_lshlrev_b32 v150, 16, v75\n\t"
    "v_and_b32 v151, 0xffff0000, v75\n\t"
    "v_lshlrev_b32 v250, 16, v79\n\t"
    "v_and_b32 v251, 0xffff0000, v79\n\t"
    "v_pk_add_f32 v[150:151], v[150:151], v[250:251]\n\t"
    "v_lshlrev_b32 v152, 16, v104\n\t"
    "v_and_b32 v153, 0xffff0000, v104\n\t"
    "v_lshlrev_b32 v250, 16, v108\n\t"
    "v_and_b32 v251, 0xffff0000, v108\n\t"
    "v_pk_add_f32 v[152:153], v[152:153], v[250:251]\n\t"
    "v_lshlrev_b32 v154, 16, v105\n\t"
    "v_and_b32 v155, 0xffff0000, v105\n\t"
    "v_lshlrev_b32 v250, 16, v109\n\t"
    "v_and_b32 v251, 0xffff0000, v109\n\t"
    "v_pk_add_f32 v[154:155], v[154:155], v[250:251]\n\t"
    "v_lshlrev_b32 v156, 16, v106\n\t"
    "v_and_b32 v157, 0xffff0000, v106\n\t"
    "v_lshlrev_b32 v250, 16, v110\n\t"
    "v_and_b32 v251, 0xffff0000, v110\n\t"
    "v_pk_add_f32 v[156:157], v[156:157], v[250:251]\n\t"
    "v_lshlrev_b32 v158, 16, v107\n\t"
    "v_and_b32 v159, 0xffff0000, v107\n\t"
    "v_lshlrev_b32 v250, 16, v111\n\t"
    "v_and_b32 v251, 0xffff0000, v111\n\t"
    "v_pk_add_f32 v[158:159], v[158:159], v[250:251]\n\t"
    "s_waitcnt vmcnt(4)\n\t"
    "v_lshlrev_b32 v160, 16, v80\n\t"
    "v_and_b32 v161, 0xffff0000, v80\n\t"
    "v_lshlrev_b32 v250, 16, v84\n\t"
    "v_and_b32 v251, 0xffff0000, v84\n\t"
    "v_pk_add_f32 v[160:161], v[160:161], v[250:251]\n\t"
    "v_lshlrev_b32 v162, 16, v81\n\t"
    "v_and_b32 v163, 0xffff0000, v81\n\t"
    "v_lshlrev_b32 v250, 16, v85\n\t"
    "v_and_b32 v251, 0xffff0000, v85\n\t"
    "v_pk_add_f32 v[162:163], v[162:163], v[250:251]\n\t"
    "v_lshlrev_b32 v164, 16, v82\n\t"
    "v_and_b32 v165, 0xffff0000, v82\n\t"
    "v_lshlrev_b32 v250, 16, v86\n\t"
    "v_and_b32 v251, 0xffff0000, v86\n\t"
    "v_pk_add_f32 v[164:165], v[164:165], v[250:251]\n\t"
    "v_lshlrev_b32 v166, 16, v83\n\t"
    "v_and_b32 v167, 0xffff0000, v83\n\t"
    "v_lshlrev_b32 v250, 16, v87\n\t"
    "v_and_b32 v251, 0xffff0000, v87\n\t"
    "v_pk_add_f32 v[166:167], v[166:167], v[250:251]\n\t"
    "v_lshlrev_b32 v168, 16, v112\n\t"
    "v_and_b32 v169, 0xffff0000, v112\n\t"
    "v_lshlrev_b32 v250, 16, v116\n\t"
    "v_and_b32 v251, 0xffff0000, v116\n\t"
    "v_pk_add_f32 v[168:169], v[168:169], v[250:251]\n\t"
    "v_lshlrev_b32 v170, 16, v113\n\t"
    "v_and_b32 v171, 0xffff0000, v113\n\t"
    "v_lshlrev_b32 v250, 16, v117\n\t"
    "v_and_b32 v251, 0xffff0000, v117\n\t"
    "v_pk_add_f32 v[170:171], v[170:171], v[250:251]\n\t"
    "v_lshlrev_b32 v172, 16, v114\n\t"
    "v_and_b32 v173, 0xffff0000, v114\n\t"
    "v_lshlrev_b32 v250, 16, v118\n\t"
    "v_and_b32 v251, 0xffff0000, v118\n\t"
    "v_pk_add_f32 v[172:173], v[172:173], v[250:251]\n\t"
    "v_lshlrev_b32 v174, 16, v115\n\t"
    "v_and_b32 v175, 0xffff0000, v115\n\t"
    "v_lshlrev_b32 v250, 16, v119\n\t"
    "v_and_b32 v251, 0xffff0000, v119\n\t"
    "v_pk_add_f32 v[174:175], v[174:175], v[250:251]\n\t"
    "s_waitcnt vmcnt(0)\n\t"
    "v_lshlrev_b32 v176, 16, v88\n\t"
    "v_and_b32 v177, 0xffff0000, v88\n\t"
    "v_lshlrev_b32 v250, 16, v92\n\t"
    "v_and_b32 v251, 0xffff0000, v92\n\t"
    "v_pk_add_f32 v[176:177], v[176:177], v[250:251]\n\t"
    "v_lshlrev_b32 v178, 16, v89\n\t"
    "v_and_b32 v179, 0xffff0000, v89\n\t"
    "v_lshlrev_b32 v250, 16, v93\n\t"
    "v_and_b32 v251, 0xffff0000, v93\n\t"
    "v_pk_add_f32 v[178:179], v[178:179], v[250:251]\n\t"
    "v_lshlrev_b32 v180, 16, v90\n\t"
    "v_and_b32 v181, 0xffff0000, v90\n\t"
    "v_lshlrev_b32 v250, 16, v94\n\t"
    "v_and_b32 v251, 0xffff0000, v94\n\t"
    "v_pk_add_f32 v[180:181], v[180:181], v[250:251]\n\t"
    "v_lshlrev_b32 v182, 16, v91\n\t"
    "v_and_b32 v183, 0xffff0000, v91\n\t"
    "v_lshlrev_b32 v250, 16, v95\n\t"
    "v_and_b32 v251, 0xffff0000, v95\n\t"
    "v_pk_add_f32 v[182:183], v[182:183], v[250:251]\n\t"
    "v_lshlrev_b32 v184, 16, v120\n\t"
    "v_and_b32 v185, 0xffff0000, v120\n\t"
    "v_lshlrev_b32 v250, 16, v124\n\t"
    "v_and_b32 v251, 0xffff0000, v124\n\t"
    "v_pk_add_f32 v[184:185], v[184:185], v[250:251]\n\t"
    "v_lshlrev_b32 v186, 16, v121\n\t"
    "v_and_b32 v187, 0xffff0000, v121\n\t"
    "v_lshlrev_b32 v250, 16, v125\n\t"
    "v_and_b32 v251, 0xffff0000, v125\n\t"
    "v_pk_add_f32 v[186:187], v[186:187], v[250:251]\n\t"
    "v_lshlrev_b32 v188, 16, v122\n\t"
    "v_and_b32 v189, 0xffff0000, v122\n\t"
    "v_lshlrev_b32 v250, 16, v126\n\t"
    "v_and_b32 v251, 0xffff0000, v126\n\t"
    "v_pk_add_f32 v[188:189], v[188:189], v[250:251]\n\t"
    "v_lshlrev_b32 v190, 16, v123\n\t"
    "v_and_b32 v191, 0xffff0000, v123\n\t"
    "v_lshlrev_b32 v250, 16, v127\n\t"
    "v_and_b32 v251, 0xffff0000, v127\n\t"
    "v_pk_add_f32 v[190:191], v[190:191], v[250:251]\n\t"
    "s_branch SF_%=\n\t"
    "SS_%=:\n\t"
    "global_load_dword v248, %[fo], %[fl] sc1\n\t"
    "s_sleep 4\n\t"
    "SA_%=:\n\t"
    "global_load_dword v249, %[fo], %[fl] sc1\n\t"
    "s_waitcnt vmcnt(1)\n\t"
    "v_cmp_ne_u32 vcc, 0, v248\n\t"
    "s_and_b32 s62, vcc_lo, 0xff\n\t"
    "s_cmp_eq_u32 s62, 0xff\n\t"
    "s_cbranch_scc1 SQ_%=\n\t"
    "SM_%=:\n\t"
    "global_load_dword v248, %[fo], %[fl] sc1\n\t"
    "s_waitcnt vmcnt(1)\n\t"
    "v_cmp_ne_u32 vcc, 0, v249\n\t"
    "s_and_b32 s62, vcc_lo, 0xff\n\t"
    "s_cmp_eq_u32 s62, 0xff\n\t"
    "s_cbranch_scc1 SQ_%=\n\t"
    "s_sub_u32 s61, s61, 1\n\t"
    "s_cbranch_scc0 SA_%=\n\t"
    "SW_%=:\n\t"
    "s_waitcnt vmcnt(0)\n\t"
    "SF_%=:\n\t"
    "v_pk_add_f32 v[128:129], v[128:129], v[144:145]\n\t"
    "v_pk_add_f32 v[130:131], v[130:131], v[146:147]\n\t"
    "v_pk_add_f32 v[132:133], v[132:133], v[148:149]\n\t"
    "v_pk_add_f32 v[134:135], v[134:135], v[150:151]\n\t"
    "v_pk_add_f32 v[136:137], v[136:137], v[152:153]\n\t"
    "v_pk_add_f32 v[138:139], v[138:139], v[154:155]\n\t"
    "v_pk_add_f32 v[140:141], v[140:141], v[156:157]\n\t"
    "v_pk_add_f32 v[142:143], v[142:143], v[158:159]\n\t"
    "v_pk_add_f32 v[160:161], v[160:161], v[176:177]\n\t"
    "v_pk_add_f32 v[162:163], v[162:163], v[178:179]\n\t"
    "v_pk_add_f32 v[164:165], v[164:165], v[180:181]\n\t"
    "v_pk_add_f32 v[166:167], v[166:167], v[182:183]\n\t"
    "v_pk_add_f32 v[168:169], v[168:169], v[184:185]\n\t"
    "v_pk_add_f32 v[170:171], v[170:171], v[186:187]\n\t"
    "v_pk_add_f32 v[172:173], v[172:173], v[188:189]\n\t"
    "v_pk_add_f32 v[174:175], v[174:175], v[190:191]\n\t"
    "v_pk_add_f32 v[128:129], v[128:129], v[160:161]\n\t"
    "v_pk_add_f32 v[130:131], v[130:131], v[162:163]\n\t"
    "v_pk_add_f32 v[132:133], v[132:133], v[164:165]\n\t"
    "v_pk_add_f32 v[134:135], v[134:135], v[166:167]\n\t"
    "v_pk_add_f32 v[136:137], v[136:137], v[168:169]\n\t"
    "v_pk_add_f32 v[138:139], v[138:139], v[170:171]\n\t"
    "v_pk_add_f32 v[140:141], v[140:141], v[172:173]\n\t"
    "v_pk_add_f32 v[142:143], v[142:143], v[174:175]\n\t"
    : "=&{v248}"(pf), "=&{v[128:143]}"(fs), "+v"(ob), "+v"(gv)
    : [fo] "v"(q8 * 4u), [fl] "s"(fl + 64), [flo] "s"(fl + 64 + g), [wid] "s"(w), [own] "s"(own), [o0] "v"(fo0), [o1] "v"(fo1), [srd] "s"(mkrsrc(ws + WS_PART2, 8u * (unsigned)FBYTES))
    : "v64", "v65", "v66", "v67", "v68", "v69", "v70", "v71", "v72", "v73", "v74", "v75", "v76", "v77", "v78", "v79", "v80", "v81", "v82", "v83", "v84", "v85", "v86", "v87", "v88", "v89", "v90", "v91", "v92", "v93", "v94", "v95", "v96", "v97", "v98", "v99", "v100", "v101", "v102", "v103", "v104", "v105", "v106", "v107", "v108", "v109", "v110", "v111", "v112", "v113", "v114", "v115", "v116", "v117", "v118", "v119", "v120", "v121", "v122", "v123", "v124", "v125", "v126", "v127", "v144", "v145", "v146", "v147", "v148", "v149", "v150", "v151", "v152", "v153", "v154", "v155", "v156", "v157", "v158", "v159", "v160", "v161", "v162", "v163", "v164", "v165", "v166", "v167", "v168", "v169", "v170", "v171", "v172", "v173", "v174", "v175", "v176", "v177", "v178", "v179", "v180", "v181", "v182", "v183", "v184", "v185", "v186", "v187", "v188", "v189", "v190", "v191", "v249", "v250", "v251", "s60", "s61", "s62", "s63", "s64", "s65", "vcc", "scc", "memory"
  );
#pragma unroll
  for (int k = 0; k < 4; ++k) {
    const f32x2 o0 = f32x2{ob[4 * k], ob[4 * k + 1]} + f32x2{gv[4 * k], gv[4 * k + 1]} * f32x2{fs[4 * k], fs[4 * k + 1]};
    const f32x2 o1 = f32x2{ob[4 * k + 2], ob[4 * k + 3]} + f32x2{gv[4 * k + 2], gv[4 * k + 3]} * f32x2{fs[4 * k + 2], fs[4 * k + 3]};
    __builtin_amdgcn_raw_buffer_store_b64(u32x2{cvtpk(o0[0], o0[1]), cvtpk(o1[0], o1[1])}, mkrsrc(out, (unsigned)FBYTES),
                                          (k & 2 ? xo1 : xo0) + (k & 1) * 16u, 0, AUXO);
  }
  asm volatile("KTAIL_END:");
}

extern "C" void kernel_launch(void* const* d_in, const int* in_sizes, int n_in, void* d_out, int out_size, void* d_ws, size_t ws_size,
                              hipStream_t stream) {
  char* ws = (char*)d_ws;
  k0_prep<<<236, 256, 0, stream>>>((const u16*)d_in[0], (const u16*)d_in[10], (const u16*)d_in[11], (const char*)d_in[3], (const char*)d_in[1], (const char*)d_in[2], ws);
  k1_main<<<256, 256, 0, stream>>>((const char*)d_in[1], (const char*)d_in[2], (const char*)d_in[3], (const char*)d_in[4], (const char*)d_in[6], (const char*)d_in[8],
                                   (const u16*)d_in[0], (const char*)d_in[7], (const u16*)d_in[9], ws, (u16*)d_out);
}
```
